```python
import torch

def _softmax(x, axis=-1):
    return torch.softmax(x, dim=axis)
def _gelu(x):
    return torch.nn.functional.gelu(x, approximate="tanh")
def _silu(x):
    return torch.nn.functional.silu(x)
def _sigmoid(x):
    return torch.sigmoid(x)
def _layernorm(x, axis=-1, eps=1e-5):
    m = x.mean(dim=axis, keepdim=True)
    v = x.var(dim=axis, keepdim=True, unbiased=False)
    return (x - m) / torch.sqrt(v + eps)
def _scatter_add(tgt, idx, src, axis=0):
    return tgt.clone().index_add_(axis, idx, src)
def _segment_sum(src, seg, n):
    out = torch.zeros((n,) + src.shape[1:], dtype=src.dtype)
    return out.index_add_(0, seg, src)
def _pad(x, pw):
    flat = tuple(v for pair in reversed(pw) for v in pair)
    return torch.nn.functional.pad(x, flat)

kernel_name = 'sequential_preLN__mha_block_cached__mixer_token_mlp__decode'

def setup_inputs():

    _w = (lambda a: (lambda t: t.to(torch.bfloat16) if t.is_floating_point() else t)(torch.as_tensor(a)))
    def _A_setup():
        B, Sq, D = 4, 1, 640
        H, Sk, Dh = 32, 4096, 192
        return {
            "x": _w((lambda *s: torch.randn(*s, dtype=torch.bfloat16) * 0.05)(B, Sq, D)),
            "k_cache": _w((lambda *s: torch.randn(*s, dtype=torch.bfloat16) * 1.6)(B, H, Sk, Dh)),
            "v_cache": _w((lambda *s: torch.randn(*s, dtype=torch.bfloat16) * 0.8)(B, H, Sk, Dh)),
            "wq": _w((lambda *s: torch.randn(*s, dtype=torch.bfloat16) * 1.6)(D, H * Dh) * (D ** -0.5)),
            "wo": _w((lambda *s: torch.randn(*s, dtype=torch.bfloat16) * 0.8)(H * Dh, D) * ((H * Dh) ** -0.5)),
        }
    _A_in = _A_setup()

    def _B_setup():
        B, S, D, F = 4, 1, 640, 1536
        return {
            "x": _w((lambda *s: torch.randn(*s, dtype=torch.bfloat16) * 0.05)(B, S, D)),
            "w1": _w((lambda *s: torch.randn(*s, dtype=torch.bfloat16) * 3.2)(S, F) * (S ** -0.5)),
            "b1": _w((lambda *s: torch.randn(*s, dtype=torch.bfloat16) * 3.2)(F) * 0.02),
            "w2": _w((lambda *s: torch.randn(*s, dtype=torch.bfloat16) * 0.4)(F, S) * (F ** -0.5)),
            "b2": _w((lambda *s: torch.randn(*s, dtype=torch.bfloat16) * 0.4)(S) * 0.02),
        }
    _B_in = _B_setup()

    return {
        "A_x": _A_in["x"],
        "A_k_cache": _A_in["k_cache"],
        "A_v_cache": _A_in["v_cache"],
        "A_wq": _A_in["wq"],
        "A_wo": _A_in["wo"],
        "B_x": _B_in["x"],
        "B_w1": _B_in["w1"],
        "B_b1": _B_in["b1"],
        "B_w2": _B_in["w2"],
        "B_b2": _B_in["b2"],
    }

def reference(A_x, A_k_cache, A_v_cache, A_wq, A_wo, B_x, B_w1, B_b1, B_w2, B_b2):
    def _A_ref(x, k_cache, v_cache, wq, wo):
        B, Sq, D = x.shape
        H, Dh = 32, 192
        q = (lambda x, *d: x.permute(*d))((x @ wq).reshape(B, Sq, H, Dh), 0, 2, 1, 3)
        scores = torch.einsum("bhqd,bhkd->bhqk", q, k_cache) * (Dh ** -0.5)
        p = _softmax(scores, axis=-1)
        o = torch.einsum("bhqk,bhkd->bhqd", p, v_cache)
        return (lambda x, *d: x.permute(*d))(o, 0, 2, 1, 3).reshape(B, Sq, H * Dh) @ wo

    def _B_ref(x, w1, b1, w2, b2):
        B, S, D = x.shape
        h = _layernorm(x, axis=-1)
        u = torch.einsum("bsd,sf->bdf", h, w1) + b1
        u = _gelu(u)
        y = torch.einsum("bdf,fs->bds", u, w2) + b2
        return (lambda x, *d: x.permute(*d))(y, 0, 2, 1)

    x = A_x
    h = x + _A_ref(_layernorm(x, axis=-1), A_k_cache, A_v_cache, A_wq, A_wo)
    h2 = h + _B_ref(_layernorm(h, axis=-1), B_w1, B_b1, B_w2, B_b2)
    return h2
```

```cpp
#include <hip/hip_runtime.h>
#include <hip/hip_bf16.h>
#include <stdint.h>

typedef unsigned u32x4 __attribute__((ext_vector_type(4)));
typedef float f32x2 __attribute__((ext_vector_type(2)));
typedef __bf16 bf16x2 __attribute__((ext_vector_type(2)));

constexpr int B = 4, D = 640, H = 32, SK = 4096, DH = 192, HD = H * DH, F = 1536;
constexpr int NSPLIT = 2, KEYS_WG = SK / NSPLIT;
constexpr int WAVES = 8, NTHR = WAVES * 64;
constexpr int PREC = 196;

__device__ __forceinline__ float bf2f(unsigned short v) { return __uint_as_float((unsigned)v << 16); }
__device__ __forceinline__ float bflo(unsigned w) { return __uint_as_float(w << 16); }
__device__ __forceinline__ float bfhi(unsigned w) { return __uint_as_float(w & 0xffff0000u); }
__device__ __forceinline__ unsigned short f2bf(float f) { __bf16 b = (__bf16)f; return __builtin_bit_cast(unsigned short, b); }
__device__ __forceinline__ float rbf(float f) { return bf2f(f2bf(f)); }
__device__ __forceinline__ float dot2(unsigned a, unsigned b, float c) {
  return __builtin_amdgcn_fdot2_f32_bf16(__builtin_bit_cast(bf16x2, a), __builtin_bit_cast(bf16x2, b), c, false);
}
template <int CTRL> __device__ __forceinline__ float dpp(float x) {
  return __builtin_bit_cast(float, __builtin_amdgcn_mov_dpp(__builtin_bit_cast(int, x), CTRL, 0xf, 0xf, true));
}
constexpr int XOR1 = 0xB1, XOR2 = 0x4E, HMIR = 0x141;
__device__ __forceinline__ float wave_sum(float v) {
  for (int o = 32; o > 0; o >>= 1) v += __shfl_xor(v, o);
  return v;
}

__global__ void __launch_bounds__(256) k_q(const unsigned short* x, const unsigned short* wq, unsigned short* q) {
  __shared__ float xn[B][D];
  __shared__ float red[32][B][64 + 1];
  const int tid = threadIdx.x, lane = tid & 63, w = tid >> 6;
  {
    float v[10]; float s = 0.f;
    for (int i = 0; i < 10; ++i) { v[i] = bf2f(x[w * D + lane + 64 * i]); s += v[i]; }
    const float mean = wave_sum(s) * (1.f / D);
    float s2 = 0.f;
    for (int i = 0; i < 10; ++i) { const float d = v[i] - mean; s2 += d * d; }
    const float rstd = rsqrtf(wave_sum(s2) * (1.f / D) + 1e-5f);
    for (int i = 0; i < 10; ++i) xn[w][lane + 64 * i] = rbf((v[i] - mean) * rstd);
  }
  __syncthreads();
  const int c8 = tid & 7, rg = tid >> 3;
  float acc[B][8] = {};
  for (int r = rg; r < D; r += 32) {
    const u32x4 wv = *reinterpret_cast<const u32x4*>(wq + (size_t)r * HD + blockIdx.x * 64 + c8 * 8);
    for (int b = 0; b < B; ++b) { const float xv = xn[b][r];
      for (int k = 0; k < 4; ++k) { acc[b][2 * k] += xv * bflo(wv[k]); acc[b][2 * k + 1] += xv * bfhi(wv[k]); } }
  }
  for (int b = 0; b < B; ++b) for (int k = 0; k < 8; ++k) red[rg][b][c8 * 8 + k] = acc[b][k];
  __syncthreads();
  { const int b = tid >> 6, n = tid & 63; float s = 0.f;
    for (int g = 0; g < 32; ++g) s += red[g][b][n];
    q[b * HD + blockIdx.x * 64 + n] = f2bf(s); }
}

__global__ void __launch_bounds__(NTHR, 2) k_attn(const unsigned short* __restrict__ q, const unsigned short* __restrict__ kc,
                                                  const unsigned short* __restrict__ vc, float* __restrict__ part) {
  __shared__ float mrg[WAVES * 8][PREC];
  const int tid = threadIdx.x, lane = tid & 63, w = __builtin_amdgcn_readfirstlane(tid >> 6);
  const int r = lane >> 3, c = lane & 7;
  const int bh = blockIdx.x / NSPLIT, s = blockIdx.x % NSPLIT;
  constexpr float C2 = 0.07216878364870322f * 1.4426950408889634f;
  u32x4 qf[3];
  for (int i = 0; i < 3; ++i) qf[i] = *reinterpret_cast<const u32x4*>(q + bh * DH + (c + 8 * i) * 8);
  const size_t base = ((size_t)bh * SK + (size_t)s * KEYS_WG + w * 16 + r) * DH + c * 8;
  const u32x4* kp = reinterpret_cast<const u32x4*>(kc + base);
  const u32x4* vp = reinterpret_cast<const u32x4*>(vc + base);
  constexpr int STEP = WAVES * 16 * DH / 8;
  constexpr int NT = KEYS_WG / (WAVES * 16);
  constexpr int DEPTH = 2;
  u32x4 K[DEPTH][6], V[DEPTH][6];
#define LOADK(d, t) _Pragma("unroll") for (int g = 0; g < 2; ++g) _Pragma("unroll") for (int i = 0; i < 3; ++i) \
    K[d][g * 3 + i] = __builtin_nontemporal_load(kp + (size_t)(t) * STEP + g * 8 * (DH / 8) + 8 * i);
#define LOADV(d, t) _Pragma("unroll") for (int g = 0; g < 2; ++g) _Pragma("unroll") for (int i = 0; i < 3; ++i) \
    V[d][g * 3 + i] = __builtin_nontemporal_load(vp + (size_t)(t) * STEP + g * 8 * (DH / 8) + 8 * i);
#pragma unroll
  for (int d = 0; d < DEPTH; ++d) { LOADK(d, d) LOADV(d, d) }
  float m = -INFINITY, l = 0.f;
  float acc[24];
#pragma unroll
  for (int k = 0; k < 24; ++k) acc[k] = 0.f;
  for (int t0 = 0; t0 < NT; t0 += DEPTH) {
#pragma unroll
    for (int d = 0; d < DEPTH; ++d) {
      const int t = t0 + d;
      float sc[2];
#pragma unroll
      for (int g = 0; g < 2; ++g) { float a = 0.f;
#pragma unroll
        for (int i = 0; i < 3; ++i)
#pragma unroll
          for (int k = 0; k < 4; ++k) a = dot2(K[d][g * 3 + i][k], qf[i][k], a);
        a += dpp<XOR1>(a); a += dpp<XOR2>(a); a += dpp<HMIR>(a);
        sc[g] = a * C2; }
      if (t + DEPTH < NT) { LOADK(d, t + DEPTH) }
      const float mn = fmaxf(m, fmaxf(sc[0], sc[1]));
      const float alpha = __builtin_amdgcn_exp2f(m - mn);
      const float p0 = __builtin_amdgcn_exp2f(sc[0] - mn), p1 = __builtin_amdgcn_exp2f(sc[1] - mn);
      l = l * alpha + p0 + p1; m = mn;
#pragma unroll
      for (int i = 0; i < 3; ++i)
#pragma unroll
        for (int k = 0; k < 4; ++k) {
          const unsigned v0 = V[d][i][k], v1 = V[d][3 + i][k];
          acc[i * 8 + 2 * k]     = fmaf(p1, bflo(v1), fmaf(p0, bflo(v0), acc[i * 8 + 2 * k] * alpha));
          acc[i * 8 + 2 * k + 1] = fmaf(p1, bfhi(v1), fmaf(p0, bfhi(v0), acc[i * 8 + 2 * k + 1] * alpha));
        }
      if (t + DEPTH < NT) { LOADV(d, t + DEPTH) }
    }
  }
  float* rec = mrg[w * 8 + r];
#pragma unroll
  for (int i = 0; i < 3; ++i)
#pragma unroll
    for (int k = 0; k < 8; ++k) rec[(c + 8 * i) * 8 + k] = acc[i * 8 + k];
  if (c == 0) { rec[192] = m; rec[193] = l; }
  __syncthreads();
  if (tid < DH) {
    float M = -INFINITY;
    for (int j = 0; j < WAVES * 8; ++j) M = fmaxf(M, mrg[j][192]);
    float L = 0.f, o = 0.f;
    for (int j = 0; j < WAVES * 8; ++j) { const float wt = __builtin_amdgcn_exp2f(mrg[j][192] - M); L += wt * mrg[j][193]; o += wt * mrg[j][tid]; }
    float* dst = part + (size_t)blockIdx.x * PREC;
    dst[tid] = o;
    if (tid == 0) { dst[192] = M; dst[193] = L; }
  }
}

__global__ void __launch_bounds__(256) k_oproj(const float* part, const unsigned short* wo, float* yp) {
  __shared__ float o[B][DH];
  __shared__ float red[24][B][80];
  const int tid = threadIdx.x, h = blockIdx.x >> 3, j = blockIdx.x & 7;
  for (int e = tid; e < B * DH; e += 256) {
    const int b = e / DH, d = e % DH;
    const float* p0 = part + (size_t)((b * H + h) * NSPLIT) * PREC;
    float M = -INFINITY;
    for (int s = 0; s < NSPLIT; ++s) M = fmaxf(M, p0[s * PREC + 192]);
    float L = 0.f, a = 0.f;
    for (int s = 0; s < NSPLIT; ++s) { const float wt = __builtin_amdgcn_exp2f(p0[s * PREC + 192] - M); L += wt * p0[s * PREC + 193]; a += wt * p0[s * PREC + d]; }
    o[b][d] = rbf(a / L);
  }
  __syncthreads();
  if (tid < 240) {
    const int c8 = tid % 10, rg = tid / 10;
    float acc[B][8] = {};
    for (int r = rg; r < DH; r += 24) {
      const u32x4 wv = *reinterpret_cast<const u32x4*>(wo + (size_t)(h * DH + r) * D + j * 80 + c8 * 8);
      for (int b = 0; b < B; ++b) { const float xv = o[b][r];
        for (int k = 0; k < 4; ++k) { acc[b][2 * k] += xv * bflo(wv[k]); acc[b][2 * k + 1] += xv * bfhi(wv[k]); } }
    }
    for (int b = 0; b < B; ++b) for (int k = 0; k < 8; ++k) red[rg][b][c8 * 8 + k] = acc[b][k];
  }
  __syncthreads();
  for (int e = tid; e < B * 80; e += 256) { const int b = e / 80, n = e % 80; float s = 0.f;
    for (int g = 0; g < 24; ++g) s += red[g][b][n];
    yp[((size_t)h * B + b) * D + j * 80 + n] = s; }
}

__global__ void __launch_bounds__(256) k_final(const unsigned short* x, const float* yp, const unsigned short* w1, const unsigned short* b1,
                                               const unsigned short* w2, const unsigned short* b2, unsigned short* out) {
  __shared__ float hs[D];
  __shared__ float tn[D];
  __shared__ float red[4][10];
  const int tid = threadIdx.x, lane = tid & 63, w = tid >> 6, b = blockIdx.x >> 6, cg = blockIdx.x & 63;
  for (int n = tid; n < D; n += 256) { float s = 0.f;
    for (int h = 0; h < H; ++h) s += yp[((size_t)h * B + b) * D + n];
    hs[n] = rbf(bf2f(x[b * D + n]) + rbf(s)); }
  __syncthreads();
  if (w == 0) {
    float v[10];
    for (int i = 0; i < 10; ++i) v[i] = hs[lane + 64 * i];
    for (int pass = 0; pass < 2; ++pass) {
      float s = 0.f;
      for (int i = 0; i < 10; ++i) s += v[i];
      const float mean = wave_sum(s) * (1.f / D);
      float s2 = 0.f;
      for (int i = 0; i < 10; ++i) { const float d = v[i] - mean; s2 += d * d; }
      const float rstd = rsqrtf(wave_sum(s2) * (1.f / D) + 1e-5f);
      for (int i = 0; i < 10; ++i) v[i] = rbf((v[i] - mean) * rstd);
    }
    for (int i = 0; i < 10; ++i) tn[lane + 64 * i] = v[i];
  }
  __syncthreads();
  float acc[10] = {};
  for (int f = tid; f < F; f += 256) {
    const float a = bf2f(w1[f]), bb = bf2f(b1[f]), cc = bf2f(w2[f]);
    for (int i = 0; i < 10; ++i) {
      const float u = fmaf(tn[cg * 10 + i], a, bb);
      const float z = u * fmaf(u * u, 0.044715f * 0.7978845608028654f, 0.7978845608028654f);
      const float e = __builtin_amdgcn_exp2f(-2.f * 1.4426950408889634f * z);
      acc[i] = fmaf(u * __builtin_amdgcn_rcpf(1.f + e), cc, acc[i]);
    }
  }
  for (int i = 0; i < 10; ++i) { const float s = wave_sum(acc[i]); if (lane == 0) red[w][i] = s; }
  __syncthreads();
  if (tid < 10) { const float y = rbf(rbf(red[0][tid] + red[1][tid] + red[2][tid] + red[3][tid]) + bf2f(b2[0]));
    out[b * D + cg * 10 + tid] = f2bf(hs[cg * 10 + tid] + y); }
}

extern "C" void kernel_launch(void* const* d_in, const int* in_sizes, int n_in, void* d_out, int out_size, void* d_ws, size_t ws_size,
                              hipStream_t stream) {
  const unsigned short* x = (const unsigned short*)d_in[0];
  const unsigned short* kc = (const unsigned short*)d_in[1];
  const unsigned short* vc = (const unsigned short*)d_in[2];
  const unsigned short* wq = (const unsigned short*)d_in[3];
  const unsigned short* wo = (const unsigned short*)d_in[4];
  const unsigned short* w1 = (const unsigned short*)d_in[6];
  const unsigned short* b1 = (const unsigned short*)d_in[7];
  const unsigned short* w2 = (const unsigned short*)d_in[8];
  const unsigned short* b2 = (const unsigned short*)d_in[9];
  unsigned short* q = (unsigned short*)d_ws;
  float* part = (float*)((char*)d_ws + 65536);
  float* yp = (float*)((char*)d_ws + 65536 + 262144);
  k_q<<<HD / 64, 256, 0, stream>>>(x, wq, q);
  k_attn<<<B * H * NSPLIT, NTHR, 0, stream>>>(q, kc, vc, part);
  k_oproj<<<H * 8, 256, 0, stream>>>(part, wo, yp);
  k_final<<<B * 64, 256, 0, stream>>>(x, yp, w1, b1, w2, b2, (unsigned short*)d_out);
}
```

```cpp
#include <hip/hip_runtime.h>
#include <stdint.h>

typedef unsigned u32x4 __attribute__((ext_vector_type(4)));
typedef unsigned u32x2 __attribute__((ext_vector_type(2)));
typedef float f32x4 __attribute__((ext_vector_type(4)));
typedef float f32x2 __attribute__((ext_vector_type(2)));
typedef __bf16 bf16x2 __attribute__((ext_vector_type(2)));
typedef __bf16 bf16x8 __attribute__((ext_vector_type(8)));
typedef __amdgpu_buffer_rsrc_t rsrc_t;
#define LAS __attribute__((address_space(3)))

constexpr int D = 640, H = 32, SK = 4096, DH = 192, HD = H * DH, F = 1536;
constexpr unsigned SEQB = SK * DH * 2, TILEB = 8 * DH * 2, KVB = 4u * H * SEQB, WB = D * HD * 2;
#define NPRE 4
constexpr int REP = 8;
constexpr unsigned P1REC = 208 * 8;
constexpr unsigned WS_QP = 4096, WS_P1 = WS_QP + H * 2 * 4 * DH * 8, WS_P2 = WS_P1 + 256 * P1REC, WS_P3 = WS_P2 + 4 * H * D * 8,
                   WS_END = WS_P3 + REP * 4 * 320 * 8;
constexpr unsigned OOB = 0x7ffffff0u;
constexpr unsigned SPIN_CAP = 1u << 17;
constexpr float L2E = 1.4426950408889634f, SCALE = 0.07216878364870322f;
constexpr int L_CNT = 0, L_Q = 4, L_REC = L_Q + 96, L_O = L_REC + 4 * 196, L_RED = L_O + 4 * 112, L_X = L_RED + 8 * 16, L_T = L_X + 8, L_HH = L_T + D,
              L_XN = L_HH + D, L_QR = L_XN + 4 * 320, L_MIX = L_QR + 4 * 256, L_WO = L_MIX + 3 * F / 2, L_PROF = L_WO + DH * 68, L_END = L_PROF + 64;
constexpr int LDS_DW = 24576;
static_assert(L_END <= LDS_DW && L_REC % 4 == 0 && L_O % 4 == 0 && L_T % 4 == 0 && L_HH % 4 == 0 && L_XN % 4 == 0 && L_QR % 4 == 0 && L_MIX % 4 == 0 && L_WO % 4 == 0, "lds map");

__device__ __forceinline__ float bflo(unsigned w) { return __uint_as_float(w << 16); }
__device__ __forceinline__ float bfhi(unsigned w) { return __uint_as_float(w & 0xffff0000u); }
__device__ __forceinline__ unsigned pk2(float lo, float hi) { const bf16x2 v = {(__bf16)lo, (__bf16)hi}; return __builtin_bit_cast(unsigned, v); }
__device__ __forceinline__ float rbf(float f) { return (float)(__bf16)f; }
__device__ __forceinline__ float dot2(unsigned a, unsigned b, float c) {
  return __builtin_amdgcn_fdot2_f32_bf16(__builtin_bit_cast(bf16x2, a), __builtin_bit_cast(bf16x2, b), c, false);
}
template <int CTRL> __device__ __forceinline__ float dpp(float x) {
  return __builtin_bit_cast(float, __builtin_amdgcn_mov_dpp(__builtin_bit_cast(int, x), CTRL, 0xf, 0xf, true));
}
constexpr int XOR1 = 0xB1, XOR2 = 0x4E, HMIR = 0x141, ROR8 = 0x128;
__device__ __forceinline__ float sw32(float a, float b) {
  const auto r = __builtin_amdgcn_permlane32_swap(__float_as_uint(a), __float_as_uint(b), false, false);
  return __uint_as_float(r[0]) + __uint_as_float(r[1]);
}
__device__ __forceinline__ float sw16(float a, float b) {
  const auto r = __builtin_amdgcn_permlane16_swap(__float_as_uint(a), __float_as_uint(b), false, false);
  return __uint_as_float(r[0]) + __uint_as_float(r[1]);
}
__device__ __forceinline__ float sum16(float v) { v += dpp<XOR1>(v); v += dpp<XOR2>(v); v += dpp<HMIR>(v); v += dpp<ROR8>(v); return v; }
__device__ __forceinline__ float sum64(float v) { v = sum16(v); v = sw16(v, v); return sw32(v, v); }
__device__ __forceinline__ rsrc_t mk(const void* p, unsigned n) { return __builtin_amdgcn_make_buffer_rsrc((void*)p, 0, n, 0x00020000); }
__device__ __forceinline__ u32x4 ld_nt(rsrc_t r, unsigned v, unsigned s) { return __builtin_amdgcn_raw_buffer_load_b128(r, v, s, 2); }
__device__ __forceinline__ u32x4 ld_sc(rsrc_t r, unsigned v) { return __builtin_amdgcn_raw_buffer_load_b128(r, v, 0, 16); }
__device__ __forceinline__ void st_sc(rsrc_t r, unsigned v, u32x4 x) { __builtin_amdgcn_raw_buffer_store_b128(x, r, v, 0, 16); }
#define PIN() do { asm volatile("" ::: "memory"); __builtin_amdgcn_sched_barrier(0); } while (0)
#define WG_BAR() do { asm volatile("s_waitcnt lgkmcnt(0)" ::: "memory"); __builtin_amdgcn_s_barrier(); PIN(); } while (0)
#define STAMP(i) do { } while (0)
struct Tag { unsigned lo, hi, t32; };
__device__ __forceinline__ u32x4 enc2(float a, float b, const Tag& t) {
  return u32x4{(__float_as_uint(a) & 0xffffff00u) | t.lo, t.hi, (__float_as_uint(b) & 0xffffff00u) | t.lo, t.hi};
}
__device__ __forceinline__ unsigned bad2(u32x4 g, const Tag& t) { return (((g[0] ^ t.lo) | (g[2] ^ t.lo)) & 0xffu) | (g[1] ^ t.hi) | (g[3] ^ t.hi); }
__device__ __forceinline__ float dec(unsigned g) { return __uint_as_float(g & 0xffffff00u); }
__device__ __forceinline__ void helper_bar(LAS unsigned* cnt, unsigned target, int lane) {
  asm volatile("s_waitcnt lgkmcnt(0)" ::: "memory");
  if (lane == 0) __hip_atomic_fetch_add(cnt, 1u, __ATOMIC_RELAXED, __HIP_MEMORY_SCOPE_WORKGROUP);
  while ((unsigned)__builtin_amdgcn_readfirstlane(__hip_atomic_load(cnt, __ATOMIC_RELAXED, __HIP_MEMORY_SCOPE_WORKGROUP)) < target) { }
  asm volatile("" ::: "memory");
}
__device__ __forceinline__ float inv_sd(float var) { return __builtin_amdgcn_rcpf(rbf(__builtin_amdgcn_sqrtf(rbf(rbf(var) + 1e-5f)))); }

__global__ void __launch_bounds__(512, 2)
fused_decode(const unsigned short* x, const unsigned short* kc, const unsigned short* vc,
             const unsigned short* wq, const unsigned short* wo, const unsigned short* w1,
             const unsigned short* b1, const unsigned short* w2, const unsigned short* b2,
             unsigned short* out, unsigned char* ws) {
  __shared__ __attribute__((aligned(16))) unsigned lds_raw[LDS_DW];
  LAS unsigned* lds = (LAS unsigned*)lds_raw;
  LAS float* ldf = (LAS float*)lds_raw;
  const int tid = threadIdx.x, lane = tid & 63, w = __builtin_amdgcn_readfirstlane(tid >> 6);
  const int bid = blockIdx.x, h = bid >> 3, j = bid & 7, b = j >> 1, s = j & 1;
  const int b2i = bid >> 6, cg = bid & 63;
  const rsrc_t wsr = mk(ws, WS_END);
  if (w == 0) STAMP(0);
  u32x2 ep = __builtin_amdgcn_raw_buffer_load_b64(wsr, 0, 0, 16);
  bf16x8 Bf[2][6];
  const unsigned lo32 = lane < 32 ? ~0u : 0u;

  if (w < 4) {
    const int r = lane >> 3, c = lane & 7;
    const unsigned voff = r * 384 + c * 16;
    const unsigned seq = (unsigned)(b * H + h) * SEQB + (s ? (511 - w) * TILEB : w * TILEB);
    const int step = s ? -4 * (int)TILEB : 4 * (int)TILEB;
    constexpr int NT = 64;
    const rsrc_t kr = mk(kc, KVB), vr = mk(vc, KVB);
    u32x4 K[NPRE][3], V[NPRE][3];
    WG_BAR();
#pragma unroll
    for (int p = 0; p < NPRE; ++p) {
      if (p) asm volatile("s_waitcnt vmcnt(3)" ::: "memory");
#pragma unroll
      for (int i = 0; i < 3; ++i) K[p][i] = ld_nt(kr, voff + 128 * i, seq + p * step);
      PIN();
      if (p) asm volatile("s_waitcnt vmcnt(3)" ::: "memory");
#pragma unroll
      for (int i = 0; i < 3; ++i) V[p][i] = ld_nt(vr, voff + 128 * i, seq + p * step);
      PIN();
    }
    if (w == 0) STAMP(1);
    WG_BAR();
    if (w == 0) STAMP(2);
    u32x4 qf[3];
#pragma unroll
    for (int i = 0; i < 3; ++i) qf[i] = *(LAS const u32x4*)(lds + L_Q + (c + 8 * i) * 4);
    float m = __uint_as_float(0xff7f0000u), l = 0.f, acc[24];
#pragma unroll
    for (int k = 0; k < 24; ++k) acc[k] = 0.f;
    auto step_fn = [&](u32x4 (&Kc)[3], u32x4 (&Vc)[3], bool refill, unsigned nbytes, unsigned soff) {
      float a0 = 0.f, a1 = 0.f, a2 = 0.f;
#pragma unroll
      for (int k = 0; k < 4; ++k) { a0 = dot2(Kc[0][k], qf[0][k], a0); a1 = dot2(Kc[1][k], qf[1][k], a1); a2 = dot2(Kc[2][k], qf[2][k], a2); }
      float a = (a0 + a1) + a2;
      PIN();
      if (refill) {
        const rsrc_t k2 = mk(kc, nbytes);
#pragma unroll
        for (int i = 0; i < 3; ++i) Kc[i] = ld_nt(k2, voff + 128 * i, soff);
      }
      PIN();
      a += dpp<XOR1>(a); a += dpp<XOR2>(a); a += dpp<HMIR>(a);
      const float sc = rbf(rbf(a) * SCALE);
      const float mn = fmaxf(m, sc);
      const float alpha = __builtin_amdgcn_exp2f((m - mn) * L2E), p = __builtin_amdgcn_exp2f((sc - mn) * L2E);
      l = fmaf(l, alpha, p); m = mn;
#pragma unroll
      for (int i = 0; i < 3; ++i)
#pragma unroll
        for (int k = 0; k < 4; ++k) {
          acc[i * 8 + 2 * k]     = fmaf(p, bflo(Vc[i][k]), acc[i * 8 + 2 * k] * alpha);
          acc[i * 8 + 2 * k + 1] = fmaf(p, bfhi(Vc[i][k]), acc[i * 8 + 2 * k + 1] * alpha);
        }
      PIN();
      if (refill) {
        const rsrc_t v2 = mk(vc, nbytes);
#pragma unroll
        for (int i = 0; i < 3; ++i) Vc[i] = ld_nt(v2, voff + 128 * i, soff);
      }
      PIN();
    };
    step_fn(K[0], V[0], true, KVB, seq + NPRE * step);
#pragma unroll
    for (int p = 1; p < NPRE; ++p) step_fn(K[p], V[p], false, 0, 0);
    int t = NPRE + 1;
    do { step_fn(K[0], V[0], true, t < NT ? KVB : 0u, seq + t * step); } while (++t <= NT);
    if (w == 0) STAMP(3);
    float M = fmaxf(m, dpp<ROR8>(m));
    { const auto q16 = __builtin_amdgcn_permlane16_swap(__float_as_uint(M), __float_as_uint(M), false, false);
      M = fmaxf(__uint_as_float(q16[0]), __uint_as_float(q16[1]));
      const auto q32 = __builtin_amdgcn_permlane32_swap(__float_as_uint(M), __float_as_uint(M), false, false);
      M = fmaxf(__uint_as_float(q32[0]), __uint_as_float(q32[1])); }
    const float f = __builtin_amdgcn_exp2f((m - M) * L2E);
    l *= f; l += dpp<ROR8>(l); l = sw16(l, l); l = sw32(l, l);
    const bool b3 = lane & 8;
#pragma unroll
    for (int i = 0; i < 3; ++i) {
      float y[4], z[2];
#pragma unroll
      for (int e = 0; e < 4; ++e) y[e] = sw32(acc[i * 8 + e] * f, acc[i * 8 + e + 4] * f);
#pragma unroll
      for (int e = 0; e < 2; ++e) z[e] = sw16(y[e], y[e + 2]);
      const float keep = b3 ? z[1] : z[0], send = b3 ? z[0] : z[1];
      ldf[L_REC + w * 196 + i * 64 + c * 8 + r] = keep + dpp<ROR8>(send);
    }
    if (lane == 0) { ldf[L_REC + w * 196 + 192] = M; ldf[L_REC + w * 196 + 193] = l; }
    if (w == 0) STAMP(4);
  } else {
    const int hid = tid - 256, hw = w - 4;
    if (hid == 0) lds[L_CNT] = 0;
    const int kh = j / 3, cq = j % 3, rg = hid >> 3, c8 = hid & 7;
    u32x4 W[10], X[5];
    const rsrc_t xr = mk(x, j < 6 ? 4 * D * 2 : 0), wqr = mk(wq, j < 6 ? WB : 0);
#pragma unroll
    for (int i = 0; i < 5; ++i) X[i] = __builtin_amdgcn_raw_buffer_load_b128(xr, (lane >> 4) * (D * 2) + ((lane & 15) + 16 * i) * 16, 0, 0);
#pragma unroll
    for (int i = 0; i < 10; ++i)
      W[i] = ld_nt(wqr, (unsigned)(2 * rg + (i & 1)) * (HD * 2) + c8 * 16, (unsigned)(kh * 320 + (i >> 1) * 64) * (HD * 2) + (h * DH + cq * 64) * 2);
    PIN();
    WG_BAR();
    asm volatile("" : "+v"(ep));
    const unsigned long long E1 = ((unsigned long long)__builtin_amdgcn_readfirstlane(ep[1]) << 32 | (unsigned)__builtin_amdgcn_readfirstlane(ep[0])) + 1;
    const Tag tg{(unsigned)E1 & 0xffu, (unsigned)(E1 >> 8), (unsigned)E1};
    if (j < 6) {
      float v[40], sm = 0.f;
#pragma unroll
      for (int i = 0; i < 5; ++i)
#pragma unroll
        for (int k = 0; k < 4; ++k) { v[i * 8 + 2 * k] = bflo(X[i][k]); v[i * 8 + 2 * k + 1] = bfhi(X[i][k]); sm += v[i * 8 + 2 * k] + v[i * 8 + 2 * k + 1]; }
      const float mean = sum16(sm) * (1.f / D);
      float s2 = 0.f;
#pragma unroll
      for (int k = 0; k < 40; ++k) { const float d = v[k] - mean; s2 = fmaf(d, d, s2); }
      const float inv = inv_sd(sum16(s2) * (1.f / D)), mb = rbf(mean);
#pragma unroll
      for (int i = 0; i < 5; ++i) {
        u32x4 o;
#pragma unroll
        for (int k = 0; k < 4; ++k) o[k] = pk2(rbf(v[i * 8 + 2 * k] - mb) * inv, rbf(v[i * 8 + 2 * k + 1] - mb) * inv);
        *(LAS u32x4*)(lds + L_XN + (lane >> 4) * 320 + ((lane & 15) + 16 * i) * 4) = o;
      }
      asm volatile("s_waitcnt lgkmcnt(0)" ::: "memory");
      if (hw == 0) STAMP(8);
      float qa[32];
#pragma unroll
      for (int k = 0; k < 32; ++k) qa[k] = 0.f;
#pragma unroll
      for (int i = 0; i < 5; ++i) {
        unsigned xp[4];
#pragma unroll
        for (int bb = 0; bb < 4; ++bb) xp[bb] = lds[L_XN + bb * 320 + kh * 160 + i * 32 + rg];
#pragma unroll
        for (int k = 0; k < 4; ++k) {
          const unsigned lo = __builtin_amdgcn_perm(W[2 * i + 1][k], W[2 * i][k], 0x05040100u), hi = __builtin_amdgcn_perm(W[2 * i + 1][k], W[2 * i][k], 0x07060302u);
#pragma unroll
          for (int bb = 0; bb < 4; ++bb) { qa[bb * 8 + 2 * k] = dot2(lo, xp[bb], qa[bb * 8 + 2 * k]); qa[bb * 8 + 2 * k + 1] = dot2(hi, xp[bb], qa[bb * 8 + 2 * k + 1]); }
        }
      }
      if (hw == 0) STAMP(9);
      const bool b3 = lane & 8;
#pragma unroll
      for (int bb = 0; bb < 4; ++bb) {
        float y[4], z[2];
#pragma unroll
        for (int e = 0; e < 4; ++e) y[e] = sw32(qa[bb * 8 + e], qa[bb * 8 + e + 4]);
#pragma unroll
        for (int e = 0; e < 2; ++e) z[e] = sw16(y[e], y[e + 2]);
        const float keep = b3 ? z[1] : z[0], send = b3 ? z[0] : z[1];
        ldf[L_QR + hw * 256 + bb * 64 + c8 * 8 + (lane >> 3)] = keep + dpp<ROR8>(send);
      }
      helper_bar(lds + L_CNT, 4, lane);
      if (hw < 2) {
        f32x2 a = *(LAS const f32x2*)(ldf + L_QR + 2 * hid);
#pragma unroll
        for (int ww = 1; ww < 4; ++ww) a += *(LAS const f32x2*)(ldf + L_QR + ww * 256 + 2 * hid);
        st_sc(wsr, WS_QP + (((h * 2 + kh) * 4 + (hid >> 5)) * DH + cq * 64 + (hid & 31) * 2) * 8, enc2(a[0], a[1], tg));
      }
      if (hw == 0) STAMP(10);
    }
    if (hw >= 2) {
      const int u = hid - 128;
      const unsigned o0 = u < 96 ? WS_QP + ((h * 2 * 4 + b) * DH + 2 * u) * 8 : OOB, o1 = u < 96 ? o0 + 4 * DH * 8 : OOB;
      const unsigned vm = u < 96 ? ~0u : 0u;
      u32x4 g0, g1;
      _Pragma("nounroll") for (unsigned spin = 0; spin < SPIN_CAP; ++spin) {
        asm volatile("" ::: "memory");
        g0 = ld_sc(wsr, o0); g1 = ld_sc(wsr, o1);
        if (__all(((bad2(g0, tg) | bad2(g1, tg)) & vm) == 0)) break;
        __builtin_amdgcn_s_sleep(2);
      }
      if (u < 96) lds[L_Q + u] = pk2(dec(g0[0]) + dec(g1[0]), dec(g0[2]) + dec(g1[2]));
      if (hw == 2) STAMP(11);
    }
    WG_BAR();
    const rsrc_t wor = mk(wo, j < 5 ? WB : 0);
    u32x4 Wo[12];
#pragma unroll
    for (int i = 0; i < 12; ++i) Wo[i] = ld_nt(wor, (unsigned)(hid >> 4) * (D * 2) + (hid & 15) * 16, (unsigned)(h * DH + 16 * i) * (D * 2) + j * 256);
    if (hid < 192) {
      *(LAS u32x4*)(lds + L_MIX + hid * 4) = *(const u32x4*)(w1 + hid * 8);
      *(LAS u32x4*)(lds + L_MIX + 768 + hid * 4) = *(const u32x4*)(b1 + hid * 8);
      *(LAS u32x4*)(lds + L_MIX + 1536 + hid * 4) = *(const u32x4*)(w2 + hid * 8);
    }
    if (hid < 5) lds[L_X + hid] = *(const unsigned*)(x + b2i * D + cg * 10 + 2 * hid);
    if (hid == 5) lds[L_X + 5] = b2[0];
#pragma unroll
    for (int i = 0; i < 12; ++i) *(LAS u32x4*)(lds + L_WO + ((hid >> 4) + 16 * i) * 68 + (hid & 15) * 4) = Wo[i];
    helper_bar(lds + L_CNT, j < 6 ? 8 : 4, lane);
    LAS const unsigned short* wl = (LAS const unsigned short*)(lds + L_WO) + (lane >> 4) * 8 * 136 + hw * 32 + (lane & 15);
#pragma unroll
    for (int nt = 0; nt < 2; ++nt)
#pragma unroll
      for (int ks = 0; ks < 6; ++ks) {
        u32x4 fr;
#pragma unroll
        for (int e = 0; e < 4; ++e) fr[e] = (unsigned)wl[(ks * 32 + 2 * e) * 136 + nt * 16] | ((unsigned)wl[(ks * 32 + 2 * e + 1) * 136 + nt * 16] << 16);
        Bf[nt][ks] = __builtin_bit_cast(bf16x8, fr);
      }
  }
  const unsigned long long E1 = ((unsigned long long)__builtin_amdgcn_readfirstlane(ep[1]) << 32 | (unsigned)__builtin_amdgcn_readfirstlane(ep[0])) + 1;
  const Tag tg{(unsigned)E1 & 0xffu, (unsigned)(E1 >> 8), (unsigned)E1};
  WG_BAR();
  if (w == 4) STAMP(12);
  if (w == 4 || w == 5) {
    const int u = tid - 256;
    float mm[4], M;
#pragma unroll
    for (int i = 0; i < 4; ++i) mm[i] = ldf[L_REC + i * 196 + 192];
    M = fmaxf(fmaxf(mm[0], mm[1]), fmaxf(mm[2], mm[3]));
    f32x2 a = {0.f, 0.f};
    const int uu = u < 96 ? u : 96;
#pragma unroll
    for (int i = 0; i < 4; ++i) a += __builtin_amdgcn_exp2f((mm[i] - M) * L2E) * *(LAS const f32x2*)(ldf + L_REC + i * 196 + 2 * uu);
    st_sc(wsr, u <= 96 ? WS_P1 + bid * P1REC + u * 16 : OOB, enc2(u < 96 ? a[0] : M, a[1], tg));
    if (w == 4) STAMP(13);
  }
  float mw1[3], mb1[3], mw2[3];
  {
    LAS const unsigned short* mx = (LAS const unsigned short*)(lds + L_MIX);
#pragma unroll
    for (int i = 0; i < 3; ++i) { mw1[i] = bflo(mx[tid + 512 * i]); mb1[i] = bflo(mx[F + tid + 512 * i]); mw2[i] = bflo(mx[2 * F + tid + 512 * i]); }
  }
  if (j < 5) {
    if (w < 4) {
      const unsigned r0 = WS_P1 + (h * 8 + 2 * w) * P1REC, oa = r0 + lane * 16, ob = lane < 32 ? oa + 1024 : OOB, oc = lane < 32 ? ob + P1REC : OOB;
      u32x4 a0, a1, c0, c1, s0, s1;
      _Pragma("nounroll") for (unsigned spin = 0; spin < SPIN_CAP; ++spin) {
        asm volatile("" ::: "memory");
        s0 = ld_sc(wsr, r0 + 96 * 16); s1 = ld_sc(wsr, r0 + P1REC + 96 * 16);
        a0 = ld_sc(wsr, oa); a1 = ld_sc(wsr, oa + P1REC); c0 = ld_sc(wsr, ob); c1 = ld_sc(wsr, oc);
        const unsigned bad = bad2(s0, tg) | bad2(s1, tg) | bad2(a0, tg) | bad2(a1, tg) | ((bad2(c0, tg) | bad2(c1, tg)) & lo32);
        if (__all(bad == 0)) break;
        __builtin_amdgcn_s_sleep(1);
      }
      if (w == 0) STAMP(5);
      const float m0 = dec(s0[0]), m1 = dec(s1[0]), M = fmaxf(m0, m1);
      float w0 = __builtin_amdgcn_exp2f((m0 - M) * L2E), w1_ = __builtin_amdgcn_exp2f((m1 - M) * L2E);
      const float inv = __builtin_amdgcn_rcpf(fmaf(w0, dec(s0[2]), w1_ * dec(s1[2])));
      w0 *= inv; w1_ *= inv;
      lds[L_O + w * 112 + lane] = pk2(fmaf(w0, dec(a0[0]), w1_ * dec(a1[0])), fmaf(w0, dec(a0[2]), w1_ * dec(a1[2])));
      if (lane < 32) lds[L_O + w * 112 + 64 + lane] = pk2(fmaf(w0, dec(c0[0]), w1_ * dec(c1[0])), fmaf(w0, dec(c0[2]), w1_ * dec(c1[2])));
    }
    WG_BAR();
    if (w >= 4) {
      f32x4 d0 = {0.f, 0.f, 0.f, 0.f}, d1 = d0;
#pragma unroll
      for (int ks = 0; ks < 6; ++ks) {
        const bf16x8 af = __builtin_bit_cast(bf16x8, *(LAS const u32x4*)(lds + L_O + (lane & 3) * 112 + ks * 16 + (lane >> 4) * 4));
        d0 = __builtin_amdgcn_mfma_f32_16x16x32_bf16(af, Bf[0][ks], d0, 0, 0, 0);
        d1 = __builtin_amdgcn_mfma_f32_16x16x32_bf16(af, Bf[1][ks], d1, 0, 0, 0);
      }
      const int g = lane >> 4, n = lane & 15;
      const bool g1 = lane & 16, g2 = lane & 32;
      const float a01 = g1 ? d0[1] : d0[0], a23 = g1 ? d0[3] : d0[2], c01 = g1 ? d1[1] : d1[0], c23 = g1 ? d1[3] : d1[2];
      const float a = g2 ? a23 : a01, cc = g2 ? c23 : c01;
      const float xa = dpp<XOR1>(a), xc = dpp<XOR1>(cc);
      const bool odd = lane & 1;
      st_sc(wsr, WS_P2 + ((g * H + h) * D + j * 128 + (w - 4) * 32 + (odd ? 15 + n : n)) * 8, enc2(odd ? xc : a, odd ? cc : xa, tg));
      if (w == 4) STAMP(14);
    }
  }
  if (w < 3) {
    const int pp = 2 * w + (lane >> 5);
    const unsigned o = pp < 5 ? WS_P2 + ((b2i * H + (lane & 31)) * D + cg * 10 + 2 * pp) * 8 : OOB;
    const unsigned xr = lds[L_X + (pp < 5 ? pp : 0)];
    const unsigned vm = pp < 5 ? ~0u : 0u;
    u32x4 g;
    _Pragma("nounroll") for (unsigned spin = 0; spin < SPIN_CAP; ++spin) {
      asm volatile("" ::: "memory");
      g = ld_sc(wsr, o);
      if (__all((bad2(g, tg) & vm) == 0)) break;
      __builtin_amdgcn_s_sleep(1);
    }
    if (w == 0) STAMP(6);
    float y0 = sum16(dec(g[0])), y1 = sum16(dec(g[2]));
    y0 = sw16(y0, y0); y1 = sw16(y1, y1);
    const unsigned hp = pk2(bflo(xr) + rbf(y0), bfhi(xr) + rbf(y1));
    __builtin_amdgcn_raw_buffer_store_b64(u32x2{hp, tg.t32}, wsr, (pp < 5 && (lane & 31) < REP) ? WS_P3 + (((lane & 31) * 4 + b2i) * 320 + cg * 5 + pp) * 8 : OOB, 0, 16);
  } else if (w == 3) {
    const unsigned o = WS_P3 + (((bid % REP) * 4 + b2i) * 320) * 8 + lane * 16, o2 = lane < 32 ? o + 2048 : OOB;
    u32x4 g[3];
    _Pragma("nounroll") for (unsigned spin = 0; spin < SPIN_CAP; ++spin) {
      asm volatile("" ::: "memory");
      g[0] = ld_sc(wsr, o); g[1] = ld_sc(wsr, o + 1024); g[2] = ld_sc(wsr, o2);
      const unsigned bad = (g[0][1] ^ tg.t32) | (g[0][3] ^ tg.t32) | (g[1][1] ^ tg.t32) | (g[1][3] ^ tg.t32) | (((g[2][1] ^ tg.t32) | (g[2][3] ^ tg.t32)) & lo32);
      if (__all(bad == 0)) break;
      __builtin_amdgcn_s_sleep(1);
    }
    STAMP(7);
    if (bid == 0 && lane == 0) __builtin_amdgcn_raw_buffer_store_b64(u32x2{(unsigned)E1, (unsigned)(E1 >> 32)}, wsr, 0, 0, 16);
    float v[12];
#pragma unroll
    for (int i = 0; i < 3; ++i) { v[4 * i] = bflo(g[i][0]); v[4 * i + 1] = bfhi(g[i][0]); v[4 * i + 2] = bflo(g[i][2]); v[4 * i + 3] = bfhi(g[i][2]); }
#pragma unroll
    for (int i = 0; i < 3; ++i) if (i < 2 || lane < 32) *(LAS f32x4*)(ldf + L_HH + (lane + 64 * i) * 4) = f32x4{v[4 * i], v[4 * i + 1], v[4 * i + 2], v[4 * i + 3]};
    const float tail = lane < 32 ? 1.f : 0.f;
#pragma unroll
    for (int pass = 0; pass < 2; ++pass) {
      float sm = ((v[0] + v[1]) + (v[2] + v[3])) + ((v[4] + v[5]) + (v[6] + v[7])) + tail * ((v[8] + v[9]) + (v[10] + v[11]));
      const float mean = sum64(sm) * (1.f / D);
      float s2 = 0.f, s3 = 0.f;
#pragma unroll
      for (int k = 0; k < 8; ++k) { const float d = v[k] - mean; s2 = fmaf(d, d, s2); }
#pragma unroll
      for (int k = 8; k < 12; ++k) { const float d = v[k] - mean; s3 = fmaf(d, d, s3); }
      const float inv = inv_sd(sum64(fmaf(tail, s3, s2)) * (1.f / D)), mb = rbf(mean);
#pragma unroll
      for (int k = 0; k < 12; ++k) v[k] = rbf(rbf(v[k] - mb) * inv);
    }
#pragma unroll
    for (int i = 0; i < 3; ++i) if (i < 2 || lane < 32) *(LAS f32x4*)(ldf + L_T + (lane + 64 * i) * 4) = f32x4{v[4 * i], v[4 * i + 1], v[4 * i + 2], v[4 * i + 3]};
  }
  WG_BAR();
  if (w == 0) STAMP(15);
  {
    float ac[10];
#pragma unroll
    for (int i = 0; i < 10; ++i) {
      const float t = ldf[L_T + cg * 10 + i];
      ac[i] = 0.f;
#pragma unroll
      for (int k = 0; k < 3; ++k) {
        const float u = fmaf(t, mw1[k], mb1[k]);
        const float z = u * fmaf(u * u, -2.f * L2E * 0.044715f * 0.7978845608028654f, -2.f * L2E * 0.7978845608028654f);
        ac[i] = fmaf(u * __builtin_amdgcn_rcpf(1.f + __builtin_amdgcn_exp2f(z)), mw2[k], ac[i]);
      }
    }
    float y[5];
#pragma unroll
    for (int i = 0; i < 5; ++i) y[i] = sw32(ac[i], ac[i + 5]);
    const float z0 = sum16(sw16(y[0], y[1])), z1 = sum16(sw16(y[2], y[3])), z2 = sum16(sw16(y[4], y[4]));
    if ((lane & 15) == 0) {
      const int o = L_RED + w * 16 + (lane >> 5) * 5, odd = (lane >> 4) & 1;
      ldf[o + odd] = z0; ldf[o + 2 + odd] = z1; ldf[o + 4] = z2;
    }
  }
  if (w == 0) STAMP(16);
  WG_BAR();
  if (tid < 10) {
    float y = 0.f;
#pragma unroll
    for (int i = 0; i < 8; ++i) y += ldf[L_RED + i * 16 + tid];
    const __bf16 o = (__bf16)(ldf[L_HH + cg * 10 + tid] + rbf(rbf(y) + bflo(lds[L_X + 5])));
    out[b2i * D + cg * 10 + tid] = __builtin_bit_cast(unsigned short, o);
  }
}

extern "C" void kernel_launch(void* const* d_in, const int* in_sizes, int n_in, void* d_out, int out_size, void* d_ws, size_t ws_size,
                              hipStream_t stream) {
  typedef const unsigned short* P;
  fused_decode<<<256, 512, 0, stream>>>((P)d_in[0], (P)d_in[1], (P)d_in[2], (P)d_in[3], (P)d_in[4], (P)d_in[6], (P)d_in[7], (P)d_in[8], (P)d_in[9],
                                        (unsigned short*)d_out, (unsigned char*)d_ws);
}
```

```cpp
#include <hip/hip_runtime.h>
#include <stdint.h>

typedef unsigned u32x4 __attribute__((ext_vector_type(4)));
typedef unsigned u32x2 __attribute__((ext_vector_type(2)));
typedef float f32x4 __attribute__((ext_vector_type(4)));
typedef float f32x2 __attribute__((ext_vector_type(2)));
typedef __bf16 bf16x2 __attribute__((ext_vector_type(2)));
typedef __bf16 bf16x8 __attribute__((ext_vector_type(8)));
typedef __amdgpu_buffer_rsrc_t rsrc_t;
#define LAS __attribute__((address_space(3)))

constexpr int D = 640, H = 32, SK = 4096, DH = 192, HD = H * DH, F = 1536;
constexpr unsigned SEQB = SK * DH * 2, TILEB = 8 * DH * 2, KVB = 4u * H * SEQB, WB = D * HD * 2;
#define NPRE 4
constexpr int REP = 8;
constexpr unsigned P1REC = 208 * 8;
constexpr unsigned WS_QP = 4096, WS_P1 = WS_QP + H * 2 * 4 * DH * 8, WS_P2 = WS_P1 + 256 * P1REC, WS_P3 = WS_P2 + 4 * H * D * 8,
                   WS_END = WS_P3 + REP * 4 * 320 * 8;
constexpr unsigned OOB = 0x7ffffff0u;
constexpr unsigned SPIN_CAP = 1u << 17;
constexpr float L2E = 1.4426950408889634f, SCALE = 0.07216878364870322f;
constexpr int L_CNT = 0, L_Q = 4, L_REC = L_Q + 96, L_O = L_REC + 4 * 196, L_RED = L_O + 4 * 112, L_X = L_RED + 8 * 16, L_T = L_X + 8, L_HH = L_T + D,
              L_XN = L_HH + D, L_QR = L_XN + 4 * 320, L_MIX = L_QR + 4 * 256, L_WO = L_MIX + 3 * F / 2, L_PROF = L_WO + DH * 68, L_END = L_PROF + 64;
constexpr int LDS_DW = 24576;
static_assert(L_END <= LDS_DW && L_REC % 4 == 0 && L_O % 4 == 0 && L_T % 4 == 0 && L_HH % 4 == 0 && L_XN % 4 == 0 && L_QR % 4 == 0 && L_MIX % 4 == 0 && L_WO % 4 == 0, "lds map");

__device__ __forceinline__ float bflo(unsigned w) { return __uint_as_float(w << 16); }
__device__ __forceinline__ float bfhi(unsigned w) { return __uint_as_float(w & 0xffff0000u); }
__device__ __forceinline__ unsigned pk2(float lo, float hi) { const bf16x2 v = {(__bf16)lo, (__bf16)hi}; return __builtin_bit_cast(unsigned, v); }
__device__ __forceinline__ float rbf(float f) { return (float)(__bf16)f; }
__device__ __forceinline__ float dot2(unsigned a, unsigned b, float c) {
  return __builtin_amdgcn_fdot2_f32_bf16(__builtin_bit_cast(bf16x2, a), __builtin_bit_cast(bf16x2, b), c, false);
}
template <int CTRL> __device__ __forceinline__ float dpp(float x) {
  return __builtin_bit_cast(float, __builtin_amdgcn_mov_dpp(__builtin_bit_cast(int, x), CTRL, 0xf, 0xf, true));
}
constexpr int XOR1 = 0xB1, XOR2 = 0x4E, HMIR = 0x141, ROR8 = 0x128;
__device__ __forceinline__ float sw32(float a, float b) {
  const auto r = __builtin_amdgcn_permlane32_swap(__float_as_uint(a), __float_as_uint(b), false, false);
  return __uint_as_float(r[0]) + __uint_as_float(r[1]);
}
__device__ __forceinline__ float sw16(float a, float b) {
  const auto r = __builtin_amdgcn_permlane16_swap(__float_as_uint(a), __float_as_uint(b), false, false);
  return __uint_as_float(r[0]) + __uint_as_float(r[1]);
}
__device__ __forceinline__ float sum16(float v) { v += dpp<XOR1>(v); v += dpp<XOR2>(v); v += dpp<HMIR>(v); v += dpp<ROR8>(v); return v; }
__device__ __forceinline__ float sum64(float v) { v = sum16(v); v = sw16(v, v); return sw32(v, v); }
__device__ __forceinline__ rsrc_t mk(const void* p, unsigned n) { return __builtin_amdgcn_make_buffer_rsrc((void*)p, 0, n, 0x00020000); }
__device__ __forceinline__ u32x4 ld_nt(rsrc_t r, unsigned v, unsigned s) { return __builtin_amdgcn_raw_buffer_load_b128(r, v, s, 2); }
__device__ __forceinline__ u32x4 ld_sc(rsrc_t r, unsigned v) { return __builtin_amdgcn_raw_buffer_load_b128(r, v, 0, 16); }
__device__ __forceinline__ void st_sc(rsrc_t r, unsigned v, u32x4 x) { __builtin_amdgcn_raw_buffer_store_b128(x, r, v, 0, 16); }
#define PIN() do { asm volatile("" ::: "memory"); __builtin_amdgcn_sched_barrier(0); } while (0)
#define WG_BAR() do { asm volatile("s_waitcnt lgkmcnt(0)" ::: "memory"); __builtin_amdgcn_s_barrier(); PIN(); } while (0)
#define STAMP(i) do { } while (0)
struct Tag { unsigned lo, hi, t32; };
__device__ __forceinline__ u32x4 enc2(float a, float b, const Tag& t) {
  return u32x4{(__float_as_uint(a) & 0xffffff00u) | t.lo, t.hi, (__float_as_uint(b) & 0xffffff00u) | t.lo, t.hi};
}
__device__ __forceinline__ unsigned bad2(u32x4 g, const Tag& t) { return (((g[0] ^ t.lo) | (g[2] ^ t.lo)) & 0xffu) | (g[1] ^ t.hi) | (g[3] ^ t.hi); }
__device__ __forceinline__ float dec(unsigned g) { return __uint_as_float(g & 0xffffff00u); }
__device__ __forceinline__ void helper_bar(LAS unsigned* cnt, unsigned target, int lane) {
  asm volatile("s_waitcnt lgkmcnt(0)" ::: "memory");
  if (lane == 0) __hip_atomic_fetch_add(cnt, 1u, __ATOMIC_RELAXED, __HIP_MEMORY_SCOPE_WORKGROUP);
  while ((unsigned)__builtin_amdgcn_readfirstlane(__hip_atomic_load(cnt, __ATOMIC_RELAXED, __HIP_MEMORY_SCOPE_WORKGROUP)) < target) { }
  asm volatile("" ::: "memory");
}
__device__ __forceinline__ float inv_sd(float var) { return __builtin_amdgcn_rcpf(rbf(__builtin_amdgcn_sqrtf(rbf(rbf(var) + 1e-5f)))); }

__global__ void __launch_bounds__(512, 2)
fused_decode(const unsigned short* x, const unsigned short* kc, const unsigned short* vc,
             const unsigned short* wq, const unsigned short* wo, const unsigned short* w1,
             const unsigned short* b1, const unsigned short* w2, const unsigned short* b2,
             unsigned short* out, unsigned char* ws) {
  __shared__ __attribute__((aligned(16))) unsigned lds_raw[LDS_DW];
  LAS unsigned* lds = (LAS unsigned*)lds_raw;
  LAS float* ldf = (LAS float*)lds_raw;
  const int tid = threadIdx.x, lane = tid & 63, w = __builtin_amdgcn_readfirstlane(tid >> 6);
  const int bid = blockIdx.x, h = bid >> 3, j = bid & 7, b = j >> 1, s = j & 1;
  const int b2i = bid >> 6, cg = bid & 63;
  const rsrc_t wsr = mk(ws, WS_END);
  const unsigned long long tstart = __builtin_amdgcn_s_memrealtime();
  u32x2 ep = __builtin_amdgcn_raw_buffer_load_b64(wsr, 0, 0, 16);
  bf16x8 Bf[2][6];
  const unsigned lo32 = lane < 32 ? ~0u : 0u;

  if (w < 4) {
    const int r = lane >> 3, c = lane & 7;
    const unsigned voff = r * 384 + c * 16;
    const unsigned seq = (unsigned)(b * H + h) * SEQB + (s ? (511 - w) * TILEB : w * TILEB);
    const int step = s ? -4 * (int)TILEB : 4 * (int)TILEB;
    constexpr int NT = 64;
    const rsrc_t kr = mk(kc, KVB), vr = mk(vc, KVB);
    u32x4 K[NPRE][3], V[NPRE][3];
    WG_BAR();
#pragma unroll
    for (int p = 0; p < NPRE; ++p) {
      if (p) asm volatile("s_waitcnt vmcnt(3)" ::: "memory");
#pragma unroll
      for (int i = 0; i < 3; ++i) K[p][i] = ld_nt(kr, voff + 128 * i, seq + p * step);
      PIN();
      if (p) asm volatile("s_waitcnt vmcnt(3)" ::: "memory");
#pragma unroll
      for (int i = 0; i < 3; ++i) V[p][i] = ld_nt(vr, voff + 128 * i, seq + p * step);
      PIN();
    }
    if (w == 0) STAMP(1);
    WG_BAR();
    if (w == 0) STAMP(2);
    u32x4 qf[3];
#pragma unroll
    for (int i = 0; i < 3; ++i) qf[i] = *(LAS const u32x4*)(lds + L_Q + (c + 8 * i) * 4);
    float m = __uint_as_float(0xff7f0000u), l = 0.f, acc[24];
#pragma unroll
    for (int k = 0; k < 24; ++k) acc[k] = 0.f;
    auto step_fn = [&](u32x4 (&Kc)[3], u32x4 (&Vc)[3], bool refill, unsigned nbytes, unsigned soff) {
      float a0 = 0.f, a1 = 0.f, a2 = 0.f;
#pragma unroll
      for (int k = 0; k < 4; ++k) { a0 = dot2(Kc[0][k], qf[0][k], a0); a1 = dot2(Kc[1][k], qf[1][k], a1); a2 = dot2(Kc[2][k], qf[2][k], a2); }
      float a = (a0 + a1) + a2;
      PIN();
      if (refill) {
        const rsrc_t k2 = mk(kc, nbytes);
#pragma unroll
        for (int i = 0; i < 3; ++i) Kc[i] = ld_nt(k2, voff + 128 * i, soff);
      }
      PIN();
      a += dpp<XOR1>(a); a += dpp<XOR2>(a); a += dpp<HMIR>(a);
      const float sc = rbf(rbf(a) * SCALE);
      const float mn = fmaxf(m, sc);
      const float alpha = __builtin_amdgcn_exp2f((m - mn) * L2E), p = __builtin_amdgcn_exp2f((sc - mn) * L2E);
      l = fmaf(l, alpha, p); m = mn;
#pragma unroll
      for (int i = 0; i < 3; ++i)
#pragma unroll
        for (int k = 0; k < 4; ++k) {
          acc[i * 8 + 2 * k]     = fmaf(p, bflo(Vc[i][k]), acc[i * 8 + 2 * k] * alpha);
          acc[i * 8 + 2 * k + 1] = fmaf(p, bfhi(Vc[i][k]), acc[i * 8 + 2 * k + 1] * alpha);
        }
      PIN();
      if (refill) {
        const rsrc_t v2 = mk(vc, nbytes);
#pragma unroll
        for (int i = 0; i < 3; ++i) Vc[i] = ld_nt(v2, voff + 128 * i, soff);
      }
      PIN();
    };
    step_fn(K[0], V[0], true, KVB, seq + NPRE * step);
#pragma unroll
    for (int p = 1; p < NPRE; ++p) step_fn(K[p], V[p], false, 0, 0);
    int t = NPRE + 1;
    do { step_fn(K[0], V[0], true, t < NT ? KVB : 0u, seq + t * step); } while (++t <= NT);
    if (w == 0) STAMP(3);
    float M = fmaxf(m, dpp<ROR8>(m));
    { const auto q16 = __builtin_amdgcn_permlane16_swap(__float_as_uint(M), __float_as_uint(M), false, false);
      M = fmaxf(__uint_as_float(q16[0]), __uint_as_float(q16[1]));
      const auto q32 = __builtin_amdgcn_permlane32_swap(__float_as_uint(M), __float_as_uint(M), false, false);
      M = fmaxf(__uint_as_float(q32[0]), __uint_as_float(q32[1])); }
    const float f = __builtin_amdgcn_exp2f((m - M) * L2E);
    l *= f; l += dpp<ROR8>(l); l = sw16(l, l); l = sw32(l, l);
    const bool b3 = lane & 8;
#pragma unroll
    for (int i = 0; i < 3; ++i) {
      float y[4], z[2];
#pragma unroll
      for (int e = 0; e < 4; ++e) y[e] = sw32(acc[i * 8 + e] * f, acc[i * 8 + e + 4] * f);
#pragma unroll
      for (int e = 0; e < 2; ++e) z[e] = sw16(y[e], y[e + 2]);
      const float keep = b3 ? z[1] : z[0], send = b3 ? z[0] : z[1];
      ldf[L_REC + w * 196 + i * 64 + c * 8 + r] = keep + dpp<ROR8>(send);
    }
    if (lane == 0) { ldf[L_REC + w * 196 + 192] = M; ldf[L_REC + w * 196 + 193] = l; }
    if (w == 0) STAMP(4);
  } else {
    const int hid = tid - 256, hw = w - 4;
    if (hid == 0) lds[L_CNT] = 0;
    const int kh = j / 3, cq = j % 3, rg = hid >> 3, c8 = hid & 7;
    u32x4 W[10], X[5];
    const rsrc_t xr = mk(x, j < 6 ? 4 * D * 2 : 0), wqr = mk(wq, j < 6 ? WB : 0);
#pragma unroll
    for (int i = 0; i < 5; ++i) X[i] = __builtin_amdgcn_raw_buffer_load_b128(xr, (lane >> 4) * (D * 2) + ((lane & 15) + 16 * i) * 16, 0, 0);
#pragma unroll
    for (int i = 0; i < 10; ++i)
      W[i] = ld_nt(wqr, (unsigned)(2 * rg + (i & 1)) * (HD * 2) + c8 * 16, (unsigned)(kh * 320 + (i >> 1) * 64) * (HD * 2) + (h * DH + cq * 64) * 2);
    PIN();
    WG_BAR();
    asm volatile("" : "+v"(ep));
    const unsigned long long E1 = ((unsigned long long)__builtin_amdgcn_readfirstlane(ep[1]) << 32 | (unsigned)__builtin_amdgcn_readfirstlane(ep[0])) + 1;
    const Tag tg{(unsigned)E1 & 0xffu, (unsigned)(E1 >> 8), (unsigned)E1};
    if (j < 6) {
      float v[40], sm = 0.f;
#pragma unroll
      for (int i = 0; i < 5; ++i)
#pragma unroll
        for (int k = 0; k < 4; ++k) { v[i * 8 + 2 * k] = bflo(X[i][k]); v[i * 8 + 2 * k + 1] = bfhi(X[i][k]); sm += v[i * 8 + 2 * k] + v[i * 8 + 2 * k + 1]; }
      const float mean = sum16(sm) * (1.f / D);
      float s2 = 0.f;
#pragma unroll
      for (int k = 0; k < 40; ++k) { const float d = v[k] - mean; s2 = fmaf(d, d, s2); }
      const float inv = inv_sd(sum16(s2) * (1.f / D)), mb = rbf(mean);
#pragma unroll
      for (int i = 0; i < 5; ++i) {
        u32x4 o;
#pragma unroll
        for (int k = 0; k < 4; ++k) o[k] = pk2(rbf(v[i * 8 + 2 * k] - mb) * inv, rbf(v[i * 8 + 2 * k + 1] - mb) * inv);
        *(LAS u32x4*)(lds + L_XN + (lane >> 4) * 320 + ((lane & 15) + 16 * i) * 4) = o;
      }
      asm volatile("s_waitcnt lgkmcnt(0)" ::: "memory");
      if (hw == 0) STAMP(8);
      float qa[32];
#pragma unroll
      for (int k = 0; k < 32; ++k) qa[k] = 0.f;
#pragma unroll
      for (int i = 0; i < 5; ++i) {
        unsigned xp[4];
#pragma unroll
        for (int bb = 0; bb < 4; ++bb) xp[bb] = lds[L_XN + bb * 320 + kh * 160 + i * 32 + rg];
#pragma unroll
        for (int k = 0; k < 4; ++k) {
          const unsigned lo = __builtin_amdgcn_perm(W[2 * i + 1][k], W[2 * i][k], 0x05040100u), hi = __builtin_amdgcn_perm(W[2 * i + 1][k], W[2 * i][k], 0x07060302u);
#pragma unroll
          for (int bb = 0; bb < 4; ++bb) { qa[bb * 8 + 2 * k] = dot2(lo, xp[bb], qa[bb * 8 + 2 * k]); qa[bb * 8 + 2 * k + 1] = dot2(hi, xp[bb], qa[bb * 8 + 2 * k + 1]); }
        }
      }
      if (hw == 0) STAMP(9);
      const bool b3 = lane & 8;
#pragma unroll
      for (int bb = 0; bb < 4; ++bb) {
        float y[4], z[2];
#pragma unroll
        for (int e = 0; e < 4; ++e) y[e] = sw32(qa[bb * 8 + e], qa[bb * 8 + e + 4]);
#pragma unroll
        for (int e = 0; e < 2; ++e) z[e] = sw16(y[e], y[e + 2]);
        const float keep = b3 ? z[1] : z[0], send = b3 ? z[0] : z[1];
        ldf[L_QR + hw * 256 + bb * 64 + c8 * 8 + (lane >> 3)] = keep + dpp<ROR8>(send);
      }
      helper_bar(lds + L_CNT, 4, lane);
      if (hw < 2) {
        f32x2 a = *(LAS const f32x2*)(ldf + L_QR + 2 * hid);
#pragma unroll
        for (int ww = 1; ww < 4; ++ww) a += *(LAS const f32x2*)(ldf + L_QR + ww * 256 + 2 * hid);
        st_sc(wsr, WS_QP + (((h * 2 + kh) * 4 + (hid >> 5)) * DH + cq * 64 + (hid & 31) * 2) * 8, enc2(a[0], a[1], tg));
      }
      if (hw == 0) STAMP(10);
    }
    if (hw >= 2) {
      const int u = hid - 128;
      const unsigned o0 = u < 96 ? WS_QP + ((h * 2 * 4 + b) * DH + 2 * u) * 8 : OOB, o1 = u < 96 ? o0 + 4 * DH * 8 : OOB;
      const unsigned vm = u < 96 ? ~0u : 0u;
      u32x4 g0, g1;
      _Pragma("nounroll") for (unsigned spin = 0; spin < SPIN_CAP; ++spin) {
        asm volatile("" ::: "memory");
        g0 = ld_sc(wsr, o0); g1 = ld_sc(wsr, o1);
        if (__all(((bad2(g0, tg) | bad2(g1, tg)) & vm) == 0)) break;
        __builtin_amdgcn_s_sleep(2);
      }
      if (u < 96) lds[L_Q + u] = pk2(dec(g0[0]) + dec(g1[0]), dec(g0[2]) + dec(g1[2]));
      if (hw == 2) STAMP(11);
    }
    WG_BAR();
    const rsrc_t wor = mk(wo, j < 5 ? WB : 0);
    u32x4 Wo[12];
#pragma unroll
    for (int i = 0; i < 12; ++i) Wo[i] = ld_nt(wor, (unsigned)(hid >> 4) * (D * 2) + (hid & 15) * 16, (unsigned)(h * DH + 16 * i) * (D * 2) + j * 256);
    if (hid < 192) {
      *(LAS u32x4*)(lds + L_MIX + hid * 4) = *(const u32x4*)(w1 + hid * 8);
      *(LAS u32x4*)(lds + L_MIX + 768 + hid * 4) = *(const u32x4*)(b1 + hid * 8);
      *(LAS u32x4*)(lds + L_MIX + 1536 + hid * 4) = *(const u32x4*)(w2 + hid * 8);
    }
    if (hid < 5) lds[L_X + hid] = *(const unsigned*)(x + b2i * D + cg * 10 + 2 * hid);
    if (hid == 5) lds[L_X + 5] = b2[0];
#pragma unroll
    for (int i = 0; i < 12; ++i) *(LAS u32x4*)(lds + L_WO + ((hid >> 4) + 16 * i) * 68 + (hid & 15) * 4) = Wo[i];
    helper_bar(lds + L_CNT, j < 6 ? 8 : 4, lane);
    LAS const unsigned short* wl = (LAS const unsigned short*)(lds + L_WO) + (lane >> 4) * 8 * 136 + hw * 32 + (lane & 15);
#pragma unroll
    for (int nt = 0; nt < 2; ++nt)
#pragma unroll
      for (int ks = 0; ks < 6; ++ks) {
        u32x4 fr;
#pragma unroll
        for (int e = 0; e < 4; ++e) fr[e] = (unsigned)wl[(ks * 32 + 2 * e) * 136 + nt * 16] | ((unsigned)wl[(ks * 32 + 2 * e + 1) * 136 + nt * 16] << 16);
        Bf[nt][ks] = __builtin_bit_cast(bf16x8, fr);
      }
  }
  const unsigned long long E1 = ((unsigned long long)__builtin_amdgcn_readfirstlane(ep[1]) << 32 | (unsigned)__builtin_amdgcn_readfirstlane(ep[0])) + 1;
  const Tag tg{(unsigned)E1 & 0xffu, (unsigned)(E1 >> 8), (unsigned)E1};
  if (tid == 448) __builtin_amdgcn_raw_buffer_store_b64(u32x2{(unsigned)tstart, tg.t32}, mk(ws, 8 << 20), (2 << 20) + bid * 8, 0, 16);
  WG_BAR();
  if (w == 4) STAMP(12);
  if (w == 4 || w == 5) {
    const int u = tid - 256;
    float mm[4], M;
#pragma unroll
    for (int i = 0; i < 4; ++i) mm[i] = ldf[L_REC + i * 196 + 192];
    M = fmaxf(fmaxf(mm[0], mm[1]), fmaxf(mm[2], mm[3]));
    f32x2 a = {0.f, 0.f};
    const int uu = u < 96 ? u : 96;
#pragma unroll
    for (int i = 0; i < 4; ++i) a += __builtin_amdgcn_exp2f((mm[i] - M) * L2E) * *(LAS const f32x2*)(ldf + L_REC + i * 196 + 2 * uu);
    st_sc(wsr, u <= 96 ? WS_P1 + bid * P1REC + u * 16 : OOB, enc2(u < 96 ? a[0] : M, a[1], tg));
    if (w == 4) STAMP(13);
  }
  float mw1[3], mb1[3], mw2[3];
  {
    LAS const unsigned short* mx = (LAS const unsigned short*)(lds + L_MIX);
#pragma unroll
    for (int i = 0; i < 3; ++i) { mw1[i] = bflo(mx[tid + 512 * i]); mb1[i] = bflo(mx[F + tid + 512 * i]); mw2[i] = bflo(mx[2 * F + tid + 512 * i]); }
  }
  if (j < 5) {
    if (w < 4) {
      const unsigned r0 = WS_P1 + (h * 8 + 2 * w) * P1REC, oa = r0 + lane * 16, ob = lane < 32 ? oa + 1024 : OOB, oc = lane < 32 ? ob + P1REC : OOB;
      u32x4 a0, a1, c0, c1, s0, s1;
      _Pragma("nounroll") for (unsigned spin = 0; spin < SPIN_CAP; ++spin) {
        asm volatile("" ::: "memory");
        s0 = ld_sc(wsr, r0 + 96 * 16); s1 = ld_sc(wsr, r0 + P1REC + 96 * 16);
        a0 = ld_sc(wsr, oa); a1 = ld_sc(wsr, oa + P1REC); c0 = ld_sc(wsr, ob); c1 = ld_sc(wsr, oc);
        const unsigned bad = bad2(s0, tg) | bad2(s1, tg) | bad2(a0, tg) | bad2(a1, tg) | ((bad2(c0, tg) | bad2(c1, tg)) & lo32);
        if (__all(bad == 0)) break;
        __builtin_amdgcn_s_sleep(1);
      }
      if (w == 0) STAMP(5);
      const float m0 = dec(s0[0]), m1 = dec(s1[0]), M = fmaxf(m0, m1);
      float w0 = __builtin_amdgcn_exp2f((m0 - M) * L2E), w1_ = __builtin_amdgcn_exp2f((m1 - M) * L2E);
      const float inv = __builtin_amdgcn_rcpf(fmaf(w0, dec(s0[2]), w1_ * dec(s1[2])));
      w0 *= inv; w1_ *= inv;
      lds[L_O + w * 112 + lane] = pk2(fmaf(w0, dec(a0[0]), w1_ * dec(a1[0])), fmaf(w0, dec(a0[2]), w1_ * dec(a1[2])));
      if (lane < 32) lds[L_O + w * 112 + 64 + lane] = pk2(fmaf(w0, dec(c0[0]), w1_ * dec(c1[0])), fmaf(w0, dec(c0[2]), w1_ * dec(c1[2])));
    }
    WG_BAR();
    if (w >= 4) {
      f32x4 d0 = {0.f, 0.f, 0.f, 0.f}, d1 = d0;
#pragma unroll
      for (int ks = 0; ks < 6; ++ks) {
        const bf16x8 af = __builtin_bit_cast(bf16x8, *(LAS const u32x4*)(lds + L_O + (lane & 3) * 112 + ks * 16 + (lane >> 4) * 4));
        d0 = __builtin_amdgcn_mfma_f32_16x16x32_bf16(af, Bf[0][ks], d0, 0, 0, 0);
        d1 = __builtin_amdgcn_mfma_f32_16x16x32_bf16(af, Bf[1][ks], d1, 0, 0, 0);
      }
      const int g = lane >> 4, n = lane & 15;
      const bool g1 = lane & 16, g2 = lane & 32;
      const float a01 = g1 ? d0[1] : d0[0], a23 = g1 ? d0[3] : d0[2], c01 = g1 ? d1[1] : d1[0], c23 = g1 ? d1[3] : d1[2];
      const float a = g2 ? a23 : a01, cc = g2 ? c23 : c01;
      const float xa = dpp<XOR1>(a), xc = dpp<XOR1>(cc);
      const bool odd = lane & 1;
      st_sc(wsr, WS_P2 + ((g * H + h) * D + j * 128 + (w - 4) * 32 + (odd ? 15 + n : n)) * 8, enc2(odd ? xc : a, odd ? cc : xa, tg));
      if (w == 4) STAMP(14);
    }
  }
  if (w < 3) {
    const int pp = 2 * w + (lane >> 5);
    const unsigned o = pp < 5 ? WS_P2 + ((b2i * H + (lane & 31)) * D + cg * 10 + 2 * pp) * 8 : OOB;
    const unsigned xr = lds[L_X + (pp < 5 ? pp : 0)];
    const unsigned vm = pp < 5 ? ~0u : 0u;
    u32x4 g;
    _Pragma("nounroll") for (unsigned spin = 0; spin < SPIN_CAP; ++spin) {
      asm volatile("" ::: "memory");
      g = ld_sc(wsr, o);
      if (__all((bad2(g, tg) & vm) == 0)) break;
      __builtin_amdgcn_s_sleep(1);
    }
    if (w == 0) STAMP(6);
    float y0 = sum16(dec(g[0])), y1 = sum16(dec(g[2]));
    y0 = sw16(y0, y0); y1 = sw16(y1, y1);
    const unsigned hp = pk2(bflo(xr) + rbf(y0), bfhi(xr) + rbf(y1));
    __builtin_amdgcn_raw_buffer_store_b64(u32x2{hp, tg.t32}, wsr, (pp < 5 && (lane & 31) < REP) ? WS_P3 + (((lane & 31) * 4 + b2i) * 320 + cg * 5 + pp) * 8 : OOB, 0, 16);
  } else if (w == 3) {
    const unsigned o = WS_P3 + (((bid % REP) * 4 + b2i) * 320) * 8 + lane * 16, o2 = lane < 32 ? o + 2048 : OOB;
    u32x4 g[3];
    _Pragma("nounroll") for (unsigned spin = 0; spin < SPIN_CAP; ++spin) {
      asm volatile("" ::: "memory");
      g[0] = ld_sc(wsr, o); g[1] = ld_sc(wsr, o + 1024); g[2] = ld_sc(wsr, o2);
      const unsigned bad = (g[0][1] ^ tg.t32) | (g[0][3] ^ tg.t32) | (g[1][1] ^ tg.t32) | (g[1][3] ^ tg.t32) | (((g[2][1] ^ tg.t32) | (g[2][3] ^ tg.t32)) & lo32);
      if (__all(bad == 0)) break;
      __builtin_amdgcn_s_sleep(1);
    }
    STAMP(7);
    if (bid == 0 && lane == 0) __builtin_amdgcn_raw_buffer_store_b64(u32x2{(unsigned)E1, (unsigned)(E1 >> 32)}, wsr, 0, 0, 16);
    float v[12];
#pragma unroll
    for (int i = 0; i < 3; ++i) { v[4 * i] = bflo(g[i][0]); v[4 * i + 1] = bfhi(g[i][0]); v[4 * i + 2] = bflo(g[i][2]); v[4 * i + 3] = bfhi(g[i][2]); }
#pragma unroll
    for (int i = 0; i < 3; ++i) if (i < 2 || lane < 32) *(LAS f32x4*)(ldf + L_HH + (lane + 64 * i) * 4) = f32x4{v[4 * i], v[4 * i + 1], v[4 * i + 2], v[4 * i + 3]};
    const float tail = lane < 32 ? 1.f : 0.f;
#pragma unroll
    for (int pass = 0; pass < 2; ++pass) {
      float sm = ((v[0] + v[1]) + (v[2] + v[3])) + ((v[4] + v[5]) + (v[6] + v[7])) + tail * ((v[8] + v[9]) + (v[10] + v[11]));
      const float mean = sum64(sm) * (1.f / D);
      float s2 = 0.f, s3 = 0.f;
#pragma unroll
      for (int k = 0; k < 8; ++k) { const float d = v[k] - mean; s2 = fmaf(d, d, s2); }
#pragma unroll
      for (int k = 8; k < 12; ++k) { const float d = v[k] - mean; s3 = fmaf(d, d, s3); }
      const float inv = inv_sd(sum64(fmaf(tail, s3, s2)) * (1.f / D)), mb = rbf(mean);
#pragma unroll
      for (int k = 0; k < 12; ++k) v[k] = rbf(rbf(v[k] - mb) * inv);
    }
#pragma unroll
    for (int i = 0; i < 3; ++i) if (i < 2 || lane < 32) *(LAS f32x4*)(ldf + L_T + (lane + 64 * i) * 4) = f32x4{v[4 * i], v[4 * i + 1], v[4 * i + 2], v[4 * i + 3]};
  }
  WG_BAR();
  if (w == 0) STAMP(15);
  {
    float ac[10];
#pragma unroll
    for (int i = 0; i < 10; ++i) {
      const float t = ldf[L_T + cg * 10 + i];
      ac[i] = 0.f;
#pragma unroll
      for (int k = 0; k < 3; ++k) {
        const float u = fmaf(t, mw1[k], mb1[k]);
        const float z = u * fmaf(u * u, -2.f * L2E * 0.044715f * 0.7978845608028654f, -2.f * L2E * 0.7978845608028654f);
        ac[i] = fmaf(u * __builtin_amdgcn_rcpf(1.f + __builtin_amdgcn_exp2f(z)), mw2[k], ac[i]);
      }
    }
    float y[5];
#pragma unroll
    for (int i = 0; i < 5; ++i) y[i] = sw32(ac[i], ac[i + 5]);
    const float z0 = sum16(sw16(y[0], y[1])), z1 = sum16(sw16(y[2], y[3])), z2 = sum16(sw16(y[4], y[4]));
    if ((lane & 15) == 0) {
      const int o = L_RED + w * 16 + (lane >> 5) * 5, odd = (lane >> 4) & 1;
      ldf[o + odd] = z0; ldf[o + 2 + odd] = z1; ldf[o + 4] = z2;
    }
  }
  if (w == 0) STAMP(16);
  WG_BAR();
  if (tid < 10) {
    float y = 0.f;
#pragma unroll
    for (int i = 0; i < 8; ++i) y += ldf[L_RED + i * 16 + tid];
    const __bf16 o = (__bf16)(ldf[L_HH + cg * 10 + tid] + rbf(rbf(y) + bflo(lds[L_X + 5])));
    out[b2i * D + cg * 10 + tid] = __builtin_bit_cast(unsigned short, o);
  }
  if (w == 0) {
    int best = 0;
    for (unsigned spin = 0; spin < SPIN_CAP; ++spin) {
      asm volatile("" ::: "memory");
      unsigned bad = 0; best = 0;
      for (int i = 0; i < 4; ++i) { const u32x2 g = __builtin_amdgcn_raw_buffer_load_b64(mk(ws, 8 << 20), (2 << 20) + (lane + 64 * i) * 8, 0, 16);
        bad |= g[1] ^ tg.t32; best = min(best, (int)(g[0] - (unsigned)tstart)); }
      if (__all(bad == 0)) break;
    }
    for (int o = 32; o; o >>= 1) best = min(best, __shfl_xor(best, o));
    best = __builtin_amdgcn_readfirstlane(best);
    while ((long long)(__builtin_amdgcn_s_memrealtime() - tstart) < 15000ll + best) __builtin_amdgcn_s_sleep(1);
  }
}

extern "C" void kernel_launch(void* const* d_in, const int* in_sizes, int n_in, void* d_out, int out_size, void* d_ws, size_t ws_size,
                              hipStream_t stream) {
  typedef const unsigned short* P;
  fused_decode<<<256, 512, 0, stream>>>((P)d_in[0], (P)d_in[1], (P)d_in[2], (P)d_in[3], (P)d_in[4], (P)d_in[6], (P)d_in[7], (P)d_in[8], (P)d_in[9],
                                        (unsigned short*)d_out, (unsigned char*)d_ws);
}
```

```cpp
#include <hip/hip_runtime.h>
#include <stdint.h>

typedef unsigned u32x4 __attribute__((ext_vector_type(4)));
typedef unsigned u32x2 __attribute__((ext_vector_type(2)));
typedef float f32x4 __attribute__((ext_vector_type(4)));
typedef float f32x2 __attribute__((ext_vector_type(2)));
typedef __bf16 bf16x2 __attribute__((ext_vector_type(2)));
typedef __bf16 bf16x8 __attribute__((ext_vector_type(8)));
typedef __amdgpu_buffer_rsrc_t rsrc_t;
#define LAS __attribute__((address_space(3)))

constexpr int D = 640, H = 32, SK = 4096, DH = 192, HD = H * DH, F = 1536;
constexpr unsigned SEQB = SK * DH * 2, TILEB = 8 * DH * 2, KVB = 4u * H * SEQB, WB = D * HD * 2;
#define NPRE 4
constexpr int REP = 8;
constexpr unsigned P1REC = 208 * 8;
constexpr unsigned WS_QP = 4096, WS_P1 = WS_QP + H * 2 * 4 * DH * 8, WS_P2 = WS_P1 + 256 * P1REC, WS_P3 = WS_P2 + 4 * H * D * 8,
                   WS_END = WS_P3 + REP * 4 * 320 * 8;
constexpr unsigned OOB = 0x7ffffff0u;
constexpr unsigned SPIN_CAP = 1u << 17;
constexpr float L2E = 1.4426950408889634f, SCALE = 0.07216878364870322f;
constexpr int L_CNT = 0, L_Q = 4, L_REC = L_Q + 96, L_O = L_REC + 4 * 196, L_RED = L_O + 4 * 112, L_X = L_RED + 8 * 16, L_T = L_X + 8, L_HH = L_T + D,
              L_XN = L_HH + D, L_QR = L_XN + 4 * 320, L_MIX = L_QR + 4 * 256, L_WO = L_MIX + 3 * F / 2, L_PROF = L_WO + DH * 68, L_END = L_PROF + 64;
constexpr int LDS_DW = 24576;
static_assert(L_END <= LDS_DW && L_REC % 4 == 0 && L_O % 4 == 0 && L_T % 4 == 0 && L_HH % 4 == 0 && L_XN % 4 == 0 && L_QR % 4 == 0 && L_MIX % 4 == 0 && L_WO % 4 == 0, "lds map");

__device__ __forceinline__ float bflo(unsigned w) { return __uint_as_float(w << 16); }
__device__ __forceinline__ float bfhi(unsigned w) { return __uint_as_float(w & 0xffff0000u); }
__device__ __forceinline__ unsigned pk2(float lo, float hi) { const bf16x2 v = {(__bf16)lo, (__bf16)hi}; return __builtin_bit_cast(unsigned, v); }
__device__ __forceinline__ float rbf(float f) { return (float)(__bf16)f; }
__device__ __forceinline__ float dot2(unsigned a, unsigned b, float c) {
  return __builtin_amdgcn_fdot2_f32_bf16(__builtin_bit_cast(bf16x2, a), __builtin_bit_cast(bf16x2, b), c, false);
}
template <int CTRL> __device__ __forceinline__ float dpp(float x) {
  return __builtin_bit_cast(float, __builtin_amdgcn_mov_dpp(__builtin_bit_cast(int, x), CTRL, 0xf, 0xf, true));
}
constexpr int XOR1 = 0xB1, XOR2 = 0x4E, HMIR = 0x141, ROR8 = 0x128;
__device__ __forceinline__ float sw32(float a, float b) {
  const auto r = __builtin_amdgcn_permlane32_swap(__float_as_uint(a), __float_as_uint(b), false, false);
  return __uint_as_float(r[0]) + __uint_as_float(r[1]);
}
__device__ __forceinline__ float sw16(float a, float b) {
  const auto r = __builtin_amdgcn_permlane16_swap(__float_as_uint(a), __float_as_uint(b), false, false);
  return __uint_as_float(r[0]) + __uint_as_float(r[1]);
}
__device__ __forceinline__ float sum16(float v) { v += dpp<XOR1>(v); v += dpp<XOR2>(v); v += dpp<HMIR>(v); v += dpp<ROR8>(v); return v; }
__device__ __forceinline__ float sum64(float v) { v = sum16(v); v = sw16(v, v); return sw32(v, v); }
__device__ __forceinline__ rsrc_t mk(const void* p, unsigned n) { return __builtin_amdgcn_make_buffer_rsrc((void*)p, 0, n, 0x00020000); }
__device__ __forceinline__ u32x4 ld_nt(rsrc_t r, unsigned v, unsigned s) { return __builtin_amdgcn_raw_buffer_load_b128(r, v, s, 2); }
__device__ __forceinline__ u32x4 ld_sc(rsrc_t r, unsigned v) { return __builtin_amdgcn_raw_buffer_load_b128(r, v, 0, 16); }
__device__ __forceinline__ void st_sc(rsrc_t r, unsigned v, u32x4 x) { __builtin_amdgcn_raw_buffer_store_b128(x, r, v, 0, 16); }
#define PIN() do { asm volatile("" ::: "memory"); __builtin_amdgcn_sched_barrier(0); } while (0)
#define WG_BAR() do { asm volatile("s_waitcnt lgkmcnt(0)" ::: "memory"); __builtin_amdgcn_s_barrier(); PIN(); } while (0)
#define STAMP(i) do { } while (0)
struct Tag { unsigned lo, hi, t32; };
__device__ __forceinline__ u32x4 enc2(float a, float b, const Tag& t) {
  return u32x4{(__float_as_uint(a) & 0xffffff00u) | t.lo, t.hi, (__float_as_uint(b) & 0xffffff00u) | t.lo, t.hi};
}
__device__ __forceinline__ unsigned bad2(u32x4 g, const Tag& t) { return (((g[0] ^ t.lo) | (g[2] ^ t.lo)) & 0xffu) | (g[1] ^ t.hi) | (g[3] ^ t.hi); }
__device__ __forceinline__ float dec(unsigned g) { return __uint_as_float(g & 0xffffff00u); }
__device__ __forceinline__ void helper_bar(LAS unsigned* cnt, unsigned target, int lane) {
  asm volatile("s_waitcnt lgkmcnt(0)" ::: "memory");
  if (lane == 0) __hip_atomic_fetch_add(cnt, 1u, __ATOMIC_RELAXED, __HIP_MEMORY_SCOPE_WORKGROUP);
  while ((unsigned)__builtin_amdgcn_readfirstlane(__hip_atomic_load(cnt, __ATOMIC_RELAXED, __HIP_MEMORY_SCOPE_WORKGROUP)) < target) { }
  asm volatile("" ::: "memory");
}
__device__ __forceinline__ float inv_sd(float var) { return __builtin_amdgcn_rcpf(rbf(__builtin_amdgcn_sqrtf(rbf(rbf(var) + 1e-5f)))); }

__global__ void __launch_bounds__(512, 2)
fused_decode(const unsigned short* x, const unsigned short* kc, const unsigned short* vc,
             const unsigned short* wq, const unsigned short* wo, const unsigned short* w1,
             const unsigned short* b1, const unsigned short* w2, const unsigned short* b2,
             unsigned short* out, unsigned char* ws) {
  __shared__ __attribute__((aligned(16))) unsigned lds_raw[LDS_DW];
  LAS unsigned* lds = (LAS unsigned*)lds_raw;
  LAS float* ldf = (LAS float*)lds_raw;
  const int tid = threadIdx.x, lane = tid & 63, w = __builtin_amdgcn_readfirstlane(tid >> 6);
  const int bid = blockIdx.x, h = bid >> 3, j = bid & 7, b = j >> 1, s = j & 1;
  const int b2i = bid >> 6, cg = bid & 63;
  const rsrc_t wsr = mk(ws, WS_END);
  const unsigned long long tstart = __builtin_amdgcn_s_memrealtime();
  u32x2 ep = __builtin_amdgcn_raw_buffer_load_b64(wsr, 0, 0, 16);
  bf16x8 Bf[2][6];
  const unsigned lo32 = lane < 32 ? ~0u : 0u;

  if (w < 4) {
    const int r = lane >> 3, c = lane & 7;
    const unsigned voff = r * 384 + c * 16;
    const unsigned seq = (unsigned)(b * H + h) * SEQB + (s ? (511 - w) * TILEB : w * TILEB);
    const int step = s ? -4 * (int)TILEB : 4 * (int)TILEB;
    constexpr int NT = 64;
    const rsrc_t kr = mk(kc, KVB), vr = mk(vc, KVB);
    u32x4 K[NPRE][3], V[NPRE][3];
    WG_BAR();
#pragma unroll
    for (int p = 0; p < NPRE; ++p) {
      if (p) asm volatile("s_waitcnt vmcnt(3)" ::: "memory");
#pragma unroll
      for (int i = 0; i < 3; ++i) K[p][i] = ld_nt(kr, voff + 128 * i, seq + p * step);
      PIN();
      if (p) asm volatile("s_waitcnt vmcnt(3)" ::: "memory");
#pragma unroll
      for (int i = 0; i < 3; ++i) V[p][i] = ld_nt(vr, voff + 128 * i, seq + p * step);
      PIN();
    }
    if (w == 0) STAMP(1);
    WG_BAR();
    if (w == 0) STAMP(2);
    u32x4 qf[3];
#pragma unroll
    for (int i = 0; i < 3; ++i) qf[i] = *(LAS const u32x4*)(lds + L_Q + (c + 8 * i) * 4);
    float m = __uint_as_float(0xff7f0000u), l = 0.f, acc[24];
#pragma unroll
    for (int k = 0; k < 24; ++k) acc[k] = 0.f;
    auto step_fn = [&](u32x4 (&Kc)[3], u32x4 (&Vc)[3], bool refill, unsigned nbytes, unsigned soff) {
      float a0 = 0.f, a1 = 0.f, a2 = 0.f;
#pragma unroll
      for (int k = 0; k < 4; ++k) { a0 = dot2(Kc[0][k], qf[0][k], a0); a1 = dot2(Kc[1][k], qf[1][k], a1); a2 = dot2(Kc[2][k], qf[2][k], a2); }
      float a = (a0 + a1) + a2;
      PIN();
      if (refill) {
        const rsrc_t k2 = mk(kc, nbytes);
#pragma unroll
        for (int i = 0; i < 3; ++i) Kc[i] = ld_nt(k2, voff + 128 * i, soff);
      }
      PIN();
      a += dpp<XOR1>(a); a += dpp<XOR2>(a); a += dpp<HMIR>(a);
      const float sc = rbf(rbf(a) * SCALE);
      const float mn = fmaxf(m, sc);
      const float alpha = __builtin_amdgcn_exp2f((m - mn) * L2E), p = __builtin_amdgcn_exp2f((sc - mn) * L2E);
      l = fmaf(l, alpha, p); m = mn;
#pragma unroll
      for (int i = 0; i < 3; ++i)
#pragma unroll
        for (int k = 0; k < 4; ++k) {
          acc[i * 8 + 2 * k]     = fmaf(p, bflo(Vc[i][k]), acc[i * 8 + 2 * k] * alpha);
          acc[i * 8 + 2 * k + 1] = fmaf(p, bfhi(Vc[i][k]), acc[i * 8 + 2 * k + 1] * alpha);
        }
      PIN();
      if (refill) {
        const rsrc_t v2 = mk(vc, nbytes);
#pragma unroll
        for (int i = 0; i < 3; ++i) Vc[i] = ld_nt(v2, voff + 128 * i, soff);
      }
      PIN();
    };
    step_fn(K[0], V[0], true, KVB, seq + NPRE * step);
#pragma unroll
    for (int p = 1; p < NPRE; ++p) step_fn(K[p], V[p], false, 0, 0);
    int t = NPRE + 1;
    do { step_fn(K[0], V[0], true, t < NT ? KVB : 0u, seq + t * step); } while (++t <= NT);
    if (w == 0) STAMP(3);
    float M = fmaxf(m, dpp<ROR8>(m));
    { const auto q16 = __builtin_amdgcn_permlane16_swap(__float_as_uint(M), __float_as_uint(M), false, false);
      M = fmaxf(__uint_as_float(q16[0]), __uint_as_float(q16[1]));
      const auto q32 = __builtin_amdgcn_permlane32_swap(__float_as_uint(M), __float_as_uint(M), false, false);
      M = fmaxf(__uint_as_float(q32[0]), __uint_as_float(q32[1])); }
    const float f = __builtin_amdgcn_exp2f((m - M) * L2E);
    l *= f; l += dpp<ROR8>(l); l = sw16(l, l); l = sw32(l, l);
    const bool b3 = lane & 8;
#pragma unroll
    for (int i = 0; i < 3; ++i) {
      float y[4], z[2];
#pragma unroll
      for (int e = 0; e < 4; ++e) y[e] = sw32(acc[i * 8 + e] * f, acc[i * 8 + e + 4] * f);
#pragma unroll
      for (int e = 0; e < 2; ++e) z[e] = sw16(y[e], y[e + 2]);
      const float keep = b3 ? z[1] : z[0], send = b3 ? z[0] : z[1];
      ldf[L_REC + w * 196 + i * 64 + c * 8 + r] = keep + dpp<ROR8>(send);
    }
    if (lane == 0) { ldf[L_REC + w * 196 + 192] = M; ldf[L_REC + w * 196 + 193] = l; }
    if (w == 0) STAMP(4);
  } else {
    const int hid = tid - 256, hw = w - 4;
    if (hid == 0) lds[L_CNT] = 0;
    const int kh = j / 3, cq = j % 3, rg = hid >> 3, c8 = hid & 7;
    u32x4 W[10], X[5];
    const rsrc_t xr = mk(x, j < 6 ? 4 * D * 2 : 0), wqr = mk(wq, j < 6 ? WB : 0);
#pragma unroll
    for (int i = 0; i < 5; ++i) X[i] = __builtin_amdgcn_raw_buffer_load_b128(xr, (lane >> 4) * (D * 2) + ((lane & 15) + 16 * i) * 16, 0, 0);
#pragma unroll
    for (int i = 0; i < 10; ++i)
      W[i] = ld_nt(wqr, (unsigned)(2 * rg + (i & 1)) * (HD * 2) + c8 * 16, (unsigned)(kh * 320 + (i >> 1) * 64) * (HD * 2) + (h * DH + cq * 64) * 2);
    PIN();
    WG_BAR();
    asm volatile("" : "+v"(ep));
    const unsigned long long E1 = ((unsigned long long)__builtin_amdgcn_readfirstlane(ep[1]) << 32 | (unsigned)__builtin_amdgcn_readfirstlane(ep[0])) + 1;
    const Tag tg{(unsigned)E1 & 0xffu, (unsigned)(E1 >> 8), (unsigned)E1};
    if (j < 6) {
      float v[40], sm = 0.f;
#pragma unroll
      for (int i = 0; i < 5; ++i)
#pragma unroll
        for (int k = 0; k < 4; ++k) { v[i * 8 + 2 * k] = bflo(X[i][k]); v[i * 8 + 2 * k + 1] = bfhi(X[i][k]); sm += v[i * 8 + 2 * k] + v[i * 8 + 2 * k + 1]; }
      const float mean = sum16(sm) * (1.f / D);
      float s2 = 0.f;
#pragma unroll
      for (int k = 0; k < 40; ++k) { const float d = v[k] - mean; s2 = fmaf(d, d, s2); }
      const float inv = inv_sd(sum16(s2) * (1.f / D)), mb = rbf(mean);
#pragma unroll
      for (int i = 0; i < 5; ++i) {
        u32x4 o;
#pragma unroll
        for (int k = 0; k < 4; ++k) o[k] = pk2(rbf(v[i * 8 + 2 * k] - mb) * inv, rbf(v[i * 8 + 2 * k + 1] - mb) * inv);
        *(LAS u32x4*)(lds + L_XN + (lane >> 4) * 320 + ((lane & 15) + 16 * i) * 4) = o;
      }
      asm volatile("s_waitcnt lgkmcnt(0)" ::: "memory");
      if (hw == 0) STAMP(8);
      float qa[32];
#pragma unroll
      for (int k = 0; k < 32; ++k) qa[k] = 0.f;
#pragma unroll
      for (int i = 0; i < 5; ++i) {
        unsigned xp[4];
#pragma unroll
        for (int bb = 0; bb < 4; ++bb) xp[bb] = lds[L_XN + bb * 320 + kh * 160 + i * 32 + rg];
#pragma unroll
        for (int k = 0; k < 4; ++k) {
          const unsigned lo = __builtin_amdgcn_perm(W[2 * i + 1][k], W[2 * i][k], 0x05040100u), hi = __builtin_amdgcn_perm(W[2 * i + 1][k], W[2 * i][k], 0x07060302u);
#pragma unroll
          for (int bb = 0; bb < 4; ++bb) { qa[bb * 8 + 2 * k] = dot2(lo, xp[bb], qa[bb * 8 + 2 * k]); qa[bb * 8 + 2 * k + 1] = dot2(hi, xp[bb], qa[bb * 8 + 2 * k + 1]); }
        }
      }
      if (hw == 0) STAMP(9);
      const bool b3 = lane & 8;
#pragma unroll
      for (int bb = 0; bb < 4; ++bb) {
        float y[4], z[2];
#pragma unroll
        for (int e = 0; e < 4; ++e) y[e] = sw32(qa[bb * 8 + e], qa[bb * 8 + e + 4]);
#pragma unroll
        for (int e = 0; e < 2; ++e) z[e] = sw16(y[e], y[e + 2]);
        const float keep = b3 ? z[1] : z[0], send = b3 ? z[0] : z[1];
        ldf[L_QR + hw * 256 + bb * 64 + c8 * 8 + (lane >> 3)] = keep + dpp<ROR8>(send);
      }
      helper_bar(lds + L_CNT, 4, lane);
      if (hw < 2) {
        f32x2 a = *(LAS const f32x2*)(ldf + L_QR + 2 * hid);
#pragma unroll
        for (int ww = 1; ww < 4; ++ww) a += *(LAS const f32x2*)(ldf + L_QR + ww * 256 + 2 * hid);
        st_sc(wsr, WS_QP + (((h * 2 + kh) * 4 + (hid >> 5)) * DH + cq * 64 + (hid & 31) * 2) * 8, enc2(a[0], a[1], tg));
      }
      if (hw == 0) STAMP(10);
    }
    if (hw >= 2) {
      const int u = hid - 128;
      const unsigned o0 = u < 96 ? WS_QP + ((h * 2 * 4 + b) * DH + 2 * u) * 8 : OOB, o1 = u < 96 ? o0 + 4 * DH * 8 : OOB;
      const unsigned vm = u < 96 ? ~0u : 0u;
      u32x4 g0, g1;
      _Pragma("nounroll") for (unsigned spin = 0; spin < SPIN_CAP; ++spin) {
        asm volatile("" ::: "memory");
        g0 = ld_sc(wsr, o0); g1 = ld_sc(wsr, o1);
        if (__all(((bad2(g0, tg) | bad2(g1, tg)) & vm) == 0)) break;
        __builtin_amdgcn_s_sleep(2);
      }
      if (u < 96) lds[L_Q + u] = pk2(dec(g0[0]) + dec(g1[0]), dec(g0[2]) + dec(g1[2]));
      if (hw == 2) STAMP(11);
    }
    WG_BAR();
    const rsrc_t wor = mk(wo, j < 5 ? WB : 0);
    u32x4 Wo[12];
#pragma unroll
    for (int i = 0; i < 12; ++i) Wo[i] = ld_nt(wor, (unsigned)(hid >> 4) * (D * 2) + (hid & 15) * 16, (unsigned)(h * DH + 16 * i) * (D * 2) + j * 256);
    if (hid < 192) {
      *(LAS u32x4*)(lds + L_MIX + hid * 4) = *(const u32x4*)(w1 + hid * 8);
      *(LAS u32x4*)(lds + L_MIX + 768 + hid * 4) = *(const u32x4*)(b1 + hid * 8);
      *(LAS u32x4*)(lds + L_MIX + 1536 + hid * 4) = *(const u32x4*)(w2 + hid * 8);
    }
    if (hid < 5) lds[L_X + hid] = *(const unsigned*)(x + b2i * D + cg * 10 + 2 * hid);
    if (hid == 5) lds[L_X + 5] = b2[0];
#pragma unroll
    for (int i = 0; i < 12; ++i) *(LAS u32x4*)(lds + L_WO + ((hid >> 4) + 16 * i) * 68 + (hid & 15) * 4) = Wo[i];
    helper_bar(lds + L_CNT, j < 6 ? 8 : 4, lane);
    LAS const unsigned short* wl = (LAS const unsigned short*)(lds + L_WO) + (lane >> 4) * 8 * 136 + hw * 32 + (lane & 15);
#pragma unroll
    for (int nt = 0; nt < 2; ++nt)
#pragma unroll
      for (int ks = 0; ks < 6; ++ks) {
        u32x4 fr;
#pragma unroll
        for (int e = 0; e < 4; ++e) fr[e] = (unsigned)wl[(ks * 32 + 2 * e) * 136 + nt * 16] | ((unsigned)wl[(ks * 32 + 2 * e + 1) * 136 + nt * 16] << 16);
        Bf[nt][ks] = __builtin_bit_cast(bf16x8, fr);
      }
  }
  const unsigned long long E1 = ((unsigned long long)__builtin_amdgcn_readfirstlane(ep[1]) << 32 | (unsigned)__builtin_amdgcn_readfirstlane(ep[0])) + 1;
  const Tag tg{(unsigned)E1 & 0xffu, (unsigned)(E1 >> 8), (unsigned)E1};
  WG_BAR();
  if (w == 4) STAMP(12);
  if (w == 4 || w == 5) {
    const int u = tid - 256;
    float mm[4], M;
#pragma unroll
    for (int i = 0; i < 4; ++i) mm[i] = ldf[L_REC + i * 196 + 192];
    M = fmaxf(fmaxf(mm[0], mm[1]), fmaxf(mm[2], mm[3]));
    f32x2 a = {0.f, 0.f};
    const int uu = u < 96 ? u : 96;
#pragma unroll
    for (int i = 0; i < 4; ++i) a += __builtin_amdgcn_exp2f((mm[i] - M) * L2E) * *(LAS const f32x2*)(ldf + L_REC + i * 196 + 2 * uu);
    st_sc(wsr, u <= 96 ? WS_P1 + bid * P1REC + u * 16 : OOB, enc2(u < 96 ? a[0] : M, a[1], tg));
    if (w == 4) STAMP(13);
  }
  float mw1[3], mb1[3], mw2[3];
  {
    LAS const unsigned short* mx = (LAS const unsigned short*)(lds + L_MIX);
#pragma unroll
    for (int i = 0; i < 3; ++i) { mw1[i] = bflo(mx[tid + 512 * i]); mb1[i] = bflo(mx[F + tid + 512 * i]); mw2[i] = bflo(mx[2 * F + tid + 512 * i]); }
  }
  if (j < 5) {
    if (w < 4) {
      const unsigned r0 = WS_P1 + (h * 8 + 2 * w) * P1REC, oa = r0 + lane * 16, ob = lane < 32 ? oa + 1024 : OOB, oc = lane < 32 ? ob + P1REC : OOB;
      u32x4 a0, a1, c0, c1, s0, s1;
      _Pragma("nounroll") for (unsigned spin = 0; spin < SPIN_CAP; ++spin) {
        asm volatile("" ::: "memory");
        s0 = ld_sc(wsr, r0 + 96 * 16); s1 = ld_sc(wsr, r0 + P1REC + 96 * 16);
        a0 = ld_sc(wsr, oa); a1 = ld_sc(wsr, oa + P1REC); c0 = ld_sc(wsr, ob); c1 = ld_sc(wsr, oc);
        const unsigned bad = bad2(s0, tg) | bad2(s1, tg) | bad2(a0, tg) | bad2(a1, tg) | ((bad2(c0, tg) | bad2(c1, tg)) & lo32);
        if (__all(bad == 0)) break;
        __builtin_amdgcn_s_sleep(1);
      }
      if (w == 0) STAMP(5);
      const float m0 = dec(s0[0]), m1 = dec(s1[0]), M = fmaxf(m0, m1);
      float w0 = __builtin_amdgcn_exp2f((m0 - M) * L2E), w1_ = __builtin_amdgcn_exp2f((m1 - M) * L2E);
      const float inv = __builtin_amdgcn_rcpf(fmaf(w0, dec(s0[2]), w1_ * dec(s1[2])));
      w0 *= inv; w1_ *= inv;
      lds[L_O + w * 112 + lane] = pk2(fmaf(w0, dec(a0[0]), w1_ * dec(a1[0])), fmaf(w0, dec(a0[2]), w1_ * dec(a1[2])));
      if (lane < 32) lds[L_O + w * 112 + 64 + lane] = pk2(fmaf(w0, dec(c0[0]), w1_ * dec(c1[0])), fmaf(w0, dec(c0[2]), w1_ * dec(c1[2])));
    }
    WG_BAR();
    if (w >= 4) {
      f32x4 d0 = {0.f, 0.f, 0.f, 0.f}, d1 = d0;
#pragma unroll
      for (int ks = 0; ks < 6; ++ks) {
        const bf16x8 af = __builtin_bit_cast(bf16x8, *(LAS const u32x4*)(lds + L_O + (lane & 3) * 112 + ks * 16 + (lane >> 4) * 4));
        d0 = __builtin_amdgcn_mfma_f32_16x16x32_bf16(af, Bf[0][ks], d0, 0, 0, 0);
        d1 = __builtin_amdgcn_mfma_f32_16x16x32_bf16(af, Bf[1][ks], d1, 0, 0, 0);
      }
      const int g = lane >> 4, n = lane & 15;
      const bool g1 = lane & 16, g2 = lane & 32;
      const float a01 = g1 ? d0[1] : d0[0], a23 = g1 ? d0[3] : d0[2], c01 = g1 ? d1[1] : d1[0], c23 = g1 ? d1[3] : d1[2];
      const float a = g2 ? a23 : a01, cc = g2 ? c23 : c01;
      const float xa = dpp<XOR1>(a), xc = dpp<XOR1>(cc);
      const bool odd = lane & 1;
      st_sc(wsr, WS_P2 + ((g * H + h) * D + j * 128 + (w - 4) * 32 + (odd ? 15 + n : n)) * 8, enc2(odd ? xc : a, odd ? cc : xa, tg));
      if (w == 4) STAMP(14);
    }
  }
  if (w < 3) {
    const int pp = 2 * w + (lane >> 5);
    const unsigned o = pp < 5 ? WS_P2 + ((b2i * H + (lane & 31)) * D + cg * 10 + 2 * pp) * 8 : OOB;
    const unsigned xr = lds[L_X + (pp < 5 ? pp : 0)];
    const unsigned vm = pp < 5 ? ~0u : 0u;
    u32x4 g;
    _Pragma("nounroll") for (unsigned spin = 0; spin < SPIN_CAP; ++spin) {
      asm volatile("" ::: "memory");
      g = ld_sc(wsr, o);
      if (__all((bad2(g, tg) & vm) == 0)) break;
      __builtin_amdgcn_s_sleep(1);
    }
    if (w == 0) STAMP(6);
    float y0 = sum16(dec(g[0])), y1 = sum16(dec(g[2]));
    y0 = sw16(y0, y0); y1 = sw16(y1, y1);
    const unsigned hp = pk2(bflo(xr) + rbf(y0), bfhi(xr) + rbf(y1));
    __builtin_amdgcn_raw_buffer_store_b64(u32x2{hp, tg.t32}, wsr, (pp < 5 && (lane & 31) < REP) ? WS_P3 + (((lane & 31) * 4 + b2i) * 320 + cg * 5 + pp) * 8 : OOB, 0, 16);
  } else if (w == 3) {
    const unsigned o = WS_P3 + (((bid % REP) * 4 + b2i) * 320) * 8 + lane * 16, o2 = lane < 32 ? o + 2048 : OOB;
    u32x4 g[3];
    _Pragma("nounroll") for (unsigned spin = 0; spin < SPIN_CAP; ++spin) {
      asm volatile("" ::: "memory");
      g[0] = ld_sc(wsr, o); g[1] = ld_sc(wsr, o + 1024); g[2] = ld_sc(wsr, o2);
      const unsigned bad = (g[0][1] ^ tg.t32) | (g[0][3] ^ tg.t32) | (g[1][1] ^ tg.t32) | (g[1][3] ^ tg.t32) | (((g[2][1] ^ tg.t32) | (g[2][3] ^ tg.t32)) & lo32);
      if (__all(bad == 0)) break;
      __builtin_amdgcn_s_sleep(1);
    }
    STAMP(7);
    if (bid == 0 && lane == 0) __builtin_amdgcn_raw_buffer_store_b64(u32x2{(unsigned)E1, (unsigned)(E1 >> 32)}, wsr, 0, 0, 16);
    float v[12];
#pragma unroll
    for (int i = 0; i < 3; ++i) { v[4 * i] = bflo(g[i][0]); v[4 * i + 1] = bfhi(g[i][0]); v[4 * i + 2] = bflo(g[i][2]); v[4 * i + 3] = bfhi(g[i][2]); }
#pragma unroll
    for (int i = 0; i < 3; ++i) if (i < 2 || lane < 32) *(LAS f32x4*)(ldf + L_HH + (lane + 64 * i) * 4) = f32x4{v[4 * i], v[4 * i + 1], v[4 * i + 2], v[4 * i + 3]};
    const float tail = lane < 32 ? 1.f : 0.f;
#pragma unroll
    for (int pass = 0; pass < 2; ++pass) {
      float sm = ((v[0] + v[1]) + (v[2] + v[3])) + ((v[4] + v[5]) + (v[6] + v[7])) + tail * ((v[8] + v[9]) + (v[10] + v[11]));
      const float mean = sum64(sm) * (1.f / D);
      float s2 = 0.f, s3 = 0.f;
#pragma unroll
      for (int k = 0; k < 8; ++k) { const float d = v[k] - mean; s2 = fmaf(d, d, s2); }
#pragma unroll
      for (int k = 8; k < 12; ++k) { const float d = v[k] - mean; s3 = fmaf(d, d, s3); }
      const float inv = inv_sd(sum64(fmaf(tail, s3, s2)) * (1.f / D)), mb = rbf(mean);
#pragma unroll
      for (int k = 0; k < 12; ++k) v[k] = rbf(rbf(v[k] - mb) * inv);
    }
#pragma unroll
    for (int i = 0; i < 3; ++i) if (i < 2 || lane < 32) *(LAS f32x4*)(ldf + L_T + (lane + 64 * i) * 4) = f32x4{v[4 * i], v[4 * i + 1], v[4 * i + 2], v[4 * i + 3]};
  }
  WG_BAR();
  if (w == 0) STAMP(15);
  {
    float ac[10];
#pragma unroll
    for (int i = 0; i < 10; ++i) {
      const float t = ldf[L_T + cg * 10 + i];
      ac[i] = 0.f;
#pragma unroll
      for (int k = 0; k < 3; ++k) {
        const float u = fmaf(t, mw1[k], mb1[k]);
        const float z = u * fmaf(u * u, -2.f * L2E * 0.044715f * 0.7978845608028654f, -2.f * L2E * 0.7978845608028654f);
        ac[i] = fmaf(u * __builtin_amdgcn_rcpf(1.f + __builtin_amdgcn_exp2f(z)), mw2[k], ac[i]);
      }
    }
    float y[5];
#pragma unroll
    for (int i = 0; i < 5; ++i) y[i] = sw32(ac[i], ac[i + 5]);
    const float z0 = sum16(sw16(y[0], y[1])), z1 = sum16(sw16(y[2], y[3])), z2 = sum16(sw16(y[4], y[4]));
    if ((lane & 15) == 0) {
      const int o = L_RED + w * 16 + (lane >> 5) * 5, odd = (lane >> 4) & 1;
      ldf[o + odd] = z0; ldf[o + 2 + odd] = z1; ldf[o + 4] = z2;
    }
  }
  if (w == 0) STAMP(16);
  WG_BAR();
  if (tid < 10) {
    float y = 0.f;
#pragma unroll
    for (int i = 0; i < 8; ++i) y += ldf[L_RED + i * 16 + tid];
    const __bf16 o = (__bf16)(ldf[L_HH + cg * 10 + tid] + rbf(rbf(y) + bflo(lds[L_X + 5])));
    out[b2i * D + cg * 10 + tid] = __builtin_bit_cast(unsigned short, o);
  }
  while (__builtin_amdgcn_s_memrealtime() - tstart < 15000ull) __builtin_amdgcn_s_sleep(1);
}

__global__ void k_empty() {}
extern "C" void kernel_launch(void* const* d_in, const int* in_sizes, int n_in, void* d_out, int out_size, void* d_ws, size_t ws_size,
                              hipStream_t stream) {
  typedef const unsigned short* P;
  fused_decode<<<256, 512, 0, stream>>>((P)d_in[0], (P)d_in[1], (P)d_in[2], (P)d_in[3], (P)d_in[4], (P)d_in[6], (P)d_in[7], (P)d_in[8], (P)d_in[9],
                                        (unsigned short*)d_out, (unsigned char*)d_ws);
  k_empty<<<256, 512, 0, stream>>>();
}
```

```cpp
#include <hip/hip_runtime.h>
#include <stdint.h>

typedef unsigned u32x4 __attribute__((ext_vector_type(4)));
typedef unsigned u32x2 __attribute__((ext_vector_type(2)));
typedef float f32x4 __attribute__((ext_vector_type(4)));
typedef float f32x2 __attribute__((ext_vector_type(2)));
typedef __bf16 bf16x2 __attribute__((ext_vector_type(2)));
typedef __bf16 bf16x8 __attribute__((ext_vector_type(8)));
typedef __amdgpu_buffer_rsrc_t rsrc_t;
#define LAS __attribute__((address_space(3)))

constexpr int D = 640, H = 32, SK = 4096, DH = 192, HD = H * DH, F = 1536;
constexpr unsigned SEQB = SK * DH * 2, TILEB = 8 * DH * 2, KVB = 4u * H * SEQB, WB = D * HD * 2;
#define NPRE 4
#define DEPTH 1
#define WQ_WAIT 15
#define SHIFT 2
#define QDELAY 0
#define H3DELAY 35
#define DK 1
#define DV 1
#define EARLYV 0
#define NOQ 0
#define NOTAIL 0
#define DIRUP 0
#define NOPUB 0
#define ROTX j*32+h
#define ILV 0
#define DRY 1
#define STR2(x) #x
#define STR(x) STR2(x)
constexpr int REP = 8;
constexpr unsigned P1REC = 208 * 8;
constexpr unsigned WS_QP = 4096, WS_P1 = WS_QP + H * 2 * 4 * DH * 8, WS_P2 = WS_P1 + 256 * P1REC, WS_P3 = WS_P2 + 4 * H * D * 8,
                   WS_END = WS_P3 + REP * 4 * 320 * 8;
constexpr unsigned OOB = 0x7ffffff0u;
constexpr unsigned SPIN_CAP = 1u << 17;
constexpr float L2E = 1.4426950408889634f, SCALE = 0.07216878364870322f;
constexpr int L_CNT = 0, L_Q = 4, L_REC = L_Q + 96, L_O = L_REC + 4 * 196, L_RED = L_O + 4 * 112, L_X = L_RED + 8 * 16, L_T = L_X + 8, L_HH = L_T + D,
              L_XN = L_HH + D, L_QR = L_XN + 4 * 320, L_MIX = L_QR + 4 * 256, L_WO = L_MIX + 3 * F / 2, L_PROF = L_WO + DH * 68, L_END = L_PROF + 64;
constexpr int LDS_DW = 24576;
static_assert(L_END <= LDS_DW && L_REC % 4 == 0 && L_O % 4 == 0 && L_T % 4 == 0 && L_HH % 4 == 0 && L_XN % 4 == 0 && L_QR % 4 == 0 && L_MIX % 4 == 0 && L_WO % 4 == 0, "lds map");

__device__ __forceinline__ float bflo(unsigned w) { return __uint_as_float(w << 16); }
__device__ __forceinline__ float bfhi(unsigned w) { return __uint_as_float(w & 0xffff0000u); }
__device__ __forceinline__ unsigned pk2(float lo, float hi) { const bf16x2 v = {(__bf16)lo, (__bf16)hi}; return __builtin_bit_cast(unsigned, v); }
__device__ __forceinline__ float rbf(float f) { return (float)(__bf16)f; }
__device__ __forceinline__ float dot2(unsigned a, unsigned b, float c) {
  return __builtin_amdgcn_fdot2_f32_bf16(__builtin_bit_cast(bf16x2, a), __builtin_bit_cast(bf16x2, b), c, false);
}
template <int CTRL> __device__ __forceinline__ float dpp(float x) {
  return __builtin_bit_cast(float, __builtin_amdgcn_mov_dpp(__builtin_bit_cast(int, x), CTRL, 0xf, 0xf, true));
}
constexpr int XOR1 = 0xB1, XOR2 = 0x4E, HMIR = 0x141, ROR8 = 0x128;
__device__ __forceinline__ float sw32(float a, float b) {
  const auto r = __builtin_amdgcn_permlane32_swap(__float_as_uint(a), __float_as_uint(b), false, false);
  return __uint_as_float(r[0]) + __uint_as_float(r[1]);
}
__device__ __forceinline__ float sw16(float a, float b) {
  const auto r = __builtin_amdgcn_permlane16_swap(__float_as_uint(a), __float_as_uint(b), false, false);
  return __uint_as_float(r[0]) + __uint_as_float(r[1]);
}
__device__ __forceinline__ float sum16(float v) { v += dpp<XOR1>(v); v += dpp<XOR2>(v); v += dpp<HMIR>(v); v += dpp<ROR8>(v); return v; }
__device__ __forceinline__ float sum64(float v) { v = sum16(v); v = sw16(v, v); return sw32(v, v); }
__device__ __forceinline__ rsrc_t mk(const void* p, unsigned n) { return __builtin_amdgcn_make_buffer_rsrc((void*)p, 0, n, 0x00020000); }
__device__ __forceinline__ u32x4 ld_nt(rsrc_t r, unsigned v, unsigned s) { return __builtin_amdgcn_raw_buffer_load_b128(r, v, s, 2); }
__device__ __forceinline__ u32x4 ld_sc(rsrc_t r, unsigned v) { return __builtin_amdgcn_raw_buffer_load_b128(r, v, 0, 16); }
__device__ __forceinline__ void st_sc(rsrc_t r, unsigned v, u32x4 x) { __builtin_amdgcn_raw_buffer_store_b128(x, r, v, 0, 16); }
#define PIN() do { asm volatile("" ::: "memory"); __builtin_amdgcn_sched_barrier(0); } while (0)
#define WG_BAR() do { asm volatile("s_waitcnt lgkmcnt(0)" ::: "memory"); __builtin_amdgcn_s_barrier(); PIN(); } while (0)
#define STAMP(i) do { } while (0)
struct Tag { unsigned lo, hi, t32; };
__device__ __forceinline__ u32x4 enc2(float a, float b, const Tag& t) {
  return u32x4{(__float_as_uint(a) & 0xffffff00u) | t.lo, t.hi, (__float_as_uint(b) & 0xffffff00u) | t.lo, t.hi};
}
__device__ __forceinline__ unsigned bad2(u32x4 g, const Tag& t) { return (((g[0] ^ t.lo) | (g[2] ^ t.lo)) & 0xffu) | (g[1] ^ t.hi) | (g[3] ^ t.hi); }
__device__ __forceinline__ float dec(unsigned g) { return __uint_as_float(g & 0xffffff00u); }
__device__ __forceinline__ void helper_bar(LAS unsigned* cnt, unsigned target, int lane) {
  asm volatile("s_waitcnt lgkmcnt(0)" ::: "memory");
  if (lane == 0) __hip_atomic_fetch_add(cnt, 1u, __ATOMIC_RELAXED, __HIP_MEMORY_SCOPE_WORKGROUP);
  while ((unsigned)__builtin_amdgcn_readfirstlane(__hip_atomic_load(cnt, __ATOMIC_RELAXED, __HIP_MEMORY_SCOPE_WORKGROUP)) < target) { }
  asm volatile("" ::: "memory");
}
__device__ __forceinline__ float inv_sd(float var) { return __builtin_amdgcn_rcpf(rbf(__builtin_amdgcn_sqrtf(rbf(rbf(var) + 1e-5f)))); }

__global__ void __launch_bounds__(512, 2)
fused_decode(const unsigned short* x, const unsigned short* kc, const unsigned short* vc,
             const unsigned short* wq, const unsigned short* wo, const unsigned short* w1,
             const unsigned short* b1, const unsigned short* w2, const unsigned short* b2,
             unsigned short* out, unsigned char* ws) {
  __shared__ __attribute__((aligned(16))) unsigned lds_raw[LDS_DW];
  LAS unsigned* lds = (LAS unsigned*)lds_raw;
  LAS float* ldf = (LAS float*)lds_raw;
  const int tid = threadIdx.x, lane = tid & 63, w = __builtin_amdgcn_readfirstlane(tid >> 6);
  const int bid = blockIdx.x, h = bid >> 3, j = (bid + h) & 7, b = j >> 1, s = j & 1;
  const bool has_q = j >= 2, has_o = j < 2 || s == 0;
  const int jo = j < 2 ? j : (j >> 1) + 1;
  const int b2i = bid >> 6, cg = bid & 63;
  asm volatile("" :: "s"(x), "s"(kc), "s"(vc), "s"(wq), "s"(wo), "s"(w1), "s"(b1), "s"(w2), "s"(b2), "s"(out), "s"(ws));
  const rsrc_t wsr0 = mk(ws, WS_END);
  const unsigned tstart = (unsigned)__builtin_amdgcn_s_memrealtime();
  if (w == 0) STAMP(0);
  u32x2 ep = __builtin_amdgcn_raw_buffer_load_b64(wsr0, 0, 0, 16);
  bf16x8 Bf[2][6];
  const unsigned lo32 = lane < 32 ? ~0u : 0u;

  if (w < 4) {
    const int r = lane >> 3, c = lane & 7;
    const unsigned voff = r * 384 + c * 16;
    const int ntw = j < 2 ? 256 : (s ? 256 + SHIFT : 256 - SHIFT);
    const int NT = (ntw + 3 - w) >> 2;
    const unsigned seq = (unsigned)(b * H + h) * SEQB + (s ? 512 - ntw : 0) * TILEB;
    const int R = (b * H + h) * 2 + s; (void)R;
    const int rot = ((ROTX) & 0xff) % ntw;
    auto toff = [&](int i) -> unsigned {
      if (ILV) return (unsigned)(b * H + h) * SEQB + (unsigned)(((i + (rot >> 2)) & 63) * 8 + s * 4 + w) * TILEB;
      int x = i * 4 + w + rot; x = x >= ntw ? x - ntw : x; return seq + (unsigned)x * TILEB; };
    const rsrc_t kr = mk(kc, KVB), vr = mk(vc, KVB);
    u32x4 K[NPRE][3], V[NPRE][3];
    WG_BAR();
#pragma unroll
    for (int p = 0; p < NPRE; ++p) {
      if (p >= DEPTH) asm volatile("s_waitcnt vmcnt(%0)" :: "i"(6 * DEPTH - 3) : "memory");
#pragma unroll
      for (int i = 0; i < 3; ++i) K[p][i] = ld_nt(kr, voff + 128 * i, toff(p));
      PIN();
      if (p >= DEPTH) asm volatile("s_waitcnt vmcnt(%0)" :: "i"(6 * DEPTH - 3) : "memory");
#pragma unroll
      for (int i = 0; i < 3; ++i) V[p][i] = ld_nt(vr, voff + 128 * i, toff(p));
      PIN();
    }
    if (w == 0) STAMP(1);
    WG_BAR();
    if (w == 0) STAMP(2);
    u32x4 qf[3];
#pragma unroll
    for (int i = 0; i < 3; ++i) qf[i] = *(LAS const u32x4*)(lds + L_Q + (c + 8 * i) * 4);
    float m = __uint_as_float(0xff7f0000u), l = 0.f, acc[24];
#pragma unroll
    for (int k = 0; k < 24; ++k) acc[k] = 0.f;
    auto step_fn = [&](u32x4 (&Kc)[3], u32x4 (&Vc)[3], bool rk, unsigned kn, unsigned ko, bool rv, unsigned vn, unsigned vo) {
      float a0 = 0.f, a1 = 0.f, a2 = 0.f;
#pragma unroll
      for (int k = 0; k < 4; ++k) { a0 = dot2(Kc[0][k], qf[0][k], a0); a1 = dot2(Kc[1][k], qf[1][k], a1); a2 = dot2(Kc[2][k], qf[2][k], a2); }
      float a = (a0 + a1) + a2;
      PIN();
      if (rk) {
        const rsrc_t k2 = mk(kc, kn);
#pragma unroll
        for (int i = 0; i < 3; ++i) Kc[i] = ld_nt(k2, voff + 128 * i, ko);
      }
      PIN();
      float vf[24];
#pragma unroll
      for (int i = 0; i < 3; ++i)
#pragma unroll
        for (int k = 0; k < 4; ++k) { vf[i * 8 + 2 * k] = bflo(Vc[i][k]); vf[i * 8 + 2 * k + 1] = bfhi(Vc[i][k]); }
      if (EARLYV) {
#pragma unroll
        for (int k = 0; k < 24; k += 8) asm volatile("" : "+v"(vf[k]), "+v"(vf[k + 1]), "+v"(vf[k + 2]), "+v"(vf[k + 3]), "+v"(vf[k + 4]), "+v"(vf[k + 5]), "+v"(vf[k + 6]), "+v"(vf[k + 7]));
        PIN();
        if (rv) {
          const rsrc_t v2 = mk(vc, vn);
#pragma unroll
          for (int i = 0; i < 3; ++i) Vc[i] = ld_nt(v2, voff + 128 * i, vo);
        }
        PIN();
      }
      a += dpp<XOR1>(a); a += dpp<XOR2>(a); a += dpp<HMIR>(a);
      const float sc = rbf(rbf(a) * SCALE);
      const float mn = fmaxf(m, sc);
      const float alpha = __builtin_amdgcn_exp2f((m - mn) * L2E), p = __builtin_amdgcn_exp2f((sc - mn) * L2E);
      l = fmaf(l, alpha, p); m = mn;
#pragma unroll
      for (int k = 0; k < 24; ++k) acc[k] = fmaf(p, vf[k], acc[k] * alpha);
      PIN();
      if (!EARLYV && rv) {
        const rsrc_t v2 = mk(vc, vn);
#pragma unroll
        for (int i = 0; i < 3; ++i) Vc[i] = ld_nt(v2, voff + 128 * i, vo);
      }
      PIN();
    };
#pragma unroll
    for (int p = 0; p < NPRE; ++p) step_fn(K[p], V[p], p < DK, KVB, toff(NPRE + p), p < DV, KVB, toff(NPRE + p));
    int t = NPRE;
    do {
#pragma unroll
      for (int d = 0; d < DK; ++d)
        step_fn(K[d], V[DV == 1 ? 0 : d], true, t + d + DK < NT ? KVB : 0u, toff(t + d + DK), true, t + d + DV < NT ? KVB : 0u, toff(t + d + DV));
      t += DK;
    } while (t < NT);
    if (w == 0) STAMP(3);
    float M = fmaxf(m, dpp<ROR8>(m));
    { const auto q16 = __builtin_amdgcn_permlane16_swap(__float_as_uint(M), __float_as_uint(M), false, false);
      M = fmaxf(__uint_as_float(q16[0]), __uint_as_float(q16[1]));
      const auto q32 = __builtin_amdgcn_permlane32_swap(__float_as_uint(M), __float_as_uint(M), false, false);
      M = fmaxf(__uint_as_float(q32[0]), __uint_as_float(q32[1])); }
    const float f = __builtin_amdgcn_exp2f((m - M) * L2E);
    l *= f; l += dpp<ROR8>(l); l = sw16(l, l); l = sw32(l, l);
    const bool b3 = lane & 8;
#pragma unroll
    for (int i = 0; i < 3; ++i) {
      float y[4], z[2];
#pragma unroll
      for (int e = 0; e < 4; ++e) y[e] = sw32(acc[i * 8 + e] * f, acc[i * 8 + e + 4] * f);
#pragma unroll
      for (int e = 0; e < 2; ++e) z[e] = sw16(y[e], y[e + 2]);
      const float keep = b3 ? z[1] : z[0], send = b3 ? z[0] : z[1];
      ldf[L_REC + w * 196 + i * 64 + c * 8 + r] = keep + dpp<ROR8>(send);
    }
    if (lane == 0) { ldf[L_REC + w * 196 + 192] = M; ldf[L_REC + w * 196 + 193] = l; }
    if (w == 0) STAMP(4);
  } else {
    const int hid = tid - 256, hw = w - 4;
    if (hid < 2) lds[L_CNT + hid] = 0;
    const int jq = has_q ? j - 2 : 0, kh = jq / 3, cq = jq % 3, rg = hid >> 3, c8 = hid & 7;
    u32x4 W[10], X[5];
    const rsrc_t xr = mk(x, has_q ? 4 * D * 2 : 0), wqr = mk(wq, has_q ? WB : 0);
#pragma unroll
    for (int i = 0; i < 5; ++i) X[i] = __builtin_amdgcn_raw_buffer_load_b128(xr, (lane >> 4) * (D * 2) + ((lane & 15) + 16 * i) * 16, 0, 2);
#pragma unroll
    for (int i = 0; i < 10; ++i)
      W[i] = ld_nt(wqr, (unsigned)(2 * rg + (i & 1)) * (HD * 2) + c8 * 16, (unsigned)(kh * 320 + (i >> 1) * 64) * (HD * 2) + (h * DH + cq * 64) * 2);
    PIN();
    if (WQ_WAIT < 15) asm volatile("s_waitcnt vmcnt(" STR(WQ_WAIT) ")" ::: "memory");
    WG_BAR();
    asm volatile("" : "+v"(ep));
    const unsigned long long E1 = ((unsigned long long)__builtin_amdgcn_readfirstlane(ep[1]) << 32 | (unsigned)__builtin_amdgcn_readfirstlane(ep[0])) + 1;
    const Tag tg{(unsigned)E1 & 0xffu, (unsigned)(E1 >> 8), (unsigned)E1};
    if (has_q) {
      float v[40], sm = 0.f;
#pragma unroll
      for (int i = 0; i < 5; ++i)
#pragma unroll
        for (int k = 0; k < 4; ++k) { v[i * 8 + 2 * k] = bflo(X[i][k]); v[i * 8 + 2 * k + 1] = bfhi(X[i][k]); sm += v[i * 8 + 2 * k] + v[i * 8 + 2 * k + 1]; }
      const float mean = sum16(sm) * (1.f / D);
      float s2 = 0.f;
#pragma unroll
      for (int k = 0; k < 40; ++k) { const float d = v[k] - mean; s2 = fmaf(d, d, s2); }
      const float inv = inv_sd(sum16(s2) * (1.f / D)), mb = rbf(mean);
#pragma unroll
      for (int i = 0; i < 5; ++i) {
        u32x4 o;
#pragma unroll
        for (int k = 0; k < 4; ++k) o[k] = pk2(rbf(v[i * 8 + 2 * k] - mb) * inv, rbf(v[i * 8 + 2 * k + 1] - mb) * inv);
        *(LAS u32x4*)(lds + L_XN + (lane >> 4) * 320 + ((lane & 15) + 16 * i) * 4) = o;
      }
      asm volatile("s_waitcnt lgkmcnt(0)" ::: "memory");
      if (hw == 0) STAMP(8);
      float qa[32];
#pragma unroll
      for (int k = 0; k < 32; ++k) qa[k] = 0.f;
#pragma unroll
      for (int i = 0; i < 5; ++i) {
        unsigned xp[4];
#pragma unroll
        for (int bb = 0; bb < 4; ++bb) xp[bb] = lds[L_XN + bb * 320 + kh * 160 + i * 32 + rg];
#pragma unroll
        for (int k = 0; k < 4; ++k) {
          const unsigned lo = __builtin_amdgcn_perm(W[2 * i + 1][k], W[2 * i][k], 0x05040100u), hi = __builtin_amdgcn_perm(W[2 * i + 1][k], W[2 * i][k], 0x07060302u);
#pragma unroll
          for (int bb = 0; bb < 4; ++bb) { qa[bb * 8 + 2 * k] = dot2(lo, xp[bb], qa[bb * 8 + 2 * k]); qa[bb * 8 + 2 * k + 1] = dot2(hi, xp[bb], qa[bb * 8 + 2 * k + 1]); }
        }
      }
      if (hw == 0) STAMP(9);
      const bool b3 = lane & 8;
#pragma unroll
      for (int bb = 0; bb < 4; ++bb) {
        float y[4], z[2];
#pragma unroll
        for (int e = 0; e < 4; ++e) y[e] = sw32(qa[bb * 8 + e], qa[bb * 8 + e + 4]);
#pragma unroll
        for (int e = 0; e < 2; ++e) z[e] = sw16(y[e], y[e + 2]);
        const float keep = b3 ? z[1] : z[0], send = b3 ? z[0] : z[1];
        ldf[L_QR + hw * 256 + bb * 64 + c8 * 8 + (lane >> 3)] = keep + dpp<ROR8>(send);
      }
      helper_bar(lds + L_CNT, 4, lane);
      if (hw < 2 && !NOPUB) {
        f32x2 a = *(LAS const f32x2*)(ldf + L_QR + 2 * hid);
#pragma unroll
        for (int ww = 1; ww < 4; ++ww) a += *(LAS const f32x2*)(ldf + L_QR + ww * 256 + 2 * hid);
        st_sc(wsr0, WS_QP + (((h * 2 + kh) * 4 + (hid >> 5)) * DH + cq * 64 + (hid & 31) * 2) * 8, enc2(a[0], a[1], tg));
      }
      if (hw == 0) STAMP(10);
    }
    if (hw >= 2 && !NOQ) {
      const int u = hid - 128;
      const unsigned o0 = u < 96 ? WS_QP + ((h * 2 * 4 + b) * DH + 2 * u) * 8 : OOB, o1 = u < 96 ? o0 + 4 * DH * 8 : OOB;
      const unsigned vm = u < 96 ? ~0u : 0u;
      u32x4 g0, g1;
      if (QDELAY) while ((unsigned)__builtin_amdgcn_s_memrealtime() - tstart < QDELAY) __builtin_amdgcn_s_sleep(2);
      _Pragma("nounroll") for (unsigned spin = 0; spin < SPIN_CAP; ++spin) {
        asm volatile("" ::: "memory");
        g0 = ld_sc(wsr0, o0); g1 = ld_sc(wsr0, o1);
        if (__all(((bad2(g0, tg) | bad2(g1, tg)) & vm) == 0)) break;
        __builtin_amdgcn_s_sleep(2);
      }
      if (u < 96) lds[L_Q + u] = pk2(dec(g0[0]) + dec(g1[0]), dec(g0[2]) + dec(g1[2]));
      if (hw == 2) STAMP(11);
    }
    WG_BAR();
    const rsrc_t wor = mk(wo, has_o ? WB : 0);
    u32x4 Wo[12];
#pragma unroll
    for (int i = 0; i < 12; ++i) Wo[i] = ld_nt(wor, (unsigned)(hid >> 4) * (D * 2) + (hid & 15) * 16, (unsigned)(h * DH + 16 * i) * (D * 2) + jo * 256);
    if (hid < 192) {
      *(LAS u32x4*)(lds + L_MIX + hid * 4) = __builtin_nontemporal_load((const u32x4*)(w1 + hid * 8));
      *(LAS u32x4*)(lds + L_MIX + 768 + hid * 4) = __builtin_nontemporal_load((const u32x4*)(b1 + hid * 8));
      *(LAS u32x4*)(lds + L_MIX + 1536 + hid * 4) = __builtin_nontemporal_load((const u32x4*)(w2 + hid * 8));
    }
    if (hid < 5) lds[L_X + hid] = *(const unsigned*)(x + b2i * D + cg * 10 + 2 * hid);
    if (hid == 5) lds[L_X + 5] = b2[0];
#pragma unroll
    for (int i = 0; i < 12; ++i) *(LAS u32x4*)(lds + L_WO + ((hid >> 4) + 16 * i) * 68 + (hid & 15) * 4) = Wo[i];
    helper_bar(lds + L_CNT, has_q ? 8 : 4, lane);
    LAS const unsigned short* wl = (LAS const unsigned short*)(lds + L_WO) + (lane >> 4) * 8 * 136 + hw * 32 + (lane & 15);
#pragma unroll
    for (int nt = 0; nt < 2; ++nt)
#pragma unroll
      for (int ks = 0; ks < 6; ++ks) {
        u32x4 fr;
#pragma unroll
        for (int e = 0; e < 4; ++e) fr[e] = (unsigned)wl[(ks * 32 + 2 * e) * 136 + nt * 16] | ((unsigned)wl[(ks * 32 + 2 * e + 1) * 136 + nt * 16] << 16);
        Bf[nt][ks] = __builtin_bit_cast(bf16x8, fr);
      }
  }
  const unsigned long long E1 = ((unsigned long long)__builtin_amdgcn_readfirstlane(ep[1]) << 32 | (unsigned)__builtin_amdgcn_readfirstlane(ep[0])) + 1;
  const Tag tg{(unsigned)E1 & 0xffu, (unsigned)(E1 >> 8), (unsigned)E1};
  bool dry = DRY && w == 7;
#pragma nounroll
  for (;;) {
  const rsrc_t wsr = mk(ws, dry ? 0u : WS_END);
  const unsigned cap = dry ? 1u : SPIN_CAP;
  if (!dry) WG_BAR();
  if (w == 4) STAMP(12);
  if (NOTAIL && !dry) { WG_BAR(); break; }
  if (w == 4 || w == 5 || dry) {
    const int u = tid - 256;
    float mm[4], M;
#pragma unroll
    for (int i = 0; i < 4; ++i) mm[i] = ldf[L_REC + i * 196 + 192];
    M = fmaxf(fmaxf(mm[0], mm[1]), fmaxf(mm[2], mm[3]));
    f32x2 a = {0.f, 0.f};
    const int uu = u < 96 ? u : 96;
#pragma unroll
    for (int i = 0; i < 4; ++i) a += __builtin_amdgcn_exp2f((mm[i] - M) * L2E) * *(LAS const f32x2*)(ldf + L_REC + i * 196 + 2 * uu);
    st_sc(wsr, u <= 96 ? WS_P1 + (h * 8 + j) * P1REC + u * 16 : OOB, enc2(u < 96 ? a[0] : M, a[1], tg));
    if (w == 4) STAMP(13);
  }
  float A0[3], A1[3], A2[3], A3[3], B0[3], B1[3];
  {
    LAS const unsigned short* mx = (LAS const unsigned short*)(lds + L_MIX);
    constexpr float K0 = -2.f * L2E * 0.7978845608028654f, K1 = K0 * 0.044715f;
#pragma unroll
    for (int i = 0; i < 3; ++i) {
      const float a = bflo(mx[tid + 512 * i]), c = bflo(mx[F + tid + 512 * i]), e = bflo(mx[2 * F + tid + 512 * i]);
      A3[i] = K1 * a * a * a; A2[i] = 3.f * K1 * a * a * c; A1[i] = a * fmaf(3.f * K1 * c, c, K0); A0[i] = c * fmaf(K1 * c, c, K0);
      B1[i] = a * e; B0[i] = c * e;
    }
  }
  if (has_o) {
    if (w < 4 || dry) {
      const unsigned r0 = WS_P1 + (h * 8 + 2 * w) * P1REC, oa = r0 + lane * 16, ob = lane < 32 ? oa + 1024 : OOB, oc = lane < 32 ? ob + P1REC : OOB;
      u32x4 a0, a1, c0, c1, s0, s1;
      _Pragma("nounroll") for (unsigned spin = 0; spin < cap; ++spin) {
        asm volatile("" ::: "memory");
        s0 = ld_sc(wsr, r0 + 96 * 16); s1 = ld_sc(wsr, r0 + P1REC + 96 * 16);
        a0 = ld_sc(wsr, oa); a1 = ld_sc(wsr, oa + P1REC); c0 = ld_sc(wsr, ob); c1 = ld_sc(wsr, oc);
        const unsigned bad = bad2(s0, tg) | bad2(s1, tg) | bad2(a0, tg) | bad2(a1, tg) | ((bad2(c0, tg) | bad2(c1, tg)) & lo32);
        if (__all(bad == 0)) break;
        __builtin_amdgcn_s_sleep(1);
      }
      if (w == 0) STAMP(5);
      const float m0 = dec(s0[0]), m1 = dec(s1[0]), M = fmaxf(m0, m1);
      float w0 = __builtin_amdgcn_exp2f((m0 - M) * L2E), w1_ = __builtin_amdgcn_exp2f((m1 - M) * L2E);
      const float inv = __builtin_amdgcn_rcpf(fmaf(w0, dec(s0[2]), w1_ * dec(s1[2])));
      w0 *= inv; w1_ *= inv;
      lds[L_O + (w & 3) * 112 + lane] = pk2(fmaf(w0, dec(a0[0]), w1_ * dec(a1[0])), fmaf(w0, dec(a0[2]), w1_ * dec(a1[2])));
      if (lane < 32) lds[L_O + (w & 3) * 112 + 64 + lane] = pk2(fmaf(w0, dec(c0[0]), w1_ * dec(c1[0])), fmaf(w0, dec(c0[2]), w1_ * dec(c1[2])));
    }
    if (!dry) WG_BAR();
    if (w >= 4) {
      f32x4 d0 = {0.f, 0.f, 0.f, 0.f}, d1 = d0;
#pragma unroll
      for (int ks = 0; ks < 6; ++ks) {
        const bf16x8 af = __builtin_bit_cast(bf16x8, *(LAS const u32x4*)(lds + L_O + (lane & 3) * 112 + ks * 16 + (lane >> 4) * 4));
        d0 = __builtin_amdgcn_mfma_f32_16x16x32_bf16(af, Bf[0][ks], d0, 0, 0, 0);
        d1 = __builtin_amdgcn_mfma_f32_16x16x32_bf16(af, Bf[1][ks], d1, 0, 0, 0);
      }
      const int g = lane >> 4, n = lane & 15;
      const bool g1 = lane & 16, g2 = lane & 32;
      const float a01 = g1 ? d0[1] : d0[0], a23 = g1 ? d0[3] : d0[2], c01 = g1 ? d1[1] : d1[0], c23 = g1 ? d1[3] : d1[2];
      const float a = g2 ? a23 : a01, cc = g2 ? c23 : c01;
      const float xa = dpp<XOR1>(a), xc = dpp<XOR1>(cc);
      const bool odd = lane & 1;
      st_sc(wsr, WS_P2 + ((g * H + h) * D + jo * 128 + (w - 4) * 32 + (odd ? 15 + n : n)) * 8, enc2(odd ? xc : a, odd ? cc : xa, tg));
      if (w == 4) STAMP(14);
    }
  }
  if (w < 3 || dry) {
    const int pp = 2 * w + (lane >> 5);
    const unsigned o = pp < 5 ? WS_P2 + ((b2i * H + (lane & 31)) * D + cg * 10 + 2 * pp) * 8 : OOB;
    const unsigned xr = lds[L_X + (pp < 5 ? pp : 0)];
    const unsigned vm = pp < 5 ? ~0u : 0u;
    u32x4 g;
    _Pragma("nounroll") for (unsigned spin = 0; spin < cap; ++spin) {
      asm volatile("" ::: "memory");
      g = ld_sc(wsr, o);
      if (__all((bad2(g, tg) & vm) == 0)) break;
      __builtin_amdgcn_s_sleep(1);
    }
    if (w == 0) STAMP(6);
    if (H3DELAY && w == 0 && lane == 0) lds[L_CNT + 1] = (unsigned)__builtin_amdgcn_s_memrealtime() | 1u;
    float y0 = sum16(dec(g[0])), y1 = sum16(dec(g[2]));
    y0 = sw16(y0, y0); y1 = sw16(y1, y1);
    const unsigned hp = pk2(bflo(xr) + rbf(y0), bfhi(xr) + rbf(y1));
    __builtin_amdgcn_raw_buffer_store_b64(u32x2{hp, tg.t32}, wsr, (pp < 5 && (lane & 31) < REP) ? WS_P3 + (((lane & 31) * 4 + b2i) * 320 + cg * 5 + pp) * 8 : OOB, 0, 16);
  }
  if (w == 3 || dry) {
    const unsigned o = WS_P3 + (((bid % REP) * 4 + b2i) * 320) * 8 + lane * 16, o2 = lane < 32 ? o + 2048 : OOB;
    u32x4 g[3];
    if (H3DELAY && !dry) {
      unsigned t2;
      _Pragma("nounroll") for (unsigned spin = 0; spin < 64 * SPIN_CAP; ++spin) {
        t2 = __builtin_amdgcn_readfirstlane(__hip_atomic_load(lds + L_CNT + 1, __ATOMIC_RELAXED, __HIP_MEMORY_SCOPE_WORKGROUP));
        if (t2) break;
        __builtin_amdgcn_s_sleep(1);
      }
      while ((int)((unsigned)__builtin_amdgcn_s_memrealtime() - t2) < H3DELAY) { }
    }
    _Pragma("nounroll") for (unsigned spin = 0; spin < cap; ++spin) {
      asm volatile("" ::: "memory");
      g[0] = ld_sc(wsr, o); g[1] = ld_sc(wsr, o + 1024); g[2] = ld_sc(wsr, o2);
      const unsigned bad = (g[0][1] ^ tg.t32) | (g[0][3] ^ tg.t32) | (g[1][1] ^ tg.t32) | (g[1][3] ^ tg.t32) | (((g[2][1] ^ tg.t32) | (g[2][3] ^ tg.t32)) & lo32);
      if (__all(bad == 0)) break;
      __builtin_amdgcn_s_sleep(1);
    }
    STAMP(7);
    if (bid == 0 && lane == 0) __builtin_amdgcn_raw_buffer_store_b64(u32x2{(unsigned)E1, (unsigned)(E1 >> 32)}, wsr, 0, 0, 16);
    float v[12];
#pragma unroll
    for (int i = 0; i < 3; ++i) { v[4 * i] = bflo(g[i][0]); v[4 * i + 1] = bfhi(g[i][0]); v[4 * i + 2] = bflo(g[i][2]); v[4 * i + 3] = bfhi(g[i][2]); }
#pragma unroll
    for (int i = 0; i < 3; ++i) if (i < 2 || lane < 32) *(LAS f32x4*)(ldf + L_HH + (lane + 64 * i) * 4) = f32x4{v[4 * i], v[4 * i + 1], v[4 * i + 2], v[4 * i + 3]};
    const float tail = lane < 32 ? 1.f : 0.f;
#pragma unroll
    for (int pass = 0; pass < 2; ++pass) {
      float sm = ((v[0] + v[1]) + (v[2] + v[3])) + ((v[4] + v[5]) + (v[6] + v[7])) + tail * ((v[8] + v[9]) + (v[10] + v[11]));
      const float mean = sum64(sm) * (1.f / D);
      float s2 = 0.f, s3 = 0.f;
#pragma unroll
      for (int k = 0; k < 8; ++k) { const float d = v[k] - mean; s2 = fmaf(d, d, s2); }
#pragma unroll
      for (int k = 8; k < 12; ++k) { const float d = v[k] - mean; s3 = fmaf(d, d, s3); }
      const float inv = inv_sd(sum64(fmaf(tail, s3, s2)) * (1.f / D)), mb = rbf(mean);
#pragma unroll
      for (int k = 0; k < 12; ++k) v[k] = rbf(rbf(v[k] - mb) * inv);
    }
#pragma unroll
    for (int i = 0; i < 3; ++i) if (i < 2 || lane < 32) *(LAS f32x4*)(ldf + L_T + (lane + 64 * i) * 4) = f32x4{v[4 * i], v[4 * i + 1], v[4 * i + 2], v[4 * i + 3]};
  }
  if (!dry) WG_BAR();
  if (w == 0) STAMP(15);
  {
    float ac[10];
#pragma unroll
    for (int i = 0; i < 5; ++i) {
      const f32x2 t = *(LAS const f32x2*)(ldf + L_T + cg * 10 + 2 * i);
      f32x2 a = {0.f, 0.f};
#pragma unroll
      for (int k = 0; k < 3; ++k) {
        const f32x2 z = __builtin_elementwise_fma(__builtin_elementwise_fma(__builtin_elementwise_fma(t, (f32x2)A3[k], (f32x2)A2[k]), t, (f32x2)A1[k]), t, (f32x2)A0[k]);
        const f32x2 d = f32x2{__builtin_amdgcn_exp2f(z[0]), __builtin_amdgcn_exp2f(z[1])} + 1.f;
        a = __builtin_elementwise_fma(__builtin_elementwise_fma(t, (f32x2)B1[k], (f32x2)B0[k]), f32x2{__builtin_amdgcn_rcpf(d[0]), __builtin_amdgcn_rcpf(d[1])}, a);
      }
      ac[2 * i] = a[0]; ac[2 * i + 1] = a[1];
    }
    float y[5];
#pragma unroll
    for (int i = 0; i < 5; ++i) y[i] = sw32(ac[i], ac[i + 5]);
    const float z0 = sum16(sw16(y[0], y[1])), z1 = sum16(sw16(y[2], y[3])), z2 = sum16(sw16(y[4], y[4]));
    if ((lane & 15) == 0) {
      const int o = L_RED + w * 16 + (lane >> 5) * 5, odd = (lane >> 4) & 1;
      ldf[o + odd] = z0; ldf[o + 2 + odd] = z1; ldf[o + 4] = z2;
    }
  }
  if (w == 0) STAMP(16);
  if (!dry) WG_BAR();
  if (w == 0 || dry) {
    float y = 0.f;
#pragma unroll
    for (int i = 0; i < 8; ++i) y += ldf[L_RED + i * 16 + (lane & 15)];
    const __bf16 o = (__bf16)(ldf[L_HH + cg * 10 + (lane & 15)] + rbf(rbf(y) + bflo(lds[L_X + 5])));
    __builtin_amdgcn_raw_buffer_store_b16(__builtin_bit_cast(unsigned short, o), mk(out, dry ? 0u : 4 * D * 2), lane < 10 ? (b2i * D + cg * 10 + lane) * 2 : OOB, 0, 0);
  }
  if (!dry) break;
  dry = false;
  }
}

extern "C" void kernel_launch(void* const* d_in, const int* in_sizes, int n_in, void* d_out, int out_size, void* d_ws, size_t ws_size,
                              hipStream_t stream) {
  typedef const unsigned short* P;
  fused_decode<<<256, 512, 0, stream>>>((P)d_in[0], (P)d_in[1], (P)d_in[2], (P)d_in[3], (P)d_in[4], (P)d_in[6], (P)d_in[7], (P)d_in[8], (P)d_in[9],
                                        (unsigned short*)d_out, (unsigned char*)d_ws);
}
```

```cpp
#include <hip/hip_runtime.h>
#include <stdint.h>

typedef unsigned u32x4 __attribute__((ext_vector_type(4)));
typedef unsigned u32x2 __attribute__((ext_vector_type(2)));
typedef float f32x4 __attribute__((ext_vector_type(4)));
typedef float f32x2 __attribute__((ext_vector_type(2)));
typedef __bf16 bf16x2 __attribute__((ext_vector_type(2)));
typedef __bf16 bf16x8 __attribute__((ext_vector_type(8)));
typedef __amdgpu_buffer_rsrc_t rsrc_t;
#define LAS __attribute__((address_space(3)))

#define NPRE 6
#define QP3 1
#define WQ_WAIT 8
#define QDELAY 0
#define SHIFT 0
#define ROTX (h * 8 + j)
#define H1SLP 1
#define H2SLP 1
#define STR2(x) #x
#define STR(x) STR2(x)

constexpr int D = 640, H = 32, SK = 4096, DH = 192, HD = H * DH, F = 1536;
constexpr unsigned SEQB = SK * DH * 2, TILEB = 8 * DH * 2, KVB = 4u * H * SEQB, WB = D * HD * 2;
constexpr unsigned P1REC = 208 * 8;
constexpr unsigned WS_QP = 4096, WS_P1 = WS_QP + H * 2 * 4 * DH * 8, WS_P2 = WS_P1 + 256 * P1REC, WS_END = WS_P2 + 4 * 16 * D * 8;
constexpr unsigned OOB = 0x7ffffff0u;
constexpr unsigned SPIN_CAP = 1u << 17;
constexpr float L2E = 1.4426950408889634f, SCALE = 0.07216878364870322f;
constexpr int OSTR = 208;
constexpr int L_CNT = 0, L_Q = 8, L_REC = L_Q + 96, L_O = L_REC + 4 * 196, L_RED = L_O + 4 * OSTR, L_ST = L_RED + 8 * 16, L_X = L_ST + 16, L_HH = L_X + 324,
              L_XN = L_HH + 320, L_QR = L_XN + 4 * 320, L_MIX = L_QR + 4 * 256, L_WO = L_MIX + 3 * F / 2, L_Y = L_WO, L_END = L_WO + 2 * DH * 36;
constexpr int LDS_DW = 21504;
static_assert(L_END <= LDS_DW && L_REC % 4 == 0 && L_O % 4 == 0 && L_XN % 4 == 0 && L_QR % 4 == 0 && L_MIX % 4 == 0 && L_WO % 4 == 0, "lds map");

__device__ __forceinline__ float bflo(unsigned w) { return __uint_as_float(w << 16); }
__device__ __forceinline__ float bfhi(unsigned w) { return __uint_as_float(w & 0xffff0000u); }
__device__ __forceinline__ unsigned pk2(float lo, float hi) { unsigned r; asm("v_cvt_pk_bf16_f32 %0, %1, %2" : "=v"(r) : "v"(lo), "v"(hi)); return r; }
__device__ __forceinline__ float rbf(float f) { return (float)(__bf16)f; }
__device__ __forceinline__ f32x2 up2(unsigned w) { return f32x2{bflo(w), bfhi(w)}; }
__device__ __forceinline__ f32x2 rbf2(f32x2 v) { return up2(pk2(v[0], v[1])); }
__device__ __forceinline__ float dot2(unsigned a, unsigned b, float c) {
  return __builtin_amdgcn_fdot2_f32_bf16(__builtin_bit_cast(bf16x2, a), __builtin_bit_cast(bf16x2, b), c, false);
}
template <int CTRL> __device__ __forceinline__ float dpp(float x) {
  return __builtin_bit_cast(float, __builtin_amdgcn_mov_dpp(__builtin_bit_cast(int, x), CTRL, 0xf, 0xf, true));
}
constexpr int XOR1 = 0xB1, XOR2 = 0x4E, HMIR = 0x141, ROR8 = 0x128;
__device__ __forceinline__ float sw32(float a, float b) {
  const auto r = __builtin_amdgcn_permlane32_swap(__float_as_uint(a), __float_as_uint(b), false, false);
  return __uint_as_float(r[0]) + __uint_as_float(r[1]);
}
__device__ __forceinline__ float sw16(float a, float b) {
  const auto r = __builtin_amdgcn_permlane16_swap(__float_as_uint(a), __float_as_uint(b), false, false);
  return __uint_as_float(r[0]) + __uint_as_float(r[1]);
}
__device__ __forceinline__ float sum16(float v) { v += dpp<XOR1>(v); v += dpp<XOR2>(v); v += dpp<HMIR>(v); v += dpp<ROR8>(v); return v; }
__device__ __forceinline__ float sum64(float v) { v = sum16(v); v = sw16(v, v); return sw32(v, v); }
__device__ __forceinline__ float scatter8(const float* a, bool b3) {
  float y[4], z[2];
#pragma unroll
  for (int e = 0; e < 4; ++e) y[e] = sw32(a[e], a[e + 4]);
#pragma unroll
  for (int e = 0; e < 2; ++e) z[e] = sw16(y[e], y[e + 2]);
  return (b3 ? z[1] : z[0]) + dpp<ROR8>(b3 ? z[0] : z[1]);
}
__device__ __forceinline__ rsrc_t mk(const void* p, unsigned n) { return __builtin_amdgcn_make_buffer_rsrc((void*)p, 0, n, 0x00020000); }
__device__ __forceinline__ u32x4 ld_nt(rsrc_t r, unsigned v, unsigned s) { return __builtin_amdgcn_raw_buffer_load_b128(r, v, s, 2); }
__device__ __forceinline__ u32x4 ld_sc(rsrc_t r, unsigned v, unsigned s = 0) { return __builtin_amdgcn_raw_buffer_load_b128(r, v, s, 16); }
__device__ __forceinline__ void st_sc(rsrc_t r, unsigned v, u32x4 x) { __builtin_amdgcn_raw_buffer_store_b128(x, r, v, 0, 16); }
__device__ __forceinline__ unsigned now() { return (unsigned)__builtin_amdgcn_s_memrealtime(); }
__device__ __forceinline__ unsigned lds_peek(LAS unsigned* p) {
  return __builtin_amdgcn_readfirstlane(__hip_atomic_load(p, __ATOMIC_RELAXED, __HIP_MEMORY_SCOPE_WORKGROUP));
}
#define PIN() do { asm volatile("" ::: "memory"); __builtin_amdgcn_sched_barrier(0); } while (0)
#define WG_BAR() do { asm volatile("s_waitcnt lgkmcnt(0)" ::: "memory"); __builtin_amdgcn_s_barrier(); PIN(); } while (0)
#define STAMP(i) do { } while (0)
struct Tag { unsigned lo, hi, t32; };
__device__ __forceinline__ u32x4 enc2(float a, float b, const Tag& t) {
  return u32x4{(__float_as_uint(a) & 0xffffff00u) | t.lo, t.hi, (__float_as_uint(b) & 0xffffff00u) | t.lo, t.hi};
}
__device__ __forceinline__ unsigned bad2(u32x4 g, const Tag& t) { return (((g[0] ^ t.lo) | (g[2] ^ t.lo)) & 0xffu) | (g[1] ^ t.hi) | (g[3] ^ t.hi); }
__device__ __forceinline__ float dec(unsigned g) { return __uint_as_float(g & 0xffffff00u); }
__device__ __forceinline__ void helper_bar(LAS unsigned* cnt, unsigned target, int lane) {
  asm volatile("s_waitcnt lgkmcnt(0)" ::: "memory");
  if (lane == 0) __hip_atomic_fetch_add(cnt, 1u, __ATOMIC_RELAXED, __HIP_MEMORY_SCOPE_WORKGROUP);
  while (lds_peek(cnt) < target) { }
  asm volatile("" ::: "memory");
}
__device__ __forceinline__ float inv_sd(float var) { return __builtin_amdgcn_rcpf(rbf(__builtin_amdgcn_sqrtf(rbf(rbf(var) + 1e-5f)))); }

__global__ void __launch_bounds__(512, 2)
fused_decode(const unsigned short* x, const unsigned short* kc, const unsigned short* vc, const unsigned short* wq, const unsigned short* wo,
             const unsigned short* w1, const unsigned short* b1, const unsigned short* w2, const unsigned short* b2, unsigned short* out,
             unsigned char* ws) {
  __shared__ __attribute__((aligned(16))) unsigned lds_raw[LDS_DW];
  LAS unsigned* lds = (LAS unsigned*)lds_raw;
  LAS float* ldf = (LAS float*)lds_raw;
  const int tid = threadIdx.x, lane = tid & 63, w = __builtin_amdgcn_readfirstlane(tid >> 6);
  const int bid = blockIdx.x, h = bid >> 3, j = (bid + h) & 7, b = j >> 1, s = j & 1;
  const bool has_q = QP3 ? (j >= 2 && s == 1) : j >= 2, has_o = j < 2 || s == 0;
  const int hp = h >> 1, co = (h & 1) * 5 + (j < 2 ? j : b + 1);
  const int b2i = bid >> 6, cg = bid & 63;
  STAMP(0);
  const unsigned tstart = QDELAY ? now() : 0u;
  asm volatile("" :: "s"(x), "s"(kc), "s"(vc), "s"(wq), "s"(wo), "s"(w1), "s"(b1), "s"(w2), "s"(b2), "s"(out), "s"(ws));
  const rsrc_t wsr0 = mk(ws, WS_END);
  u32x2 ep = __builtin_amdgcn_raw_buffer_load_b64(wsr0, 0, 0, 16);
  bf16x8 Bf[12];

  if (w < 4) {
    const int r = lane >> 3, c = lane & 7;
    const unsigned voff = r * 384 + c * 16;
    const int ntw = 256 + (j < 2 ? 0 : (s ? -SHIFT : SHIFT)), NT = (ntw + 3 - w) >> 2;
    const unsigned seq = (unsigned)(b * H + h) * SEQB + (s ? 512 - ntw : 0) * TILEB;
    const int rot = (ROTX) % ntw;
    auto toff = [&](int i) -> unsigned { const int y = i * 4 + w + rot; return seq + (unsigned)(y >= ntw ? y - ntw : y) * TILEB; };
    const rsrc_t kr = mk(kc, KVB), vr = mk(vc, KVB);
    u32x4 K[NPRE][3], V[NPRE][3];
    WG_BAR();
#pragma unroll
    for (int p = 0; p < NPRE; ++p) {
      if (p) asm volatile("s_waitcnt vmcnt(3)" ::: "memory");
#pragma unroll
      for (int i = 0; i < 3; ++i) K[p][i] = ld_nt(kr, voff + 128 * i, toff(p));
      PIN();
      if (p) asm volatile("s_waitcnt vmcnt(3)" ::: "memory");
#pragma unroll
      for (int i = 0; i < 3; ++i) V[p][i] = ld_nt(vr, voff + 128 * i, toff(p));
      PIN();
    }
    WG_BAR();
    STAMP(1);
    u32x4 qf[3];
#pragma unroll
    for (int i = 0; i < 3; ++i) qf[i] = *(LAS const u32x4*)(lds + L_Q + (c + 8 * i) * 4);
    float m = __uint_as_float(0xff7f0000u), l = 0.f, acc[24];
#pragma unroll
    for (int k = 0; k < 24; ++k) acc[k] = 0.f;
    auto step_fn = [&](u32x4 (&Kc)[3], u32x4 (&Vc)[3], bool refill, unsigned nbytes, unsigned soff) {
      float a0 = 0.f, a1 = 0.f, a2 = 0.f;
#pragma unroll
      for (int k = 0; k < 4; ++k) { a0 = dot2(Kc[0][k], qf[0][k], a0); a1 = dot2(Kc[1][k], qf[1][k], a1); a2 = dot2(Kc[2][k], qf[2][k], a2); }
      float a = (a0 + a1) + a2;
      PIN();
      if (refill) {
        const rsrc_t k2 = mk(kc, nbytes);
#pragma unroll
        for (int i = 0; i < 3; ++i) Kc[i] = ld_nt(k2, voff + 128 * i, soff);
      }
      PIN();
      a += dpp<XOR1>(a); a += dpp<XOR2>(a); a += dpp<HMIR>(a);
      const float sc = rbf(rbf(a) * SCALE);
      const float mn = fmaxf(m, sc);
      const float alpha = __builtin_amdgcn_exp2f((m - mn) * L2E), p = __builtin_amdgcn_exp2f((sc - mn) * L2E);
      l = fmaf(l, alpha, p); m = mn;
#pragma unroll
      for (int i = 0; i < 3; ++i)
#pragma unroll
        for (int k = 0; k < 4; ++k) {
          acc[i * 8 + 2 * k]     = fmaf(p, bflo(Vc[i][k]), acc[i * 8 + 2 * k] * alpha);
          acc[i * 8 + 2 * k + 1] = fmaf(p, bfhi(Vc[i][k]), acc[i * 8 + 2 * k + 1] * alpha);
        }
      PIN();
      if (refill) {
        const rsrc_t v2 = mk(vc, nbytes);
#pragma unroll
        for (int i = 0; i < 3; ++i) Vc[i] = ld_nt(v2, voff + 128 * i, soff);
      }
      PIN();
    };
#pragma unroll
    for (int p = 0; p < NPRE; ++p) step_fn(K[p], V[p], p == 0, KVB, toff(NPRE));
    int t = NPRE + 1;
    do { step_fn(K[0], V[0], true, t < NT ? KVB : 0u, toff(t)); } while (++t <= NT);
    float M = fmaxf(m, dpp<ROR8>(m));
    { const auto q16 = __builtin_amdgcn_permlane16_swap(__float_as_uint(M), __float_as_uint(M), false, false);
      M = fmaxf(__uint_as_float(q16[0]), __uint_as_float(q16[1]));
      const auto q32 = __builtin_amdgcn_permlane32_swap(__float_as_uint(M), __float_as_uint(M), false, false);
      M = fmaxf(__uint_as_float(q32[0]), __uint_as_float(q32[1])); }
    const float f = __builtin_amdgcn_exp2f((m - M) * L2E);
    l *= f; l += dpp<ROR8>(l); l = sw16(l, l); l = sw32(l, l);
#pragma unroll
    for (int k = 0; k < 24; ++k) acc[k] *= f;
#pragma unroll
    for (int i = 0; i < 3; ++i) ldf[L_REC + w * 196 + i * 64 + c * 8 + r] = scatter8(acc + i * 8, lane & 8);
    if (lane == 0) { ldf[L_REC + w * 196 + 192] = M; ldf[L_REC + w * 196 + 193] = l; }
  } else {
    const int hid = tid - 256, hw = w - 4;
    if (hid < 8) lds[L_CNT + hid] = 0;
    constexpr int NW = QP3 ? 20 : 10;
    const int jq = has_q ? j - 2 : 0, kh = QP3 ? 0 : jq / 3, cq = QP3 ? jq >> 1 : jq % 3, rg = hid >> 3, c8 = hid & 7;
    u32x4 W[NW], X[5];
    const rsrc_t xr = mk(x, has_q ? 4 * D * 2 : 0), wqr = mk(wq, has_q ? WB : 0);
#pragma unroll
    for (int i = 0; i < 5; ++i) X[i] = ld_nt(xr, (lane >> 4) * (D * 2) + ((lane & 15) + 16 * i) * 16, 0);
    PIN();
#pragma unroll
    for (int i = 0; i < NW; ++i) {
      W[i] = ld_nt(wqr, (unsigned)(2 * rg + (i & 1)) * (HD * 2) + c8 * 16, (unsigned)(kh * 320 + (i >> 1) * 64) * (HD * 2) + (h * DH + cq * 64) * 2);
      if (i & 1) PIN();
    }
    if (WQ_WAIT < NW + 5) asm volatile("s_waitcnt vmcnt(" STR(WQ_WAIT) ")" ::: "memory");
    WG_BAR();
    asm volatile("" : "+v"(ep));
    const unsigned long long E1 = ((unsigned long long)__builtin_amdgcn_readfirstlane(ep[1]) << 32 | (unsigned)__builtin_amdgcn_readfirstlane(ep[0])) + 1;
    const Tag tg{(unsigned)E1 & 0xffu, (unsigned)(E1 >> 8), (unsigned)E1};
    if (has_q) {
      f32x2 v[20], sm = {0.f, 0.f};
#pragma unroll
      for (int i = 0; i < 20; ++i) { v[i] = up2(X[i >> 2][i & 3]); sm += v[i]; }
      const float mean = sum16(sm[0] + sm[1]) * (1.f / D);
      f32x2 s2 = {0.f, 0.f};
#pragma unroll
      for (int i = 0; i < 20; ++i) { const f32x2 d = v[i] - mean; s2 = __builtin_elementwise_fma(d, d, s2); }
      const float inv = inv_sd(sum16(s2[0] + s2[1]) * (1.f / D)), mb = rbf(mean);
#pragma unroll
      for (int i = 0; i < 5; ++i) {
        u32x4 o;
#pragma unroll
        for (int k = 0; k < 4; ++k) { const f32x2 y = rbf2(v[i * 4 + k] - mb) * inv; o[k] = pk2(y[0], y[1]); }
        *(LAS u32x4*)(lds + L_XN + (lane >> 4) * 320 + ((lane & 15) + 16 * i) * 4) = o;
      }
      asm volatile("s_waitcnt lgkmcnt(0)" ::: "memory");
      float qa[32];
#pragma unroll
      for (int k = 0; k < 32; ++k) qa[k] = 0.f;
#pragma unroll
      for (int i = 0; i < NW / 2; ++i) {
        unsigned xp[4];
#pragma unroll
        for (int bb = 0; bb < 4; ++bb) xp[bb] = lds[L_XN + bb * 320 + kh * 160 + i * 32 + rg];
#pragma unroll
        for (int k = 0; k < 4; ++k) {
          const unsigned lo = __builtin_amdgcn_perm(W[2 * i + 1][k], W[2 * i][k], 0x05040100u), hi = __builtin_amdgcn_perm(W[2 * i + 1][k], W[2 * i][k], 0x07060302u);
#pragma unroll
          for (int bb = 0; bb < 4; ++bb) { qa[bb * 8 + 2 * k] = dot2(lo, xp[bb], qa[bb * 8 + 2 * k]); qa[bb * 8 + 2 * k + 1] = dot2(hi, xp[bb], qa[bb * 8 + 2 * k + 1]); }
        }
        __builtin_amdgcn_sched_barrier(0);
      }
#pragma unroll
      for (int bb = 0; bb < 4; ++bb) ldf[L_QR + hw * 256 + bb * 64 + c8 * 8 + (lane >> 3)] = scatter8(qa + bb * 8, lane & 8);
      helper_bar(lds + L_CNT, 4, lane);
      if (QP3) {
        if (hw == 0) {
          f32x4 a = *(LAS const f32x4*)(ldf + L_QR + 4 * lane);
#pragma unroll
          for (int ww = 1; ww < 4; ++ww) a += *(LAS const f32x4*)(ldf + L_QR + ww * 256 + 4 * lane);
          st_sc(wsr0, WS_QP + ((h * 4 + (lane >> 4)) * 96 + cq * 32 + (lane & 15) * 2) * 8, u32x4{pk2(a[0], a[1]), tg.t32, pk2(a[2], a[3]), tg.t32});
        }
      } else if (hw < 2) {
        f32x2 a = *(LAS const f32x2*)(ldf + L_QR + 2 * hid);
#pragma unroll
        for (int ww = 1; ww < 4; ++ww) a += *(LAS const f32x2*)(ldf + L_QR + ww * 256 + 2 * hid);
        st_sc(wsr0, WS_QP + (((h * 2 + kh) * 4 + (hid >> 5)) * DH + cq * 64 + (hid & 31) * 2) * 8, enc2(a[0], a[1], tg));
      }
    }
    STAMP(3);
    if (QP3 ? hw == 3 : hw >= 2) {
      if (QDELAY) while (now() - tstart < QDELAY) __builtin_amdgcn_s_sleep(2);
      if (QP3) {
        const unsigned o = lane < 48 ? WS_QP + (h * 4 + b) * 96 * 8 + lane * 16 : OOB, vm = lane < 48 ? ~0u : 0u;
        u32x4 g;
#pragma nounroll
        for (unsigned spin = 0; spin < SPIN_CAP; ++spin) {
          asm volatile("" ::: "memory");
          g = ld_sc(wsr0, o);
          if (__all((((g[1] ^ tg.t32) | (g[3] ^ tg.t32)) & vm) == 0)) break;
          __builtin_amdgcn_s_sleep(2);
        }
        if (lane < 48) *(LAS u32x2*)(lds + L_Q + 2 * lane) = u32x2{g[0], g[2]};
      } else {
        const int u = hid - 128;
        const unsigned o0 = u < 96 ? WS_QP + ((h * 2 * 4 + b) * DH + 2 * u) * 8 : OOB, o1 = u < 96 ? o0 + 4 * DH * 8 : OOB, vm = u < 96 ? ~0u : 0u;
        u32x4 g0, g1;
#pragma nounroll
        for (unsigned spin = 0; spin < SPIN_CAP; ++spin) {
          asm volatile("" ::: "memory");
          g0 = ld_sc(wsr0, o0); g1 = ld_sc(wsr0, o1);
          if (__all(((bad2(g0, tg) | bad2(g1, tg)) & vm) == 0)) break;
          __builtin_amdgcn_s_sleep(2);
        }
        if (u < 96) lds[L_Q + u] = pk2(dec(g0[0]) + dec(g1[0]), dec(g0[2]) + dec(g1[2]));
      }
    }
    WG_BAR();
    const rsrc_t wor = mk(wo, has_o ? WB : 0);
    u32x4 Wo[12];
#pragma unroll
    for (int i = 0; i < 12; ++i) Wo[i] = ld_nt(wor, (unsigned)rg * (D * 2) + c8 * 16, (unsigned)(hp * 2 * DH + 32 * i) * (D * 2) + co * 128);
    if (hid < 192) {
      *(LAS u32x4*)(lds + L_MIX + hid * 4) = __builtin_nontemporal_load((const u32x4*)(w1 + hid * 8));
      *(LAS u32x4*)(lds + L_MIX + 768 + hid * 4) = __builtin_nontemporal_load((const u32x4*)(b1 + hid * 8));
      *(LAS u32x4*)(lds + L_MIX + 1536 + hid * 4) = __builtin_nontemporal_load((const u32x4*)(w2 + hid * 8));
    }
    if (hid < 80) *(LAS u32x4*)(lds + L_X + hid * 4) = __builtin_nontemporal_load((const u32x4*)(x + b2i * D + hid * 8));
    if (hid == 80) lds[L_X + 320] = b2[0];
#pragma unroll
    for (int i = 0; i < 12; ++i) *(LAS u32x4*)(lds + L_WO + (rg + 32 * i) * 36 + c8 * 4) = Wo[i];
    helper_bar(lds + L_CNT, has_q ? 8 : 4, lane);
    LAS const unsigned short* wl = (LAS const unsigned short*)(lds + L_WO) + (lane >> 4) * 8 * 72 + hw * 16 + (lane & 15);
#pragma unroll
    for (int ks = 0; ks < 12; ++ks) {
      u32x4 fr;
#pragma unroll
      for (int e = 0; e < 4; ++e) fr[e] = (unsigned)wl[(ks * 32 + 2 * e) * 72] | ((unsigned)wl[(ks * 32 + 2 * e + 1) * 72] << 16);
      Bf[ks] = __builtin_bit_cast(bf16x8, fr);
    }
  }
  const unsigned long long E1 = ((unsigned long long)__builtin_amdgcn_readfirstlane(ep[1]) << 32 | (unsigned)__builtin_amdgcn_readfirstlane(ep[0])) + 1;
  const Tag tg{(unsigned)E1 & 0xffu, (unsigned)(E1 >> 8), (unsigned)E1};
  const int u = tid - 256, uu = u < 96 ? u : 96;
  unsigned a_p1 = u <= 96 ? WS_P1 + (h * 8 + j) * P1REC + u * 16 : OOB;
  unsigned a_p2 = (lane & 1) ? OOB : WS_P2 + (((lane >> 4) * 16 + hp) * D + co * 64 + (w - 4) * 16 + (lane & 15)) * 8;
  LAS const float* a_rec = ldf + L_REC + 2 * (w < 4 ? 0 : uu);
  LAS const unsigned* a_o = lds + L_O + (lane & 3) * OSTR + (lane >> 4) * 4;
  LAS const unsigned* own = lds + L_HH + cg * 5 + (lane < 5 ? lane : 0);
  asm volatile("" : "+v"(a_p1), "+v"(a_p2), "+v"(a_rec), "+v"(a_o), "+v"(own));
  const rsrc_t wsr = wsr0;
  WG_BAR();
  STAMP(2);
  if (w == 4 || w == 5) {
    float mm[4];
    f32x2 rv[4];
#pragma unroll
    for (int i = 0; i < 4; ++i) { mm[i] = ldf[L_REC + i * 196 + 192]; rv[i] = *(LAS const f32x2*)(a_rec + i * 196); }
    __builtin_amdgcn_sched_barrier(0);
    const float M = fmaxf(fmaxf(mm[0], mm[1]), fmaxf(mm[2], mm[3]));
    f32x2 a = {0.f, 0.f};
#pragma unroll
    for (int i = 0; i < 4; ++i) a += __builtin_amdgcn_exp2f((mm[i] - M) * L2E) * rv[i];
    st_sc(wsr, a_p1, enc2(u < 96 ? a[0] : M, a[1], tg));
    STAMP(3);
  }
  float A0[3], A1[3], A2[3], A3[3], B0[3], B1[3];
  {
    LAS const unsigned short* mx = (LAS const unsigned short*)(lds + L_MIX);
    constexpr float K0 = -2.f * L2E * 0.7978845608028654f, K1 = K0 * 0.044715f;
#pragma unroll
    for (int i = 0; i < 3; ++i) {
      const float a = bflo(mx[tid + 512 * i]), c = bflo(mx[F + tid + 512 * i]), e = bflo(mx[2 * F + tid + 512 * i]);
      A3[i] = K1 * a * a * a; A2[i] = 3.f * K1 * a * a * c; A1[i] = a * fmaf(3.f * K1 * c, c, K0); A0[i] = c * fmaf(K1 * c, c, K0);
      B1[i] = a * e; B0[i] = c * e;
      asm volatile("" : "+v"(A0[i]), "+v"(A1[i]), "+v"(A2[i]), "+v"(A3[i]), "+v"(B0[i]), "+v"(B1[i]));
    }
  }
  if (has_o) {
    {
      const unsigned oa = WS_P1 + (hp * 16 + (w >> 2) * 8 + 2 * (w & 3)) * P1REC + lane * 16, ob = lane < 33 ? oa + 1024 : OOB;
      const unsigned vm = lane < 33 ? ~0u : 0u;
      LAS unsigned* da = lds + L_O + (w & 3) * OSTR + (w >> 2) * 96 + lane;
      LAS unsigned* db = lane < 32 ? da + 64 : lds + L_QR + lane;
      asm volatile("" : "+v"(da), "+v"(db));
      u32x4 a[2], c[2];
      unsigned pend = 3;
#pragma nounroll
      for (unsigned spin = 0; spin < SPIN_CAP; ++spin) {
        asm volatile("" ::: "memory");
#pragma unroll
        for (int i = 0; i < 2; ++i) if (pend >> i & 1) { a[i] = ld_sc(wsr, oa, i * P1REC); c[i] = ld_sc(wsr, ob, i * P1REC); }
        asm volatile("s_waitcnt vmcnt(0)" ::: "memory");
#pragma unroll
        for (int i = 0; i < 2; ++i) if (pend >> i & 1) { if (__all((bad2(a[i], tg) | (bad2(c[i], tg) & vm)) == 0)) pend &= ~(1u << i); }
        if (!pend) break;
        __builtin_amdgcn_s_sleep(H1SLP);
      }
      if (w < 4) STAMP(4);
      const float m0 = dec(__builtin_amdgcn_readlane(c[0][0], 32)), m1 = dec(__builtin_amdgcn_readlane(c[1][0], 32)), M = fmaxf(m0, m1);
      const float l0 = dec(__builtin_amdgcn_readlane(c[0][2], 32)), l1 = dec(__builtin_amdgcn_readlane(c[1][2], 32));
      float w0 = __builtin_amdgcn_exp2f((m0 - M) * L2E), w1_ = __builtin_amdgcn_exp2f((m1 - M) * L2E);
      const float inv = __builtin_amdgcn_rcpf(fmaf(w0, l0, w1_ * l1));
      w0 *= inv; w1_ *= inv;
      const f32x2 oa_ = __builtin_elementwise_fma(f32x2{dec(a[0][0]), dec(a[0][2])}, (f32x2)w0, f32x2{dec(a[1][0]), dec(a[1][2])} * w1_);
      const f32x2 oc_ = __builtin_elementwise_fma(f32x2{dec(c[0][0]), dec(c[0][2])}, (f32x2)w0, f32x2{dec(c[1][0]), dec(c[1][2])} * w1_);
      *da = pk2(oa_[0], oa_[1]); *db = pk2(oc_[0], oc_[1]);
    }
    WG_BAR();
    if (w >= 4) {
      u32x4 af[12];
#pragma unroll
      for (int ks = 0; ks < 12; ++ks) af[ks] = *(LAS const u32x4*)(a_o + ks * 16);
      __builtin_amdgcn_sched_barrier(0);
      f32x4 d0 = {0.f, 0.f, 0.f, 0.f}, d1 = d0;
#pragma unroll
      for (int ks = 0; ks < 12; ks += 2) {
        d0 = __builtin_amdgcn_mfma_f32_16x16x32_bf16(__builtin_bit_cast(bf16x8, af[ks]), Bf[ks], d0, 0, 0, 0);
        d1 = __builtin_amdgcn_mfma_f32_16x16x32_bf16(__builtin_bit_cast(bf16x8, af[ks + 1]), Bf[ks + 1], d1, 0, 0, 0);
      }
      d0 += d1;
      const bool g1 = lane & 16, g2 = lane & 32;
      const float a01 = g1 ? d0[1] : d0[0], a23 = g1 ? d0[3] : d0[2], a = g2 ? a23 : a01;
      st_sc(wsr, a_p2, enc2(a, dpp<XOR1>(a), tg));
      STAMP(4);
    }
  }
  {
    const unsigned o = WS_P2 + (b2i * 16 + 2 * w) * D * 8 + lane * 16;
    LAS f32x2* dy = (LAS f32x2*)(ldf + L_Y) + w * 320 + lane;
    asm volatile("" : "+v"(dy));
    u32x4 g[2][5];
    unsigned pend = 3;
#pragma nounroll
    for (unsigned spin = 0; spin < SPIN_CAP; ++spin) {
      asm volatile("" ::: "memory");
#pragma unroll
      for (int i = 0; i < 2; ++i) if (pend >> i & 1) {
#pragma unroll
        for (int k = 0; k < 5; ++k) g[i][k] = ld_sc(wsr, o + k * 1024, i * D * 8);
      }
      asm volatile("s_waitcnt vmcnt(0)" ::: "memory");
#pragma unroll
      for (int i = 0; i < 2; ++i) if (pend >> i & 1) {
        if (__all((bad2(g[i][0], tg) | bad2(g[i][1], tg) | bad2(g[i][2], tg) | bad2(g[i][3], tg) | bad2(g[i][4], tg)) == 0)) pend &= ~(1u << i);
      }
      if (!pend) break;
      __builtin_amdgcn_s_sleep(H2SLP);
    }
    STAMP(5);
#pragma unroll
    for (int k = 0; k < 5; ++k) dy[64 * k] = f32x2{dec(g[0][k][0]), dec(g[0][k][2])} + f32x2{dec(g[1][k][0]), dec(g[1][k][2])};
  }
  f32x2 xr = up2(lds[L_X + (tid < 320 ? tid : 0)]);
  LAS const f32x2* sy = (LAS const f32x2*)(ldf + L_Y) + tid;
  asm volatile("" : "+v"(xr), "+v"(sy));
  WG_BAR();
  if (w < 5) {
    f32x2 yy[8];
#pragma unroll
    for (int i = 0; i < 8; ++i) yy[i] = sy[320 * i];
    __builtin_amdgcn_sched_barrier(0);
    const f32x2 y0 = (yy[0] + yy[1]) + (yy[2] + yy[3]), y1 = (yy[4] + yy[5]) + (yy[6] + yy[7]);
    const f32x2 hs = xr + rbf2(y0 + y1);
    const unsigned hp2 = pk2(hs[0], hs[1]);
    lds[L_HH + tid] = hp2;
    const f32x2 hv = up2(hp2);
    const float piv = __uint_as_float(__builtin_amdgcn_readfirstlane(__float_as_uint(hv[0])));
    const f32x2 d = hv - piv, dd = d * d;
    const float sp = sw32(d[0] + d[1], dd[0] + dd[1]), sq = sum16(sw16(sp, sp));
    const float s1 = __uint_as_float(__builtin_amdgcn_readlane(__float_as_uint(sq), 0)), s2 = __uint_as_float(__builtin_amdgcn_readlane(__float_as_uint(sq), 32));
    *(LAS f32x2*)(ldf + L_ST + 2 * w) = f32x2{fmaf(s1, 1.f / 128, piv), fmaf(-s1, s1 * (1.f / 128), s2)};
  }
  WG_BAR();
  {
    const f32x4 sa = *(LAS const f32x4*)(ldf + L_ST), sb = *(LAS const f32x4*)(ldf + L_ST + 4);
    const f32x2 sc = *(LAS const f32x2*)(ldf + L_ST + 8);
    const unsigned hw2 = *own;
    __builtin_amdgcn_sched_barrier(0);
    const f32x2 hv = up2(hw2);
    const float mean = (((sa[0] + sa[2]) + (sb[0] + sb[2])) + sc[0]) * 0.2f;
    const float e0 = sa[0] - mean, e1 = sa[2] - mean, e2 = sb[0] - mean, e3 = sb[2] - mean, e4 = sc[0] - mean;
    const float var = fmaf(fmaf(e0, e0, e1 * e1) + fmaf(e2, e2, e3 * e3) + e4 * e4, 0.2f, (((sa[1] + sa[3]) + (sb[1] + sb[3])) + sc[1]) * (1.f / D));
    const float inv = inv_sd(var), mb = rbf(mean);
    const float inv2 = inv_sd(var * inv * inv), mb2 = rbf((mean - mb) * inv);
    const f32x2 tv = rbf2(rbf2(rbf2(rbf2(hv - mb) * inv) - mb2) * inv2);
    STAMP(6);
    float ac[10];
#pragma unroll
    for (int i = 0; i < 5; ++i) {
      const f32x2 t = {__uint_as_float(__builtin_amdgcn_readlane(__float_as_uint(tv[0]), i)), __uint_as_float(__builtin_amdgcn_readlane(__float_as_uint(tv[1]), i))};
      f32x2 a = {0.f, 0.f};
#pragma unroll
      for (int k = 0; k < 3; ++k) {
        const f32x2 z = __builtin_elementwise_fma(__builtin_elementwise_fma(__builtin_elementwise_fma(t, (f32x2)A3[k], (f32x2)A2[k]), t, (f32x2)A1[k]), t, (f32x2)A0[k]);
        const f32x2 d = f32x2{__builtin_amdgcn_exp2f(z[0]), __builtin_amdgcn_exp2f(z[1])} + 1.f;
        a = __builtin_elementwise_fma(__builtin_elementwise_fma(t, (f32x2)B1[k], (f32x2)B0[k]), f32x2{__builtin_amdgcn_rcpf(d[0]), __builtin_amdgcn_rcpf(d[1])}, a);
      }
      ac[2 * i] = a[0]; ac[2 * i + 1] = a[1];
    }
    float y[5];
#pragma unroll
    for (int i = 0; i < 5; ++i) y[i] = sw32(ac[i], ac[i + 5]);
    const float z0 = sum16(sw16(y[0], y[1])), z1 = sum16(sw16(y[2], y[3])), z2 = sum16(sw16(y[4], y[4]));
    if ((lane & 15) == 0) {
      const int o = L_RED + w * 16 + (lane >> 5) * 5, odd = (lane >> 4) & 1;
      ldf[o + odd] = z0; ldf[o + 2 + odd] = z1; ldf[o + 4] = z2;
    }
  }
  const unsigned hp2 = lds[L_HH + cg * 5 + ((lane >> 1) & 7) % 5];
  float hres = (lane & 1) ? bfhi(hp2) : bflo(hp2), bias = bflo(lds[L_X + 320]);
  unsigned a_out = lane < 10 ? (b2i * D + cg * 10 + lane) * 2 : OOB;
  asm volatile("" : "+v"(hres), "+v"(bias), "+v"(a_out));
  WG_BAR();
  if (w == 0) {
    float yy[8];
#pragma unroll
    for (int i = 0; i < 8; ++i) yy[i] = ldf[L_RED + i * 16 + (lane & 15)];
    const float y = ((yy[0] + yy[1]) + (yy[2] + yy[3])) + ((yy[4] + yy[5]) + (yy[6] + yy[7]));
    const __bf16 o = (__bf16)(hres + rbf(rbf(y) + bias));
    __builtin_amdgcn_raw_buffer_store_b16(__builtin_bit_cast(unsigned short, o), mk(out, 4 * D * 2), a_out, 0, 0);
  }
  if (bid == 0 && tid == 64) __builtin_amdgcn_raw_buffer_store_b64(u32x2{(unsigned)E1, (unsigned)(E1 >> 32)}, wsr, 0, 0, 16);
}

extern "C" void kernel_launch(void* const* d_in, const int* in_sizes, int n_in, void* d_out, int out_size, void* d_ws, size_t ws_size,
                              hipStream_t stream) {
  typedef const unsigned short* P;
  fused_decode<<<256, 512, 0, stream>>>((P)d_in[0], (P)d_in[1], (P)d_in[2], (P)d_in[3], (P)d_in[4], (P)d_in[6], (P)d_in[7], (P)d_in[8], (P)d_in[9],
                                        (unsigned short*)d_out, (unsigned char*)d_ws);
}
```

```cpp
#include <hip/hip_runtime.h>
#include <stdint.h>

typedef unsigned u32x4 __attribute__((ext_vector_type(4)));
typedef unsigned u32x2 __attribute__((ext_vector_type(2)));
typedef float f32x4 __attribute__((ext_vector_type(4)));
typedef float f32x2 __attribute__((ext_vector_type(2)));
typedef __bf16 bf16x2 __attribute__((ext_vector_type(2)));
typedef __bf16 bf16x8 __attribute__((ext_vector_type(8)));
typedef __amdgpu_buffer_rsrc_t rsrc_t;
#define LAS __attribute__((address_space(3)))

#define NPRE 6
#define WQ_WAIT 25
#define QDELAY 0
#define EDEP 3
#define EKFIRST 0
#define SDELAY 0
#define WQMODE 2
#define NOQ 0
#define SHIFT 0
#define ROTX (h * 8 + j)
#define H1SLP 1
#define H2SLP 1
#define STR2(x) #x
#define STR(x) STR2(x)

constexpr int D = 640, H = 32, SK = 4096, DH = 192, HD = H * DH, F = 1536;
constexpr unsigned SEQB = SK * DH * 2, TILEB = 8 * DH * 2, KVB = 4u * H * SEQB, WB = D * HD * 2;
constexpr unsigned P1REC = 208 * 8;
constexpr unsigned WS_QP = 4096, WS_P1 = WS_QP + H * 4 * 96 * 8, WS_P2 = WS_P1 + 256 * P1REC, WS_END = WS_P2 + 4 * 16 * D * 8;
constexpr unsigned OOB = 0x7ffffff0u;
constexpr unsigned SPIN_CAP = 1u << 17;
constexpr float L2E = 1.4426950408889634f, SCALE = 0.07216878364870322f;
constexpr int OSTR = 208;
constexpr int L_CNT = 0, L_Q = 8, L_REC = L_Q + 96, L_O = L_REC + 4 * 196, L_RED = L_O + 4 * OSTR, L_ST = L_RED + 8 * 16, L_X = L_ST + 16, L_HH = L_X + 324,
              L_XN = L_HH + 320, L_QR = L_XN + 16 * 132, L_MIX = L_QR + 4 * 256, L_WO = L_MIX + 3 * F / 2, L_Y = L_WO, L_END = L_WO + 2 * DH * 36;
constexpr int LDS_DW = 22528;
static_assert(L_END <= LDS_DW && L_REC % 4 == 0 && L_O % 4 == 0 && L_XN % 4 == 0 && L_QR % 4 == 0 && L_MIX % 4 == 0 && L_WO % 4 == 0, "lds map");

__device__ __forceinline__ float bflo(unsigned w) { return __uint_as_float(w << 16); }
__device__ __forceinline__ float bfhi(unsigned w) { return __uint_as_float(w & 0xffff0000u); }
__device__ __forceinline__ unsigned pk2(float lo, float hi) { unsigned r; asm("v_cvt_pk_bf16_f32 %0, %1, %2" : "=v"(r) : "v"(lo), "v"(hi)); return r; }
__device__ __forceinline__ float rbf(float f) { return (float)(__bf16)f; }
__device__ __forceinline__ f32x2 up2(unsigned w) { return f32x2{bflo(w), bfhi(w)}; }
__device__ __forceinline__ f32x2 rbf2(f32x2 v) { return up2(pk2(v[0], v[1])); }
__device__ __forceinline__ float dot2(unsigned a, unsigned b, float c) {
  return __builtin_amdgcn_fdot2_f32_bf16(__builtin_bit_cast(bf16x2, a), __builtin_bit_cast(bf16x2, b), c, false);
}
template <int CTRL> __device__ __forceinline__ float dpp(float x) {
  return __builtin_bit_cast(float, __builtin_amdgcn_mov_dpp(__builtin_bit_cast(int, x), CTRL, 0xf, 0xf, true));
}
constexpr int XOR1 = 0xB1, XOR2 = 0x4E, HMIR = 0x141, ROR8 = 0x128;
__device__ __forceinline__ float sw32(float a, float b) {
  const auto r = __builtin_amdgcn_permlane32_swap(__float_as_uint(a), __float_as_uint(b), false, false);
  return __uint_as_float(r[0]) + __uint_as_float(r[1]);
}
__device__ __forceinline__ float sw16(float a, float b) {
  const auto r = __builtin_amdgcn_permlane16_swap(__float_as_uint(a), __float_as_uint(b), false, false);
  return __uint_as_float(r[0]) + __uint_as_float(r[1]);
}
__device__ __forceinline__ float sum16(float v) { v += dpp<XOR1>(v); v += dpp<XOR2>(v); v += dpp<HMIR>(v); v += dpp<ROR8>(v); return v; }
__device__ __forceinline__ float sum64(float v) { v = sum16(v); v = sw16(v, v); return sw32(v, v); }
__device__ __forceinline__ float scatter8(const float* a, bool b3) {
  float y[4], z[2];
#pragma unroll
  for (int e = 0; e < 4; ++e) y[e] = sw32(a[e], a[e + 4]);
#pragma unroll
  for (int e = 0; e < 2; ++e) z[e] = sw16(y[e], y[e + 2]);
  return (b3 ? z[1] : z[0]) + dpp<ROR8>(b3 ? z[0] : z[1]);
}
typedef short s16x4 __attribute__((ext_vector_type(4)));
__device__ __forceinline__ bf16x8 tr8(LAS const unsigned* p, int step) {
  const s16x4 a = __builtin_amdgcn_ds_read_tr16_b64_v4i16((LAS s16x4*)p), b = __builtin_amdgcn_ds_read_tr16_b64_v4i16((LAS s16x4*)(p + step));
  return __builtin_bit_cast(bf16x8, __builtin_shufflevector(a, b, 0, 1, 2, 3, 4, 5, 6, 7));
}
__device__ __forceinline__ rsrc_t mk(const void* p, unsigned n) { return __builtin_amdgcn_make_buffer_rsrc((void*)p, 0, n, 0x00020000); }
__device__ __forceinline__ u32x4 ld_nt(rsrc_t r, unsigned v, unsigned s) { return __builtin_amdgcn_raw_buffer_load_b128(r, v, s, 2); }
__device__ __forceinline__ u32x4 ld_sc(rsrc_t r, unsigned v, unsigned s = 0) { return __builtin_amdgcn_raw_buffer_load_b128(r, v, s, 16); }
__device__ __forceinline__ void st_sc(rsrc_t r, unsigned v, u32x4 x) { __builtin_amdgcn_raw_buffer_store_b128(x, r, v, 0, 16); }
__device__ __forceinline__ unsigned now() { return (unsigned)__builtin_amdgcn_s_memrealtime(); }
__device__ __forceinline__ unsigned lds_peek(LAS unsigned* p) {
  return __builtin_amdgcn_readfirstlane(__hip_atomic_load(p, __ATOMIC_RELAXED, __HIP_MEMORY_SCOPE_WORKGROUP));
}
#define PIN() do { asm volatile("" ::: "memory"); __builtin_amdgcn_sched_barrier(0); } while (0)
#define WG_BAR() do { asm volatile("s_waitcnt lgkmcnt(0)" ::: "memory"); __builtin_amdgcn_s_barrier(); PIN(); } while (0)
#define STAMP(i) do { } while (0)
struct Tag { unsigned lo, hi, t32; };
__device__ __forceinline__ u32x4 enc2(float a, float b, const Tag& t) {
  return u32x4{(__float_as_uint(a) & 0xffffff00u) | t.lo, t.hi, (__float_as_uint(b) & 0xffffff00u) | t.lo, t.hi};
}
__device__ __forceinline__ unsigned bad2(u32x4 g, const Tag& t) { return (((g[0] ^ t.lo) | (g[2] ^ t.lo)) & 0xffu) | (g[1] ^ t.hi) | (g[3] ^ t.hi); }
__device__ __forceinline__ float dec(unsigned g) { return __uint_as_float(g & 0xffffff00u); }
__device__ __forceinline__ void helper_bar(LAS unsigned* cnt, unsigned target, int lane) {
  asm volatile("s_waitcnt lgkmcnt(0)" ::: "memory");
  if (lane == 0) __hip_atomic_fetch_add(cnt, 1u, __ATOMIC_RELAXED, __HIP_MEMORY_SCOPE_WORKGROUP);
  while (lds_peek(cnt) < target) { }
  asm volatile("" ::: "memory");
}
__device__ __forceinline__ float inv_sd(float var) { return __builtin_amdgcn_rcpf(rbf(__builtin_amdgcn_sqrtf(rbf(rbf(var) + 1e-5f)))); }

__global__ void __launch_bounds__(512, 2)
fused_decode(const unsigned short* x, const unsigned short* kc, const unsigned short* vc, const unsigned short* wq, const unsigned short* wo,
             const unsigned short* w1, const unsigned short* b1, const unsigned short* w2, const unsigned short* b2, unsigned short* out,
             unsigned char* ws) {
  __shared__ __attribute__((aligned(16))) unsigned lds_raw[LDS_DW];
  LAS unsigned* lds = (LAS unsigned*)lds_raw;
  LAS float* ldf = (LAS float*)lds_raw;
  const int tid = threadIdx.x, lane = tid & 63, w = __builtin_amdgcn_readfirstlane(tid >> 6);
  const int bid = blockIdx.x, h = bid >> 3, j = (bid + h) & 7, b = j >> 1, s = j & 1;
  const bool has_q = j >= 2 && s == 1, has_o = j < 2 || s == 0;
  const int hp = h >> 1, co = (h & 1) * 5 + (j < 2 ? j : b + 1);
  const int b2i = bid >> 6, cg = bid & 63;
  STAMP(0);
  const unsigned tstart = (QDELAY | SDELAY) ? now() : 0u;
  asm volatile("" :: "s"(x), "s"(kc), "s"(vc), "s"(wq), "s"(wo), "s"(w1), "s"(b1), "s"(w2), "s"(b2), "s"(out), "s"(ws));
  const rsrc_t wsr0 = mk(ws, WS_END);
  u32x2 ep = __builtin_amdgcn_raw_buffer_load_b64(wsr0, 0, 0, 16);
  bf16x8 Bf[12];

  if (w < 4) {
    const int r = lane >> 3, c = lane & 7;
    const unsigned voff = r * 384 + c * 16;
    const int ntw = 256 + (j < 2 ? 0 : (s ? -SHIFT : SHIFT)), NT = (ntw + 3 - w) >> 2;
    const unsigned seq = (unsigned)(b * H + h) * SEQB + (s ? 512 - ntw : 0) * TILEB;
    const int rot = (ROTX) % ntw;
    auto toff = [&](int i) -> unsigned { const int y = i * 4 + w + rot; return seq + (unsigned)(y >= ntw ? y - ntw : y) * TILEB; };
    const rsrc_t kr = mk(kc, KVB), vr = mk(vc, KVB);
    u32x4 K[NPRE][3], V[NPRE][3];
    WG_BAR();
    if (SDELAY) while (now() - tstart < SDELAY) __builtin_amdgcn_s_sleep(1);
#pragma unroll
    for (int p = 0; p < NPRE; ++p) {
      if (p) asm volatile("s_waitcnt vmcnt(3)" ::: "memory");
#pragma unroll
      for (int i = 0; i < 3; ++i) K[p][i] = ld_nt(kr, voff + 128 * i, toff(p));
      PIN();
      if (p) asm volatile("s_waitcnt vmcnt(3)" ::: "memory");
#pragma unroll
      for (int i = 0; i < 3; ++i) V[p][i] = ld_nt(vr, voff + 128 * i, toff(p));
      PIN();
    }
    WG_BAR();
    STAMP(1);
    u32x4 qf[3];
#pragma unroll
    for (int i = 0; i < 3; ++i) qf[i] = *(LAS const u32x4*)(lds + L_Q + (c + 8 * i) * 4);
    float m = __uint_as_float(0xff7f0000u), l = 0.f, acc[24];
#pragma unroll
    for (int k = 0; k < 24; ++k) acc[k] = 0.f;
    auto step_fn = [&](u32x4 (&Kc)[3], u32x4 (&Vc)[3], bool refill, unsigned nbytes, unsigned soff) {
      float a0 = 0.f, a1 = 0.f, a2 = 0.f;
#pragma unroll
      for (int k = 0; k < 4; ++k) { a0 = dot2(Kc[0][k], qf[0][k], a0); a1 = dot2(Kc[1][k], qf[1][k], a1); a2 = dot2(Kc[2][k], qf[2][k], a2); }
      float a = (a0 + a1) + a2;
      PIN();
      if (refill) {
        const rsrc_t k2 = mk(kc, nbytes);
#pragma unroll
        for (int i = 0; i < 3; ++i) Kc[i] = ld_nt(k2, voff + 128 * i, soff);
      }
      PIN();
      a += dpp<XOR1>(a); a += dpp<XOR2>(a); a += dpp<HMIR>(a);
      const float sc = rbf(rbf(a) * SCALE);
      const float mn = fmaxf(m, sc);
      const float alpha = __builtin_amdgcn_exp2f((m - mn) * L2E), p = __builtin_amdgcn_exp2f((sc - mn) * L2E);
      l = fmaf(l, alpha, p); m = mn;
#pragma unroll
      for (int i = 0; i < 3; ++i)
#pragma unroll
        for (int k = 0; k < 4; ++k) {
          acc[i * 8 + 2 * k]     = fmaf(p, bflo(Vc[i][k]), acc[i * 8 + 2 * k] * alpha);
          acc[i * 8 + 2 * k + 1] = fmaf(p, bfhi(Vc[i][k]), acc[i * 8 + 2 * k + 1] * alpha);
        }
      PIN();
      if (refill) {
        const rsrc_t v2 = mk(vc, nbytes);
#pragma unroll
        for (int i = 0; i < 3; ++i) Vc[i] = ld_nt(v2, voff + 128 * i, soff);
      }
      PIN();
    };
#pragma unroll
    for (int p = 0; p < NPRE; ++p) step_fn(K[p], V[p], p == 0, KVB, toff(NPRE));
    int t = NPRE + 1;
    do { step_fn(K[0], V[0], true, EDEP > 1 || t < NT ? KVB : 0u, toff(t)); } while (++t <= NT - (EDEP > 1 ? EDEP : 0));
    if (EDEP > 1) {
#pragma unroll
      for (int p = 1; p < EDEP; ++p) {
#pragma unroll
        for (int i = 0; i < 3; ++i) K[p][i] = ld_nt(kr, voff + 128 * i, toff(NT - EDEP + p));
        PIN();
        if (!EKFIRST) {
#pragma unroll
          for (int i = 0; i < 3; ++i) V[p][i] = ld_nt(vr, voff + 128 * i, toff(NT - EDEP + p));
          PIN();
        }
      }
      if (EKFIRST) {
#pragma unroll
        for (int p = 1; p < EDEP; ++p) {
#pragma unroll
          for (int i = 0; i < 3; ++i) V[p][i] = ld_nt(vr, voff + 128 * i, toff(NT - EDEP + p));
          PIN();
        }
      }
#pragma unroll
      for (int p = 0; p < EDEP; ++p) step_fn(K[p], V[p], false, 0, 0);
    }
    float M = fmaxf(m, dpp<ROR8>(m));
    { const auto q16 = __builtin_amdgcn_permlane16_swap(__float_as_uint(M), __float_as_uint(M), false, false);
      M = fmaxf(__uint_as_float(q16[0]), __uint_as_float(q16[1]));
      const auto q32 = __builtin_amdgcn_permlane32_swap(__float_as_uint(M), __float_as_uint(M), false, false);
      M = fmaxf(__uint_as_float(q32[0]), __uint_as_float(q32[1])); }
    const float f = __builtin_amdgcn_exp2f((m - M) * L2E);
    l *= f; l += dpp<ROR8>(l); l = sw16(l, l); l = sw32(l, l);
#pragma unroll
    for (int k = 0; k < 24; ++k) acc[k] *= f;
#pragma unroll
    for (int i = 0; i < 3; ++i) ldf[L_REC + w * 196 + i * 64 + c * 8 + r] = scatter8(acc + i * 8, lane & 8);
    if (lane == 0) { ldf[L_REC + w * 196 + 192] = M; ldf[L_REC + w * 196 + 193] = l; }
  } else {
    const int hid = tid - 256, hw = w - 4;
    if (hid < 8) lds[L_CNT + hid] = 0;
    const int cq = (j - 2) >> 1, rg = hid >> 3, c8 = hid & 7;
    u32x4 W[20], X[5];
    const rsrc_t xr = mk(x, has_q ? 4 * D * 2 : 0), wqr = mk(wq, has_q ? WB : 0);
    unsigned warm = 0;
    if (WQMODE == 2) warm = __builtin_amdgcn_raw_buffer_load_b32(wqr, lane < 32 ? (unsigned)(160 * hw + 5 * lane) * (HD * 2) + (h * DH + cq * 64) * 2 : OOB, 0, 2);
    if (WQMODE == 4 && hw < 2) warm = __builtin_amdgcn_raw_buffer_load_b32(wqr, (unsigned)(320 * hw + 5 * lane) * (HD * 2) + (h * DH + cq * 64) * 2, 0, 2);
    PIN();
#pragma unroll
    for (int i = 0; i < 5; ++i) { const int ch = 20 * hw + (lane & 15) + 16 * i; X[i] = ld_nt(xr, (lane >> 4) * (D * 2) + (ch >= 80 ? ch - 80 : ch) * 16, 0); }
    PIN();
#pragma unroll
    for (int i = 0; i < 20; ++i) {
      if (WQMODE == 1) W[i] = ld_nt(wqr, lane * 16, (unsigned)((h * 3 + cq) * 80 + hw * 20 + i) * 1024);
      else if (WQMODE == 3) W[i] = ld_nt(wqr, (unsigned)(lane >> 3) * (HD * 2) + c8 * 16, (unsigned)(8 * hw + 32 * i) * (HD * 2) + (h * DH + cq * 64) * 2);
      else W[i] = ld_nt(wqr, (unsigned)(lane >> 3) * (HD * 2) + c8 * 16, (unsigned)(160 * hw + 8 * i) * (HD * 2) + (h * DH + cq * 64) * 2);
      if ((i & 3) == 3) PIN();
    }
    if (WQ_WAIT < 25) asm volatile("s_waitcnt vmcnt(" STR(WQ_WAIT) ")" ::: "memory");
    WG_BAR();
    asm volatile("" : "+v"(ep), "+v"(warm));
    const unsigned long long E1 = ((unsigned long long)__builtin_amdgcn_readfirstlane(ep[1]) << 32 | (unsigned)__builtin_amdgcn_readfirstlane(ep[0])) + 1;
    const Tag tg{(unsigned)E1 & 0xffu, (unsigned)(E1 >> 8), (unsigned)E1};
    if (has_q) {
      float sa = 0.f, sb = 0.f;
#pragma unroll
      for (int i = 0; i < 20; i += 2) { sa = dot2(X[i >> 2][i & 3], 0x3f803f80u, sa); sb = dot2(X[i >> 2][(i & 3) + 1], 0x3f803f80u, sb); }
      const float mean = sum16(sa + sb) * (1.f / D);
      f32x2 s2 = {0.f, 0.f}, s3 = s2;
#pragma unroll
      for (int i = 0; i < 20; i += 2) {
        const f32x2 d = up2(X[i >> 2][i & 3]) - mean, e = up2(X[i >> 2][(i & 3) + 1]) - mean;
        s2 = __builtin_elementwise_fma(d, d, s2); s3 = __builtin_elementwise_fma(e, e, s3);
      }
      s2 += s3;
      const float inv = inv_sd(sum16(s2[0] + s2[1]) * (1.f / D)), mb = rbf(mean);
      LAS unsigned* xn = lds + L_XN + hw * 4 * 132;
#pragma unroll
      for (int i = 0; i < 2; ++i) {
        u32x4 o;
#pragma unroll
        for (int k = 0; k < 4; ++k) { const f32x2 y = rbf2(up2(X[i][k]) - mb) * inv; o[k] = pk2(y[0], y[1]); }
        *(LAS u32x4*)(xn + (lane >> 4) * 132 + ((lane & 15) + 16 * i) * 4) = o;
      }
      asm volatile("" ::: "memory");
      u32x4 af[5];
#pragma unroll
      for (int ks = 0; ks < 5; ++ks) af[ks] = *(LAS const u32x4*)(xn + (lane & 3) * 132 + ks * 16 + (lane >> 4) * 4);
      LAS unsigned* st = lds + L_WO + hw * 64 * 36;
      f32x4 dq[4];
#pragma unroll
      for (int nt = 0; nt < 4; ++nt) dq[nt] = f32x4{0.f, 0.f, 0.f, 0.f};
#pragma unroll
      for (int ks = 0; ks < 5; ++ks) {
        LAS unsigned* sk = st + (ks & 1) * 32 * 36;
#pragma unroll
        for (int i = 0; i < 4; ++i) *(LAS u32x4*)(sk + (8 * i + (lane >> 3)) * 36 + c8 * 4) = W[4 * ks + i];
        asm volatile("" ::: "memory");
        LAS const unsigned* rd = sk + (8 * (lane >> 4) + ((lane >> 2) & 3)) * 36 + (lane & 3) * 2;
#pragma unroll
        for (int nt = 0; nt < 4; ++nt) dq[nt] = __builtin_amdgcn_mfma_f32_16x16x32_bf16(__builtin_bit_cast(bf16x8, af[ks]), tr8(rd + nt * 8, 4 * 36), dq[nt], 0, 0, 0);
        asm volatile("" ::: "memory");
      }
      const bool g1 = lane & 16, g2 = lane & 32;
#pragma unroll
      for (int nt = 0; nt < 4; ++nt) {
        const float a01 = g1 ? dq[nt][1] : dq[nt][0], a23 = g1 ? dq[nt][3] : dq[nt][2];
        ldf[L_QR + hw * 256 + (lane >> 4) * 64 + nt * 16 + (lane & 15)] = g2 ? a23 : a01;
      }
      helper_bar(lds + L_CNT, 4, lane);
      if (hw == 0) {
        f32x4 a = *(LAS const f32x4*)(ldf + L_QR + 4 * lane);
#pragma unroll
        for (int ww = 1; ww < 4; ++ww) a += *(LAS const f32x4*)(ldf + L_QR + ww * 256 + 4 * lane);
        st_sc(wsr0, WS_QP + ((h * 4 + (lane >> 4)) * 96 + cq * 32 + (lane & 15) * 2) * 8, u32x4{pk2(a[0], a[1]), tg.t32, pk2(a[2], a[3]), tg.t32});
      }
    }
    STAMP(3);
    if (hw == 3 && !NOQ) {
      if (QDELAY) while (now() - tstart < QDELAY) __builtin_amdgcn_s_sleep(2);
      const unsigned o = lane < 48 ? WS_QP + (h * 4 + b) * 96 * 8 + lane * 16 : OOB, vm = lane < 48 ? ~0u : 0u;
      u32x4 g;
#pragma nounroll
      for (unsigned spin = 0; spin < SPIN_CAP; ++spin) {
        asm volatile("" ::: "memory");
        g = ld_sc(wsr0, o);
        if (__all((((g[1] ^ tg.t32) | (g[3] ^ tg.t32)) & vm) == 0)) break;
        __builtin_amdgcn_s_sleep(2);
      }
      if (lane < 48) *(LAS u32x2*)(lds + L_Q + 2 * lane) = u32x2{g[0], g[2]};
      STAMP(1);
    }
    WG_BAR();
    const rsrc_t wor = mk(wo, has_o ? WB : 0);
    u32x4 Wo[12];
#pragma unroll
    for (int i = 0; i < 12; ++i) Wo[i] = ld_nt(wor, (unsigned)rg * (D * 2) + c8 * 16, (unsigned)(hp * 2 * DH + 32 * i) * (D * 2) + co * 128);
    if (hid < 192) {
      *(LAS u32x4*)(lds + L_MIX + hid * 4) = __builtin_nontemporal_load((const u32x4*)(w1 + hid * 8));
      *(LAS u32x4*)(lds + L_MIX + 768 + hid * 4) = __builtin_nontemporal_load((const u32x4*)(b1 + hid * 8));
      *(LAS u32x4*)(lds + L_MIX + 1536 + hid * 4) = __builtin_nontemporal_load((const u32x4*)(w2 + hid * 8));
    }
    if (hid < 80) *(LAS u32x4*)(lds + L_X + hid * 4) = __builtin_nontemporal_load((const u32x4*)(x + b2i * D + hid * 8));
    if (hid == 80) lds[L_X + 320] = b2[0];
#pragma unroll
    for (int i = 0; i < 12; ++i) *(LAS u32x4*)(lds + L_WO + (rg + 32 * i) * 36 + c8 * 4) = Wo[i];
    helper_bar(lds + L_CNT, has_q ? 8 : 4, lane);
    LAS const unsigned* wl = lds + L_WO + (8 * (lane >> 4) + ((lane >> 2) & 3)) * 36 + hw * 8 + (lane & 3) * 2;
#pragma unroll
    for (int ks = 0; ks < 12; ++ks) Bf[ks] = tr8(wl + ks * 32 * 36, 4 * 36);
  }
  const unsigned long long E1 = ((unsigned long long)__builtin_amdgcn_readfirstlane(ep[1]) << 32 | (unsigned)__builtin_amdgcn_readfirstlane(ep[0])) + 1;
  const Tag tg{(unsigned)E1 & 0xffu, (unsigned)(E1 >> 8), (unsigned)E1};
  const int u = tid - 256, uu = u < 96 ? u : 96;
  unsigned a_p1 = u <= 96 ? WS_P1 + (h * 8 + j) * P1REC + u * 16 : OOB;
  unsigned a_p2 = (lane & 1) ? OOB : WS_P2 + (((lane >> 4) * 16 + hp) * D + co * 64 + (w - 4) * 16 + (lane & 15)) * 8;
  LAS const float* a_rec = ldf + L_REC + 2 * (w < 4 ? 0 : uu);
  LAS const unsigned* a_o = lds + L_O + (lane & 3) * OSTR + (lane >> 4) * 4;
  LAS const unsigned* own = lds + L_HH + cg * 5 + (lane < 5 ? lane : 0);
  asm volatile("" : "+v"(a_p1), "+v"(a_p2), "+v"(a_rec), "+v"(a_o), "+v"(own));
  const rsrc_t wsr = wsr0;
  WG_BAR();
  STAMP(2);
  if (w == 4 || w == 5) {
    float mm[4];
    f32x2 rv[4];
#pragma unroll
    for (int i = 0; i < 4; ++i) { mm[i] = ldf[L_REC + i * 196 + 192]; rv[i] = *(LAS const f32x2*)(a_rec + i * 196); }
    __builtin_amdgcn_sched_barrier(0);
    const float M = fmaxf(fmaxf(mm[0], mm[1]), fmaxf(mm[2], mm[3]));
    f32x2 a = {0.f, 0.f};
#pragma unroll
    for (int i = 0; i < 4; ++i) a += __builtin_amdgcn_exp2f((mm[i] - M) * L2E) * rv[i];
    st_sc(wsr, a_p1, enc2(u < 96 ? a[0] : M, a[1], tg));
    STAMP(3);
  }
  float A0[3], A1[3], A2[3], A3[3], B0[3], B1[3];
  {
    LAS const unsigned short* mx = (LAS const unsigned short*)(lds + L_MIX);
    constexpr float K0 = -2.f * L2E * 0.7978845608028654f, K1 = K0 * 0.044715f;
#pragma unroll
    for (int i = 0; i < 3; ++i) {
      const float a = bflo(mx[tid + 512 * i]), c = bflo(mx[F + tid + 512 * i]), e = bflo(mx[2 * F + tid + 512 * i]);
      A3[i] = K1 * a * a * a; A2[i] = 3.f * K1 * a * a * c; A1[i] = a * fmaf(3.f * K1 * c, c, K0); A0[i] = c * fmaf(K1 * c, c, K0);
      B1[i] = a * e; B0[i] = c * e;
      asm volatile("" : "+v"(A0[i]), "+v"(A1[i]), "+v"(A2[i]), "+v"(A3[i]), "+v"(B0[i]), "+v"(B1[i]));
    }
  }
  if (has_o) {
    {
      const unsigned oa = WS_P1 + (hp * 16 + (w >> 2) * 8 + 2 * (w & 3)) * P1REC + lane * 16, ob = lane < 33 ? oa + 1024 : OOB;
      const unsigned vm = lane < 33 ? ~0u : 0u;
      LAS unsigned* da = lds + L_O + (w & 3) * OSTR + (w >> 2) * 96 + lane;
      LAS unsigned* db = lane < 32 ? da + 64 : lds + L_QR + lane;
      asm volatile("" : "+v"(da), "+v"(db));
      u32x4 a[2], c[2];
      unsigned pend = 3;
#pragma nounroll
      for (unsigned spin = 0; spin < SPIN_CAP; ++spin) {
        asm volatile("" ::: "memory");
#pragma unroll
        for (int i = 0; i < 2; ++i) if (pend >> i & 1) { a[i] = ld_sc(wsr, oa, i * P1REC); c[i] = ld_sc(wsr, ob, i * P1REC); }
        asm volatile("s_waitcnt vmcnt(0)" ::: "memory");
#pragma unroll
        for (int i = 0; i < 2; ++i) if (pend >> i & 1) { if (__all((bad2(a[i], tg) | (bad2(c[i], tg) & vm)) == 0)) pend &= ~(1u << i); }
        if (!pend) break;
        __builtin_amdgcn_s_sleep(H1SLP);
      }
      if (w < 4) STAMP(4);
      const float m0 = dec(__builtin_amdgcn_readlane(c[0][0], 32)), m1 = dec(__builtin_amdgcn_readlane(c[1][0], 32)), M = fmaxf(m0, m1);
      const float l0 = dec(__builtin_amdgcn_readlane(c[0][2], 32)), l1 = dec(__builtin_amdgcn_readlane(c[1][2], 32));
      float w0 = __builtin_amdgcn_exp2f((m0 - M) * L2E), w1_ = __builtin_amdgcn_exp2f((m1 - M) * L2E);
      const float inv = __builtin_amdgcn_rcpf(fmaf(w0, l0, w1_ * l1));
      w0 *= inv; w1_ *= inv;
      const f32x2 oa_ = __builtin_elementwise_fma(f32x2{dec(a[0][0]), dec(a[0][2])}, (f32x2)w0, f32x2{dec(a[1][0]), dec(a[1][2])} * w1_);
      const f32x2 oc_ = __builtin_elementwise_fma(f32x2{dec(c[0][0]), dec(c[0][2])}, (f32x2)w0, f32x2{dec(c[1][0]), dec(c[1][2])} * w1_);
      *da = pk2(oa_[0], oa_[1]); *db = pk2(oc_[0], oc_[1]);
    }
    WG_BAR();
    if (w >= 4) {
      u32x4 af[12];
#pragma unroll
      for (int ks = 0; ks < 12; ++ks) af[ks] = *(LAS const u32x4*)(a_o + ks * 16);
      __builtin_amdgcn_sched_barrier(0);
      f32x4 d0 = {0.f, 0.f, 0.f, 0.f}, d1 = d0;
#pragma unroll
      for (int ks = 0; ks < 12; ks += 2) {
        d0 = __builtin_amdgcn_mfma_f32_16x16x32_bf16(__builtin_bit_cast(bf16x8, af[ks]), Bf[ks], d0, 0, 0, 0);
        d1 = __builtin_amdgcn_mfma_f32_16x16x32_bf16(__builtin_bit_cast(bf16x8, af[ks + 1]), Bf[ks + 1], d1, 0, 0, 0);
      }
      d0 += d1;
      const bool g1 = lane & 16, g2 = lane & 32;
      const float a01 = g1 ? d0[1] : d0[0], a23 = g1 ? d0[3] : d0[2], a = g2 ? a23 : a01;
      st_sc(wsr, a_p2, enc2(a, dpp<XOR1>(a), tg));
      STAMP(4);
    }
  }
  {
    const unsigned o = WS_P2 + (b2i * 16 + 2 * w) * D * 8 + lane * 16;
    LAS f32x2* dy = (LAS f32x2*)(ldf + L_Y) + w * 320 + lane;
    asm volatile("" : "+v"(dy));
    u32x4 g[2][5];
    unsigned pend = 3;
#pragma nounroll
    for (unsigned spin = 0; spin < SPIN_CAP; ++spin) {
      asm volatile("" ::: "memory");
#pragma unroll
      for (int i = 0; i < 2; ++i) if (pend >> i & 1) {
#pragma unroll
        for (int k = 0; k < 5; ++k) g[i][k] = ld_sc(wsr, o + k * 1024, i * D * 8);
      }
      asm volatile("s_waitcnt vmcnt(0)" ::: "memory");
#pragma unroll
      for (int i = 0; i < 2; ++i) if (pend >> i & 1) {
        if (__all((bad2(g[i][0], tg) | bad2(g[i][1], tg) | bad2(g[i][2], tg) | bad2(g[i][3], tg) | bad2(g[i][4], tg)) == 0)) pend &= ~(1u << i);
      }
      if (!pend) break;
      __builtin_amdgcn_s_sleep(H2SLP);
    }
    STAMP(5);
#pragma unroll
    for (int k = 0; k < 5; ++k) dy[64 * k] = f32x2{dec(g[0][k][0]), dec(g[0][k][2])} + f32x2{dec(g[1][k][0]), dec(g[1][k][2])};
  }
  f32x2 xr = up2(lds[L_X + (tid < 320 ? tid : 0)]);
  LAS const f32x2* sy = (LAS const f32x2*)(ldf + L_Y) + tid;
  asm volatile("" : "+v"(xr), "+v"(sy));
  WG_BAR();
  if (w < 5) {
    f32x2 yy[8];
#pragma unroll
    for (int i = 0; i < 8; ++i) yy[i] = sy[320 * i];
    __builtin_amdgcn_sched_barrier(0);
    const f32x2 y0 = (yy[0] + yy[1]) + (yy[2] + yy[3]), y1 = (yy[4] + yy[5]) + (yy[6] + yy[7]);
    const f32x2 hs = xr + rbf2(y0 + y1);
    const unsigned hp2 = pk2(hs[0], hs[1]);
    lds[L_HH + tid] = hp2;
    const f32x2 hv = up2(hp2);
    const float piv = __uint_as_float(__builtin_amdgcn_readfirstlane(__float_as_uint(hv[0])));
    const f32x2 d = hv - piv, dd = d * d;
    const float sp = sw32(d[0] + d[1], dd[0] + dd[1]), sq = sum16(sw16(sp, sp));
    const float s1 = __uint_as_float(__builtin_amdgcn_readlane(__float_as_uint(sq), 0)), s2 = __uint_as_float(__builtin_amdgcn_readlane(__float_as_uint(sq), 32));
    *(LAS f32x2*)(ldf + L_ST + 2 * w) = f32x2{fmaf(s1, 1.f / 128, piv), fmaf(-s1, s1 * (1.f / 128), s2)};
  }
  WG_BAR();
  {
    const f32x4 sa = *(LAS const f32x4*)(ldf + L_ST), sb = *(LAS const f32x4*)(ldf + L_ST + 4);
    const f32x2 sc = *(LAS const f32x2*)(ldf + L_ST + 8);
    const unsigned hw2 = *own;
    __builtin_amdgcn_sched_barrier(0);
    const f32x2 hv = up2(hw2);
    const float mean = (((sa[0] + sa[2]) + (sb[0] + sb[2])) + sc[0]) * 0.2f;
    const float e0 = sa[0] - mean, e1 = sa[2] - mean, e2 = sb[0] - mean, e3 = sb[2] - mean, e4 = sc[0] - mean;
    const float var = fmaf(fmaf(e0, e0, e1 * e1) + fmaf(e2, e2, e3 * e3) + e4 * e4, 0.2f, (((sa[1] + sa[3]) + (sb[1] + sb[3])) + sc[1]) * (1.f / D));
    const float inv = inv_sd(var), mb = rbf(mean);
    const float inv2 = inv_sd(var * inv * inv), mb2 = rbf((mean - mb) * inv);
    const f32x2 tv = rbf2(rbf2(rbf2(rbf2(hv - mb) * inv) - mb2) * inv2);
    STAMP(6);
    float ac[10];
#pragma unroll
    for (int i = 0; i < 5; ++i) {
      const f32x2 t = {__uint_as_float(__builtin_amdgcn_readlane(__float_as_uint(tv[0]), i)), __uint_as_float(__builtin_amdgcn_readlane(__float_as_uint(tv[1]), i))};
      f32x2 a = {0.f, 0.f};
#pragma unroll
      for (int k = 0; k < 3; ++k) {
        const f32x2 z = __builtin_elementwise_fma(__builtin_elementwise_fma(__builtin_elementwise_fma(t, (f32x2)A3[k], (f32x2)A2[k]), t, (f32x2)A1[k]), t, (f32x2)A0[k]);
        const f32x2 d = f32x2{__builtin_amdgcn_exp2f(z[0]), __builtin_amdgcn_exp2f(z[1])} + 1.f;
        a = __builtin_elementwise_fma(__builtin_elementwise_fma(t, (f32x2)B1[k], (f32x2)B0[k]), f32x2{__builtin_amdgcn_rcpf(d[0]), __builtin_amdgcn_rcpf(d[1])}, a);
      }
      ac[2 * i] = a[0]; ac[2 * i + 1] = a[1];
    }
    float y[5];
#pragma unroll
    for (int i = 0; i < 5; ++i) y[i] = sw32(ac[i], ac[i + 5]);
    const float z0 = sum16(sw16(y[0], y[1])), z1 = sum16(sw16(y[2], y[3])), z2 = sum16(sw16(y[4], y[4]));
    if ((lane & 15) == 0) {
      const int o = L_RED + w * 16 + (lane >> 5) * 5, odd = (lane >> 4) & 1;
      ldf[o + odd] = z0; ldf[o + 2 + odd] = z1; ldf[o + 4] = z2;
    }
  }
  const unsigned hp2 = lds[L_HH + cg * 5 + ((lane >> 1) & 7) % 5];
  float hres = (lane & 1) ? bfhi(hp2) : bflo(hp2), bias = bflo(lds[L_X + 320]);
  unsigned a_out = lane < 10 ? (b2i * D + cg * 10 + lane) * 2 : OOB;
  asm volatile("" : "+v"(hres), "+v"(bias), "+v"(a_out));
  WG_BAR();
  if (w == 0) {
    float yy[8];
#pragma unroll
    for (int i = 0; i < 8; ++i) yy[i] = ldf[L_RED + i * 16 + (lane & 15)];
    const float y = ((yy[0] + yy[1]) + (yy[2] + yy[3])) + ((yy[4] + yy[5]) + (yy[6] + yy[7]));
    const __bf16 o = (__bf16)(hres + rbf(rbf(y) + bias));
    __builtin_amdgcn_raw_buffer_store_b16(__builtin_bit_cast(unsigned short, o), mk(out, 4 * D * 2), a_out, 0, 16);
  }
  if (bid == 0 && tid == 64) __builtin_amdgcn_raw_buffer_store_b64(u32x2{(unsigned)E1, (unsigned)(E1 >> 32)}, wsr, 0, 0, 16);
}

extern "C" void kernel_launch(void* const* d_in, const int* in_sizes, int n_in, void* d_out, int out_size, void* d_ws, size_t ws_size,
                              hipStream_t stream) {
  typedef const unsigned short* P;
  fused_decode<<<256, 512, 0, stream>>>((P)d_in[0], (P)d_in[1], (P)d_in[2], (P)d_in[3], (P)d_in[4], (P)d_in[6], (P)d_in[7], (P)d_in[8], (P)d_in[9],
                                        (unsigned short*)d_out, (unsigned char*)d_ws);
}
```

```cpp
#include <hip/hip_runtime.h>
#include <stdint.h>

typedef unsigned u32x4 __attribute__((ext_vector_type(4)));
typedef unsigned u32x2 __attribute__((ext_vector_type(2)));
typedef float f32x4 __attribute__((ext_vector_type(4)));
typedef float f32x2 __attribute__((ext_vector_type(2)));
typedef __bf16 bf16x2 __attribute__((ext_vector_type(2)));
typedef __bf16 bf16x8 __attribute__((ext_vector_type(8)));
typedef __amdgpu_buffer_rsrc_t rsrc_t;
#define LAS __attribute__((address_space(3)))

#define NPRE 6
#define WQ_WAIT 25
#define QDELAY 0
#define SDELAY 0
#define NOQ 0
#define PAIR 1
#define EARLYV 1
#define WAITT 400
#define CK1 14
#define CK2 26
#define SW 32
#define CK3 44
#define EPS 0.0f
#define EPS3 0.0f
#define C1 240
#define C2 430
#define SHIFT 0
#define ROTX (h * 8 + j)
#define H1SLP 1
#define H2SLP 1
#define STR2(x) #x
#define STR(x) STR2(x)

constexpr int D = 640, H = 32, SK = 4096, DH = 192, HD = H * DH, F = 1536;
constexpr unsigned SEQB = SK * DH * 2, TILEB = 8 * DH * 2, KVB = 4u * H * SEQB, WB = D * HD * 2;
constexpr unsigned P1REC = 208 * 8;
constexpr unsigned WS_TS = 4096, WS_QP = WS_TS + 4 * 64 * 128, WS_P1 = WS_QP + H * 4 * 96 * 8, WS_P2 = WS_P1 + 256 * P1REC, WS_END = WS_P2 + 4 * 16 * D * 8;
constexpr unsigned OOB = 0x7ffffff0u;
constexpr unsigned SPIN_CAP = 1u << 17;
constexpr float L2E = 1.4426950408889634f, SCALE = 0.07216878364870322f;
constexpr int OSTR = 208;
constexpr int L_CNT = 0, L_Q = 8, L_REC = L_Q + 96, L_O = L_REC + 4 * 196, L_RED = L_O + 4 * OSTR, L_ST = L_RED + 8 * 16, L_X = L_ST + 16, L_HH = L_X + 324,
              L_XN = L_HH + 320, L_QR = L_XN + 16 * 132, L_MIX = L_QR + 4 * 256, L_WO = L_MIX + 3 * F / 2, L_Y = L_WO, L_END = L_WO + 2 * DH * 36;
constexpr int LDS_DW = 22528;
static_assert(L_END <= LDS_DW && L_REC % 4 == 0 && L_O % 4 == 0 && L_XN % 4 == 0 && L_QR % 4 == 0 && L_MIX % 4 == 0 && L_WO % 4 == 0, "lds map");

__device__ __forceinline__ float bflo(unsigned w) { return __uint_as_float(w << 16); }
__device__ __forceinline__ float bfhi(unsigned w) { return __uint_as_float(w & 0xffff0000u); }
__device__ __forceinline__ unsigned pk2(float lo, float hi) { unsigned r; asm("v_cvt_pk_bf16_f32 %0, %1, %2" : "=v"(r) : "v"(lo), "v"(hi)); return r; }
__device__ __forceinline__ float rbf(float f) { return (float)(__bf16)f; }
__device__ __forceinline__ f32x2 up2(unsigned w) { return f32x2{bflo(w), bfhi(w)}; }
__device__ __forceinline__ f32x2 rbf2(f32x2 v) { return up2(pk2(v[0], v[1])); }
__device__ __forceinline__ float dot2(unsigned a, unsigned b, float c) {
  return __builtin_amdgcn_fdot2_f32_bf16(__builtin_bit_cast(bf16x2, a), __builtin_bit_cast(bf16x2, b), c, false);
}
template <int CTRL> __device__ __forceinline__ float dpp(float x) {
  return __builtin_bit_cast(float, __builtin_amdgcn_mov_dpp(__builtin_bit_cast(int, x), CTRL, 0xf, 0xf, true));
}
constexpr int XOR1 = 0xB1, XOR2 = 0x4E, HMIR = 0x141, ROR8 = 0x128;
__device__ __forceinline__ float sw32(float a, float b) {
  const auto r = __builtin_amdgcn_permlane32_swap(__float_as_uint(a), __float_as_uint(b), false, false);
  return __uint_as_float(r[0]) + __uint_as_float(r[1]);
}
__device__ __forceinline__ float sw16(float a, float b) {
  const auto r = __builtin_amdgcn_permlane16_swap(__float_as_uint(a), __float_as_uint(b), false, false);
  return __uint_as_float(r[0]) + __uint_as_float(r[1]);
}
__device__ __forceinline__ float sum16(float v) { v += dpp<XOR1>(v); v += dpp<XOR2>(v); v += dpp<HMIR>(v); v += dpp<ROR8>(v); return v; }
__device__ __forceinline__ float sum64(float v) { v = sum16(v); v = sw16(v, v); return sw32(v, v); }
__device__ __forceinline__ float scatter8(const float* a, bool b3) {
  float y[4], z[2];
#pragma unroll
  for (int e = 0; e < 4; ++e) y[e] = sw32(a[e], a[e + 4]);
#pragma unroll
  for (int e = 0; e < 2; ++e) z[e] = sw16(y[e], y[e + 2]);
  return (b3 ? z[1] : z[0]) + dpp<ROR8>(b3 ? z[0] : z[1]);
}
typedef short s16x4 __attribute__((ext_vector_type(4)));
__device__ __forceinline__ bf16x8 tr8(LAS const unsigned* p, int step) {
  const s16x4 a = __builtin_amdgcn_ds_read_tr16_b64_v4i16((LAS s16x4*)p), b = __builtin_amdgcn_ds_read_tr16_b64_v4i16((LAS s16x4*)(p + step));
  return __builtin_bit_cast(bf16x8, __builtin_shufflevector(a, b, 0, 1, 2, 3, 4, 5, 6, 7));
}
__device__ __forceinline__ rsrc_t mk(const void* p, unsigned n) { return __builtin_amdgcn_make_buffer_rsrc((void*)p, 0, n, 0x00020000); }
__device__ __forceinline__ u32x4 ld_nt(rsrc_t r, unsigned v, unsigned s) { return __builtin_amdgcn_raw_buffer_load_b128(r, v, s, 2); }
__device__ __forceinline__ u32x4 ld_sc(rsrc_t r, unsigned v, unsigned s = 0) { return __builtin_amdgcn_raw_buffer_load_b128(r, v, s, 16); }
__device__ __forceinline__ void st_sc(rsrc_t r, unsigned v, u32x4 x) { __builtin_amdgcn_raw_buffer_store_b128(x, r, v, 0, 16); }
__device__ __forceinline__ unsigned now() { return (unsigned)__builtin_amdgcn_s_memrealtime(); }
__device__ __forceinline__ unsigned lds_peek(LAS unsigned* p) {
  return __builtin_amdgcn_readfirstlane(__hip_atomic_load(p, __ATOMIC_RELAXED, __HIP_MEMORY_SCOPE_WORKGROUP));
}
#define PIN() do { asm volatile("" ::: "memory"); __builtin_amdgcn_sched_barrier(0); } while (0)
#define WG_BAR() do { asm volatile("s_waitcnt lgkmcnt(0)" ::: "memory"); __builtin_amdgcn_s_barrier(); PIN(); } while (0)
#define STAMP(i) do { } while (0)
struct Tag { unsigned lo, hi, t32; };
__device__ __forceinline__ u32x4 enc2(float a, float b, const Tag& t) {
  return u32x4{(__float_as_uint(a) & 0xffffff00u) | t.lo, t.hi, (__float_as_uint(b) & 0xffffff00u) | t.lo, t.hi};
}
__device__ __forceinline__ unsigned bad2(u32x4 g, const Tag& t) { return (((g[0] ^ t.lo) | (g[2] ^ t.lo)) & 0xffu) | (g[1] ^ t.hi) | (g[3] ^ t.hi); }
__device__ __forceinline__ float dec(unsigned g) { return __uint_as_float(g & 0xffffff00u); }
__device__ __forceinline__ void helper_bar(LAS unsigned* cnt, unsigned target, int lane) {
  asm volatile("s_waitcnt lgkmcnt(0)" ::: "memory");
  if (lane == 0) __hip_atomic_fetch_add(cnt, 1u, __ATOMIC_RELAXED, __HIP_MEMORY_SCOPE_WORKGROUP);
  while (lds_peek(cnt) < target) { }
  asm volatile("" ::: "memory");
}
__device__ __forceinline__ float inv_sd(float var) { return __builtin_amdgcn_rcpf(rbf(__builtin_amdgcn_sqrtf(rbf(rbf(var) + 1e-5f)))); }

__global__ void __launch_bounds__(512, 2)
fused_decode(const unsigned short* x, const unsigned short* kc, const unsigned short* vc, const unsigned short* wq, const unsigned short* wo,
             const unsigned short* w1, const unsigned short* b1, const unsigned short* w2, const unsigned short* b2, unsigned short* out,
             unsigned char* ws) {
  __shared__ __attribute__((aligned(16))) unsigned lds_raw[LDS_DW];
  LAS unsigned* lds = (LAS unsigned*)lds_raw;
  LAS float* ldf = (LAS float*)lds_raw;
  const int tid = threadIdx.x, lane = tid & 63, w = __builtin_amdgcn_readfirstlane(tid >> 6);
  const int bid = blockIdx.x, h = bid >> 3, j = (bid + h) & 7, b = j >> 1, s = j & 1;
  const bool has_q = j >= 2 && s == 1, has_o = j < 2 || s == 0;
  const int hp = h >> 1, co = (h & 1) * 5 + (j < 2 ? j : b + 1);
  const int b2i = bid >> 6, cg = bid & 63;
  STAMP(0);
  const unsigned tstart = (QDELAY | SDELAY) ? now() : 0u;
  asm volatile("" :: "s"(x), "s"(kc), "s"(vc), "s"(wq), "s"(wo), "s"(w1), "s"(b1), "s"(w2), "s"(b2), "s"(out), "s"(ws));
  const rsrc_t wsr0 = mk(ws, WS_END);
  u32x2 ep = __builtin_amdgcn_raw_buffer_load_b64(wsr0, 0, 0, 16);
  bf16x8 Bf[12];

  if (w < 4) {
    const int r = lane >> 3, c = lane & 7;
    const int ntw = 256 + (j < 2 ? 0 : (s ? -SHIFT : SHIFT)), NT = (ntw + 3 - w) >> 2;
    const unsigned seq = (unsigned)(b * H + h) * SEQB;
    const int rot = __builtin_amdgcn_readfirstlane((ROTX) % ntw), y0 = s ? 512 - ntw : 0;
    const bool hi = r >> 2;
    const unsigned kb = PAIR == 1 ? (hi ? 171 : 0) : (hi ? 86 : 0), c1 = PAIR == 1 ? 171 : (hi ? 85 : 86), d1 = PAIR == 1 ? (hi ? 170 : 171) : 85,
                   c2 = PAIR == 1 ? (hi ? 342 : 341) : 171, d2 = PAIR == 1 ? 171 : (hi ? 85 : 86);
    constexpr int PB = PAIR == 1 ? 9 : 8;
    auto ty = [&](int i) -> int { const int y1 = i * 4 + w + rot; return y0 + (y1 >= ntw ? y1 - ntw : y1); };
    auto tv = [&](int i) -> unsigned {
      if (!PAIR) return (unsigned)(r * 384 + c * 16);
      const unsigned mm = ((4 * ty(i)) & ((1 << PB) - 1)) + (r & 3);
      return (mm + kb + (mm >= c1 ? d1 : 0) + (mm >= c2 ? d2 : 0)) * 384 + c * 16; };
    auto ts = [&](int i) -> unsigned {
      return __builtin_amdgcn_readfirstlane(PAIR ? seq + (unsigned)((4 * ty(i)) >> PB) * (768u << PB) : seq + ty(i) * TILEB); };
    const rsrc_t kr = mk(kc, KVB), vr = mk(vc, KVB);
    u32x4 K[NPRE][3], V[NPRE][3];
    WG_BAR();
    if (SDELAY) while (now() - tstart < SDELAY) __builtin_amdgcn_s_sleep(1);
#pragma unroll
    for (int p = 0; p < NPRE; ++p) {
      if (p) asm volatile("s_waitcnt vmcnt(3)" ::: "memory");
#pragma unroll
      for (int i = 0; i < 3; ++i) K[p][i] = ld_nt(kr, tv(p) + 128 * i, ts(p));
      PIN();
      if (p) asm volatile("s_waitcnt vmcnt(3)" ::: "memory");
#pragma unroll
      for (int i = 0; i < 3; ++i) V[p][i] = ld_nt(vr, tv(p) + 128 * i, ts(p));
      PIN();
    }
    WG_BAR();
    STAMP(1);
    u32x4 qf[3];
#pragma unroll
    for (int i = 0; i < 3; ++i) qf[i] = *(LAS const u32x4*)(lds + L_Q + (c + 8 * i) * 4);
    float m = __uint_as_float(0xff7f0000u), l = 0.f, acc[24];
#pragma unroll
    for (int k = 0; k < 24; ++k) acc[k] = 0.f;
    unsigned pace = 0;
    bool stamp3 = false;
    auto step_fn = [&](u32x4 (&Kc)[3], u32x4 (&Vc)[3], bool refill, unsigned nbytes, unsigned av, unsigned as) {
      float a0 = 0.f, a1 = 0.f, a2 = 0.f;
#pragma unroll
      for (int k = 0; k < 4; ++k) { a0 = dot2(Kc[0][k], qf[0][k], a0); a1 = dot2(Kc[1][k], qf[1][k], a1); a2 = dot2(Kc[2][k], qf[2][k], a2); }
      float a = (a0 + a1) + a2;
      asm volatile("" : "+v"(a));
      PIN();
      if (refill) {
        if (pace) { unsigned tn; while ((int)(((tn = now()) << 6) - pace) < 0) { }
                    if (stamp3) { if (lane == 0) lds[L_CNT + 3] = tn | 1u; stamp3 = false; } }
        const rsrc_t k2 = mk(kc, nbytes);
#pragma unroll
        for (int i = 0; i < 3; ++i) Kc[i] = ld_nt(k2, av + 128 * i, as);
      }
      PIN();
      f32x2 vf[12];
#pragma unroll
      for (int k = 0; k < 12; ++k) vf[k] = up2(Vc[k >> 2][k & 3]);
      if (EARLYV && refill) {
        asm volatile("" : "+v"(vf[0]), "+v"(vf[1]), "+v"(vf[2]), "+v"(vf[3]), "+v"(vf[4]), "+v"(vf[5]));
        asm volatile("" : "+v"(vf[6]), "+v"(vf[7]), "+v"(vf[8]), "+v"(vf[9]), "+v"(vf[10]), "+v"(vf[11]));
        PIN();
        const rsrc_t v2 = mk(vc, nbytes);
#pragma unroll
        for (int i = 0; i < 3; ++i) Vc[i] = ld_nt(v2, av + 128 * i, as);
        PIN();
      }
      a += dpp<XOR1>(a); a += dpp<XOR2>(a); a += dpp<HMIR>(a);
      const float sc = rbf(rbf(a) * SCALE);
      const float mn = fmaxf(m, sc);
      const float alpha = __builtin_amdgcn_exp2f((m - mn) * L2E), p = __builtin_amdgcn_exp2f((sc - mn) * L2E);
      l = fmaf(l, alpha, p); m = mn;
#pragma unroll
      for (int k = 0; k < 12; ++k) { acc[2 * k] = fmaf(p, vf[k][0], acc[2 * k] * alpha); acc[2 * k + 1] = fmaf(p, vf[k][1], acc[2 * k + 1] * alpha); }
      PIN();
      if (!EARLYV && refill) {
        const rsrc_t v2 = mk(vc, nbytes);
#pragma unroll
        for (int i = 0; i < 3; ++i) Vc[i] = ld_nt(v2, av + 128 * i, as);
      }
      PIN();
    };
#pragma unroll
    for (int p = 0; p < NPRE; ++p) step_fn(K[p], V[p], p == 0, KVB, tv(NPRE), ts(NPRE));
    int t = NPRE + 1;
    do {
      if (w == 0 && (t == CK1 || t == CK2)) { const unsigned tn = now() | 1u; if (lane == 0) lds[L_CNT + (t == CK1 ? 1 : 2)] = tn; }
      step_fn(K[0], V[0], true, KVB, tv(t), ts(t));
    } while (++t < SW);
    static_assert(SHIFT == 0 && (64 - SW) % 2 == 0, "pairs of rounds");
    unsigned tend;
    while ((tend = lds_peek(lds + L_CNT + 4)) == 0) __builtin_amdgcn_s_sleep(1);
    unsigned tgt = now() << 6;
    int tau = (int)((tend << 6) - tgt) / (63 - SW);
#pragma unroll
    for (int i = 0; i < 3; ++i) K[1][i] = ld_nt(kr, tv(t) + 128 * i, ts(t));
    PIN();
#pragma unroll
    for (int i = 0; i < 3; ++i) V[1][i] = ld_nt(vr, tv(t) + 128 * i, ts(t));
    PIN();
    do {
      const unsigned t3 = lds_peek(lds + L_CNT + 5);
      if (t3 && t3 != tend) { tend = t3; tau = (int)((tend << 6) - tgt) / (63 - t); }
      tgt += tau;
      pace = tgt;
      step_fn(K[0], V[0], true, KVB, tv(t + 1), ts(t + 1));
      tgt += tau;
      pace = t + 2 < 64 ? tgt : 0u;
      if (w == 0 && t + 2 == CK3) stamp3 = true;
      step_fn(K[1], V[1], true, t + 2 < 64 ? KVB : 0u, tv(t + 2), ts(t + 2));
      t += 2;
    } while (t < 64);
    step_fn(K[0], V[0], false, 0, 0, 0);
    float M = fmaxf(m, dpp<ROR8>(m));
    { const auto q16 = __builtin_amdgcn_permlane16_swap(__float_as_uint(M), __float_as_uint(M), false, false);
      M = fmaxf(__uint_as_float(q16[0]), __uint_as_float(q16[1]));
      const auto q32 = __builtin_amdgcn_permlane32_swap(__float_as_uint(M), __float_as_uint(M), false, false);
      M = fmaxf(__uint_as_float(q32[0]), __uint_as_float(q32[1])); }
    const float f = __builtin_amdgcn_exp2f((m - M) * L2E);
    l *= f; l += dpp<ROR8>(l); l = sw16(l, l); l = sw32(l, l);
#pragma unroll
    for (int k = 0; k < 24; ++k) acc[k] *= f;
#pragma unroll
    for (int i = 0; i < 3; ++i) ldf[L_REC + w * 196 + i * 64 + c * 8 + r] = scatter8(acc + i * 8, lane & 8);
    if (lane == 0) { ldf[L_REC + w * 196 + 192] = M; ldf[L_REC + w * 196 + 193] = l; }
  } else {
    const int hid = tid - 256, hw = w - 4;
    if (hid < 8) lds[L_CNT + hid] = 0;
    const int cq = (j - 2) >> 1, rg = hid >> 3, c8 = hid & 7;
    u32x4 W[20], X[5];
    const rsrc_t xr = mk(x, has_q ? 4 * D * 2 : 0), wqr = mk(wq, has_q ? WB : 0);
#pragma unroll
    for (int i = 0; i < 5; ++i) { const int ch = 20 * hw + (lane & 15) + 16 * i; X[i] = ld_nt(xr, (lane >> 4) * (D * 2) + (ch >= 80 ? ch - 80 : ch) * 16, 0); }
    PIN();
#pragma unroll
    for (int i = 0; i < 20; ++i) {
      W[i] = ld_nt(wqr, (unsigned)(lane >> 3) * (HD * 2) + c8 * 16, (unsigned)(160 * hw + 8 * i) * (HD * 2) + (h * DH + cq * 64) * 2);
      if ((i & 3) == 3) PIN();
    }
    if (WQ_WAIT < 25) asm volatile("s_waitcnt vmcnt(" STR(WQ_WAIT) ")" ::: "memory");
    WG_BAR();
    asm volatile("" : "+v"(ep));
    const unsigned long long E1 = ((unsigned long long)__builtin_amdgcn_readfirstlane(ep[1]) << 32 | (unsigned)__builtin_amdgcn_readfirstlane(ep[0])) + 1;
    const Tag tg{(unsigned)E1 & 0xffu, (unsigned)(E1 >> 8), (unsigned)E1};
    if (has_q) {
      float sa = 0.f, sb = 0.f;
#pragma unroll
      for (int i = 0; i < 20; i += 2) { sa = dot2(X[i >> 2][i & 3], 0x3f803f80u, sa); sb = dot2(X[i >> 2][(i & 3) + 1], 0x3f803f80u, sb); }
      const float mean = sum16(sa + sb) * (1.f / D);
      f32x2 s2 = {0.f, 0.f}, s3 = s2;
#pragma unroll
      for (int i = 0; i < 20; i += 2) {
        const f32x2 d = up2(X[i >> 2][i & 3]) - mean, e = up2(X[i >> 2][(i & 3) + 1]) - mean;
        s2 = __builtin_elementwise_fma(d, d, s2); s3 = __builtin_elementwise_fma(e, e, s3);
      }
      s2 += s3;
      const float inv = inv_sd(sum16(s2[0] + s2[1]) * (1.f / D)), mb = rbf(mean);
      LAS unsigned* xn = lds + L_XN + hw * 4 * 132;
#pragma unroll
      for (int i = 0; i < 2; ++i) {
        u32x4 o;
#pragma unroll
        for (int k = 0; k < 4; ++k) { const f32x2 y = rbf2(up2(X[i][k]) - mb) * inv; o[k] = pk2(y[0], y[1]); }
        *(LAS u32x4*)(xn + (lane >> 4) * 132 + ((lane & 15) + 16 * i) * 4) = o;
      }
      asm volatile("" ::: "memory");
      u32x4 af[5];
#pragma unroll
      for (int ks = 0; ks < 5; ++ks) af[ks] = *(LAS const u32x4*)(xn + (lane & 3) * 132 + ks * 16 + (lane >> 4) * 4);
      LAS unsigned* st = lds + L_WO + hw * 64 * 36;
      f32x4 dq[4];
#pragma unroll
      for (int nt = 0; nt < 4; ++nt) dq[nt] = f32x4{0.f, 0.f, 0.f, 0.f};
#pragma unroll
      for (int ks = 0; ks < 5; ++ks) {
        LAS unsigned* sk = st + (ks & 1) * 32 * 36;
#pragma unroll
        for (int i = 0; i < 4; ++i) *(LAS u32x4*)(sk + (8 * i + (lane >> 3)) * 36 + c8 * 4) = W[4 * ks + i];
        asm volatile("" ::: "memory");
        LAS const unsigned* rd = sk + (8 * (lane >> 4) + ((lane >> 2) & 3)) * 36 + (lane & 3) * 2;
#pragma unroll
        for (int nt = 0; nt < 4; ++nt) dq[nt] = __builtin_amdgcn_mfma_f32_16x16x32_bf16(__builtin_bit_cast(bf16x8, af[ks]), tr8(rd + nt * 8, 4 * 36), dq[nt], 0, 0, 0);
        asm volatile("" ::: "memory");
      }
      const bool g1 = lane & 16, g2 = lane & 32;
#pragma unroll
      for (int nt = 0; nt < 4; ++nt) {
        const float a01 = g1 ? dq[nt][1] : dq[nt][0], a23 = g1 ? dq[nt][3] : dq[nt][2];
        ldf[L_QR + hw * 256 + (lane >> 4) * 64 + nt * 16 + (lane & 15)] = g2 ? a23 : a01;
      }
      helper_bar(lds + L_CNT, 4, lane);
      if (hw == 0) {
        f32x4 a = *(LAS const f32x4*)(ldf + L_QR + 4 * lane);
#pragma unroll
        for (int ww = 1; ww < 4; ++ww) a += *(LAS const f32x4*)(ldf + L_QR + ww * 256 + 4 * lane);
        st_sc(wsr0, WS_QP + ((h * 4 + (lane >> 4)) * 96 + cq * 32 + (lane & 15) * 2) * 8, u32x4{pk2(a[0], a[1]), tg.t32, pk2(a[2], a[3]), tg.t32});
      }
    }
    STAMP(3);
    if (hw == 3 && !NOQ) {
      if (QDELAY) while (now() - tstart < QDELAY) __builtin_amdgcn_s_sleep(2);
      const unsigned o = lane < 48 ? WS_QP + (h * 4 + b) * 96 * 8 + lane * 16 : OOB, vm = lane < 48 ? ~0u : 0u;
      u32x4 g;
#pragma nounroll
      for (unsigned spin = 0; spin < SPIN_CAP; ++spin) {
        asm volatile("" ::: "memory");
        g = ld_sc(wsr0, o);
        if (__all((((g[1] ^ tg.t32) | (g[3] ^ tg.t32)) & vm) == 0)) break;
        __builtin_amdgcn_s_sleep(2);
      }
      if (lane < 48) *(LAS u32x2*)(lds + L_Q + 2 * lane) = u32x2{g[0], g[2]};
      STAMP(1);
    }
    WG_BAR();
    const rsrc_t wor = mk(wo, has_o ? WB : 0);
    u32x4 Wo[12];
#pragma unroll
    for (int i = 0; i < 12; ++i) Wo[i] = ld_nt(wor, (unsigned)rg * (D * 2) + c8 * 16, (unsigned)(hp * 2 * DH + 32 * i) * (D * 2) + co * 128);
    if (hid < 192) {
      *(LAS u32x4*)(lds + L_MIX + hid * 4) = __builtin_nontemporal_load((const u32x4*)(w1 + hid * 8));
      *(LAS u32x4*)(lds + L_MIX + 768 + hid * 4) = __builtin_nontemporal_load((const u32x4*)(b1 + hid * 8));
      *(LAS u32x4*)(lds + L_MIX + 1536 + hid * 4) = __builtin_nontemporal_load((const u32x4*)(w2 + hid * 8));
    }
    if (hid < 80) *(LAS u32x4*)(lds + L_X + hid * 4) = __builtin_nontemporal_load((const u32x4*)(x + b2i * D + hid * 8));
    if (hid == 80) lds[L_X + 320] = b2[0];
#pragma unroll
    for (int i = 0; i < 12; ++i) *(LAS u32x4*)(lds + L_WO + (rg + 32 * i) * 36 + c8 * 4) = Wo[i];
    helper_bar(lds + L_CNT, has_q ? 8 : 4, lane);
    LAS const unsigned* wl = lds + L_WO + (8 * (lane >> 4) + ((lane >> 2) & 3)) * 36 + hw * 8 + (lane & 3) * 2;
#pragma unroll
    for (int ks = 0; ks < 12; ++ks) Bf[ks] = tr8(wl + ks * 32 * 36, 4 * 36);
    if (hw == 2) {
      unsigned tm0 = 0;
#pragma nounroll
      for (int ck = 0; ck < 3; ++ck) {
        unsigned t2;
        while ((t2 = lds_peek(lds + L_CNT + 1 + ck)) == 0) __builtin_amdgcn_s_sleep(8);
        if ((bid & 3) == ((h >> 1) & 3) && lane == 0) __builtin_amdgcn_raw_buffer_store_b64(u32x2{t2, tg.t32}, wsr0, WS_TS + (ck * 64 + (bid >> 2)) * 128, 0, 16);
        while (now() - t2 < WAITT) __builtin_amdgcn_s_sleep(8);
        const u32x2 g = __builtin_amdgcn_raw_buffer_load_b64(wsr0, WS_TS + (ck * 64 + lane) * 128, 0, 16);
        const unsigned tm = t2 + (int)(sum64(g[1] == tg.t32 ? (float)(int)(g[0] - t2) : (float)(WAITT + 30)) * (1.f / 64));
        const float left = ck == 1 ? (63.f - CK2) / (CK2 - CK1) * (1.f - EPS) : (63.f - CK3) / (CK3 - CK2) * (1.f - EPS3);
        if (ck && lane == 0) lds[L_CNT + 3 + ck] = (tm + (int)((float)(int)(tm - tm0) * left)) | 1u;
        tm0 = tm;
      }
    }
  }
  const unsigned long long E1 = ((unsigned long long)__builtin_amdgcn_readfirstlane(ep[1]) << 32 | (unsigned)__builtin_amdgcn_readfirstlane(ep[0])) + 1;
  const Tag tg{(unsigned)E1 & 0xffu, (unsigned)(E1 >> 8), (unsigned)E1};
  const int u = tid - 256, uu = u < 96 ? u : 96;
  unsigned a_p1 = u <= 96 ? WS_P1 + (h * 8 + j) * P1REC + u * 16 : OOB;
  unsigned a_p2 = (lane & 1) ? OOB : WS_P2 + (((lane >> 4) * 16 + hp) * D + co * 64 + (w - 4) * 16 + (lane & 15)) * 8;
  LAS const float* a_rec = ldf + L_REC + 2 * (w < 4 ? 0 : uu);
  LAS const unsigned* a_o = lds + L_O + (lane & 3) * OSTR + (lane >> 4) * 4;
  LAS const unsigned* own = lds + L_HH + cg * 5 + (lane < 5 ? lane : 0);
  asm volatile("" : "+v"(a_p1), "+v"(a_p2), "+v"(a_rec), "+v"(a_o), "+v"(own));
  const rsrc_t wsr = wsr0;
  WG_BAR();
  STAMP(2);
  if (w == 4 || w == 5) {
    float mm[4];
    f32x2 rv[4];
#pragma unroll
    for (int i = 0; i < 4; ++i) { mm[i] = ldf[L_REC + i * 196 + 192]; rv[i] = *(LAS const f32x2*)(a_rec + i * 196); }
    __builtin_amdgcn_sched_barrier(0);
    const float M = fmaxf(fmaxf(mm[0], mm[1]), fmaxf(mm[2], mm[3]));
    f32x2 a = {0.f, 0.f};
#pragma unroll
    for (int i = 0; i < 4; ++i) a += __builtin_amdgcn_exp2f((mm[i] - M) * L2E) * rv[i];
    st_sc(wsr, a_p1, enc2(u < 96 ? a[0] : M, a[1], tg));
    if (w == 4) STAMP(1);
  }
  float A0[3], A1[3], A2[3], A3[3], B0[3], B1[3];
  {
    LAS const unsigned short* mx = (LAS const unsigned short*)(lds + L_MIX);
    constexpr float K0 = -2.f * L2E * 0.7978845608028654f, K1 = K0 * 0.044715f;
#pragma unroll
    for (int i = 0; i < 3; ++i) {
      const float a = bflo(mx[tid + 512 * i]), c = bflo(mx[F + tid + 512 * i]), e = bflo(mx[2 * F + tid + 512 * i]);
      A3[i] = K1 * a * a * a; A2[i] = 3.f * K1 * a * a * c; A1[i] = a * fmaf(3.f * K1 * c, c, K0); A0[i] = c * fmaf(K1 * c, c, K0);
      B1[i] = a * e; B0[i] = c * e;
      asm volatile("" : "+v"(A0[i]), "+v"(A1[i]), "+v"(A2[i]), "+v"(A3[i]), "+v"(B0[i]), "+v"(B1[i]));
    }
  }
  const unsigned tc5 = lds_peek(lds + L_CNT + 5), tcom = tc5 ? tc5 : lds_peek(lds + L_CNT + 4);
  if (has_o) {
    {
      const unsigned oa = WS_P1 + (hp * 16 + (w >> 2) * 8 + 2 * (w & 3)) * P1REC + lane * 16, ob = lane < 33 ? oa + 1024 : OOB;
      const unsigned vm = lane < 33 ? ~0u : 0u;
      LAS unsigned* da = lds + L_O + (w & 3) * OSTR + (w >> 2) * 96 + lane;
      LAS unsigned* db = lane < 32 ? da + 64 : lds + L_QR + lane;
      asm volatile("" : "+v"(da), "+v"(db));
      u32x4 a[2], c[2];
      unsigned pend = 3;
      if (C1) while ((int)(now() - tcom) < C1) { }
#pragma nounroll
      for (unsigned spin = 0; spin < SPIN_CAP; ++spin) {
        asm volatile("" ::: "memory");
#pragma unroll
        for (int i = 0; i < 2; ++i) if (pend >> i & 1) { a[i] = ld_sc(wsr, oa, i * P1REC); c[i] = ld_sc(wsr, ob, i * P1REC); }
        asm volatile("s_waitcnt vmcnt(0)" ::: "memory");
#pragma unroll
        for (int i = 0; i < 2; ++i) if (pend >> i & 1) { if (__all((bad2(a[i], tg) | (bad2(c[i], tg) & vm)) == 0)) pend &= ~(1u << i); }
        if (!pend) break;
        __builtin_amdgcn_s_sleep(H1SLP);
      }
      STAMP(4);
      const float m0 = dec(__builtin_amdgcn_readlane(c[0][0], 32)), m1 = dec(__builtin_amdgcn_readlane(c[1][0], 32)), M = fmaxf(m0, m1);
      const float l0 = dec(__builtin_amdgcn_readlane(c[0][2], 32)), l1 = dec(__builtin_amdgcn_readlane(c[1][2], 32));
      float w0 = __builtin_amdgcn_exp2f((m0 - M) * L2E), w1_ = __builtin_amdgcn_exp2f((m1 - M) * L2E);
      const float inv = __builtin_amdgcn_rcpf(fmaf(w0, l0, w1_ * l1));
      w0 *= inv; w1_ *= inv;
      const f32x2 oa_ = __builtin_elementwise_fma(f32x2{dec(a[0][0]), dec(a[0][2])}, (f32x2)w0, f32x2{dec(a[1][0]), dec(a[1][2])} * w1_);
      const f32x2 oc_ = __builtin_elementwise_fma(f32x2{dec(c[0][0]), dec(c[0][2])}, (f32x2)w0, f32x2{dec(c[1][0]), dec(c[1][2])} * w1_);
      *da = pk2(oa_[0], oa_[1]); *db = pk2(oc_[0], oc_[1]);
    }
    WG_BAR();
    if (w >= 4) {
      u32x4 af[12];
#pragma unroll
      for (int ks = 0; ks < 12; ++ks) af[ks] = *(LAS const u32x4*)(a_o + ks * 16);
      __builtin_amdgcn_sched_barrier(0);
      f32x4 d0 = {0.f, 0.f, 0.f, 0.f}, d1 = d0;
#pragma unroll
      for (int ks = 0; ks < 12; ks += 2) {
        d0 = __builtin_amdgcn_mfma_f32_16x16x32_bf16(__builtin_bit_cast(bf16x8, af[ks]), Bf[ks], d0, 0, 0, 0);
        d1 = __builtin_amdgcn_mfma_f32_16x16x32_bf16(__builtin_bit_cast(bf16x8, af[ks + 1]), Bf[ks + 1], d1, 0, 0, 0);
      }
      d0 += d1;
      const bool g1 = lane & 16, g2 = lane & 32;
      const float a01 = g1 ? d0[1] : d0[0], a23 = g1 ? d0[3] : d0[2], a = g2 ? a23 : a01;
      st_sc(wsr, a_p2, enc2(a, dpp<XOR1>(a), tg));
      if (w != 6) STAMP(3);
    }
  }
  {
    const unsigned o = WS_P2 + (b2i * 16 + 2 * w) * D * 8 + lane * 16;
    LAS f32x2* dy = (LAS f32x2*)(ldf + L_Y) + w * 320 + lane;
    asm volatile("" : "+v"(dy));
    u32x4 g[2][5];
    unsigned pend = 3;
    if (C2) while ((int)(now() - tcom) < C2) { }
#pragma nounroll
    for (unsigned spin = 0; spin < SPIN_CAP; ++spin) {
      asm volatile("" ::: "memory");
#pragma unroll
      for (int i = 0; i < 2; ++i) if (pend >> i & 1) {
#pragma unroll
        for (int k = 0; k < 5; ++k) g[i][k] = ld_sc(wsr, o + k * 1024, i * D * 8);
      }
      asm volatile("s_waitcnt vmcnt(0)" ::: "memory");
#pragma unroll
      for (int i = 0; i < 2; ++i) if (pend >> i & 1) {
        if (__all((bad2(g[i][0], tg) | bad2(g[i][1], tg) | bad2(g[i][2], tg) | bad2(g[i][3], tg) | bad2(g[i][4], tg)) == 0)) pend &= ~(1u << i);
      }
      if (!pend) break;
      __builtin_amdgcn_s_sleep(H2SLP);
    }
    STAMP(5);
#pragma unroll
    for (int k = 0; k < 5; ++k) dy[64 * k] = f32x2{dec(g[0][k][0]), dec(g[0][k][2])} + f32x2{dec(g[1][k][0]), dec(g[1][k][2])};
  }
  f32x2 xr = up2(lds[L_X + (tid < 320 ? tid : 0)]);
  LAS const f32x2* sy = (LAS const f32x2*)(ldf + L_Y) + tid;
  asm volatile("" : "+v"(xr), "+v"(sy));
  WG_BAR();
  if (w < 5) {
    f32x2 yy[8];
#pragma unroll
    for (int i = 0; i < 8; ++i) yy[i] = sy[320 * i];
    __builtin_amdgcn_sched_barrier(0);
    const f32x2 y0 = (yy[0] + yy[1]) + (yy[2] + yy[3]), y1 = (yy[4] + yy[5]) + (yy[6] + yy[7]);
    const f32x2 hs = xr + rbf2(y0 + y1);
    const unsigned hp2 = pk2(hs[0], hs[1]);
    lds[L_HH + tid] = hp2;
    const f32x2 hv = up2(hp2);
    const float piv = __uint_as_float(__builtin_amdgcn_readfirstlane(__float_as_uint(hv[0])));
    const f32x2 d = hv - piv, dd = d * d;
    const float sp = sw32(d[0] + d[1], dd[0] + dd[1]), sq = sum16(sw16(sp, sp));
    const float s1 = __uint_as_float(__builtin_amdgcn_readlane(__float_as_uint(sq), 0)), s2 = __uint_as_float(__builtin_amdgcn_readlane(__float_as_uint(sq), 32));
    *(LAS f32x2*)(ldf + L_ST + 2 * w) = f32x2{fmaf(s1, 1.f / 128, piv), fmaf(-s1, s1 * (1.f / 128), s2)};
  }
  WG_BAR();
  {
    const f32x4 sa = *(LAS const f32x4*)(ldf + L_ST), sb = *(LAS const f32x4*)(ldf + L_ST + 4);
    const f32x2 sc = *(LAS const f32x2*)(ldf + L_ST + 8);
    const unsigned hw2 = *own;
    __builtin_amdgcn_sched_barrier(0);
    const f32x2 hv = up2(hw2);
    const float mean = (((sa[0] + sa[2]) + (sb[0] + sb[2])) + sc[0]) * 0.2f;
    const float e0 = sa[0] - mean, e1 = sa[2] - mean, e2 = sb[0] - mean, e3 = sb[2] - mean, e4 = sc[0] - mean;
    const float var = fmaf(fmaf(e0, e0, e1 * e1) + fmaf(e2, e2, e3 * e3) + e4 * e4, 0.2f, (((sa[1] + sa[3]) + (sb[1] + sb[3])) + sc[1]) * (1.f / D));
    const float inv = inv_sd(var), mb = rbf(mean);
    const float inv2 = inv_sd(var * inv * inv), mb2 = rbf((mean - mb) * inv);
    const f32x2 tv = rbf2(rbf2(rbf2(rbf2(hv - mb) * inv) - mb2) * inv2);
    STAMP(6);
    float ac[10];
#pragma unroll
    for (int i = 0; i < 5; ++i) {
      const f32x2 t = {__uint_as_float(__builtin_amdgcn_readlane(__float_as_uint(tv[0]), i)), __uint_as_float(__builtin_amdgcn_readlane(__float_as_uint(tv[1]), i))};
      f32x2 a = {0.f, 0.f};
#pragma unroll
      for (int k = 0; k < 3; ++k) {
        const f32x2 z = __builtin_elementwise_fma(__builtin_elementwise_fma(__builtin_elementwise_fma(t, (f32x2)A3[k], (f32x2)A2[k]), t, (f32x2)A1[k]), t, (f32x2)A0[k]);
        const f32x2 d = f32x2{__builtin_amdgcn_exp2f(z[0]), __builtin_amdgcn_exp2f(z[1])} + 1.f;
        a = __builtin_elementwise_fma(__builtin_elementwise_fma(t, (f32x2)B1[k], (f32x2)B0[k]), f32x2{__builtin_amdgcn_rcpf(d[0]), __builtin_amdgcn_rcpf(d[1])}, a);
      }
      ac[2 * i] = a[0]; ac[2 * i + 1] = a[1];
    }
    float y[5];
#pragma unroll
    for (int i = 0; i < 5; ++i) y[i] = sw32(ac[i], ac[i + 5]);
    const float z0 = sum16(sw16(y[0], y[1])), z1 = sum16(sw16(y[2], y[3])), z2 = sum16(sw16(y[4], y[4]));
    if ((lane & 15) == 0) {
      const int o = L_RED + w * 16 + (lane >> 5) * 5, odd = (lane >> 4) & 1;
      ldf[o + odd] = z0; ldf[o + 2 + odd] = z1; ldf[o + 4] = z2;
    }
  }
  const unsigned hp2 = lds[L_HH + cg * 5 + ((lane >> 1) & 7) % 5];
  float hres = (lane & 1) ? bfhi(hp2) : bflo(hp2), bias = bflo(lds[L_X + 320]);
  unsigned a_out = lane < 10 ? (b2i * D + cg * 10 + lane) * 2 : OOB;
  asm volatile("" : "+v"(hres), "+v"(bias), "+v"(a_out));
  WG_BAR();
  if (w == 0) {
    float yy[8];
#pragma unroll
    for (int i = 0; i < 8; ++i) yy[i] = ldf[L_RED + i * 16 + (lane & 15)];
    const float y = ((yy[0] + yy[1]) + (yy[2] + yy[3])) + ((yy[4] + yy[5]) + (yy[6] + yy[7]));
    const __bf16 o = (__bf16)(hres + rbf(rbf(y) + bias));
    __builtin_amdgcn_raw_buffer_store_b16(__builtin_bit_cast(unsigned short, o), mk(out, 4 * D * 2), a_out, 0, 16);
  }
  if (bid == 0 && tid == 64) __builtin_amdgcn_raw_buffer_store_b64(u32x2{(unsigned)E1, (unsigned)(E1 >> 32)}, wsr, 0, 0, 16);
}

extern "C" void kernel_launch(void* const* d_in, const int* in_sizes, int n_in, void* d_out, int out_size, void* d_ws, size_t ws_size,
                              hipStream_t stream) {
  typedef const unsigned short* P;
  fused_decode<<<256, 512, 0, stream>>>((P)d_in[0], (P)d_in[1], (P)d_in[2], (P)d_in[3], (P)d_in[4], (P)d_in[6], (P)d_in[7], (P)d_in[8], (P)d_in[9],
                                        (unsigned short*)d_out, (unsigned char*)d_ws);
}
```

```cpp
#include <hip/hip_runtime.h>
#include <stdint.h>

typedef unsigned u32x4 __attribute__((ext_vector_type(4)));
typedef unsigned u32x2 __attribute__((ext_vector_type(2)));
typedef float f32x4 __attribute__((ext_vector_type(4)));
typedef float f32x2 __attribute__((ext_vector_type(2)));
typedef __bf16 bf16x2 __attribute__((ext_vector_type(2)));
typedef __bf16 bf16x8 __attribute__((ext_vector_type(8)));
typedef __amdgpu_buffer_rsrc_t rsrc_t;
#define LAS __attribute__((address_space(3)))

#define NPRE 6
#define WQ_WAIT 25
#define QDELAY 0
#define SDELAY 0
#define NOQ 0
#define PAIR 1
#define EARLYV 1
#define WAITT 400
#define CK1 14
#define CK2 26
#define SW 32
#define CK3 44
#define EPS 0.0f
#define EPS3 0.0f
#define C1 240
#define C2 450
#define PSH 0
#define OUTAUX 16
#define PRIO 1
#define CKG 28
#define FORCE_FB 0
#define SHIFT 0
#define ROTX 0
#define H1SLP 1
#define H2SLP 1
#define STR2(x) #x
#define STR(x) STR2(x)

constexpr int D = 640, H = 32, SK = 4096, DH = 192, HD = H * DH, F = 1536;
constexpr unsigned SEQB = SK * DH * 2, TILEB = 8 * DH * 2, KVB = 4u * H * SEQB, WB = D * HD * 2;
constexpr unsigned P1REC = 208 * 8;
constexpr unsigned WS_TS = 4096, WS_TB = WS_TS + 4 * 64 * 128, WS_QP = WS_TB + 256 * 128, WS_P1 = WS_QP + H * 4 * 96 * 8, WS_P2 = WS_P1 + 256 * P1REC, WS_END = WS_P2 + 4 * 16 * D * 8;
constexpr unsigned OOB = 0x7ffffff0u;
constexpr unsigned SPIN_CAP = 1u << 17;
constexpr unsigned TAB0 = 100u << 7;
constexpr float L2E = 1.4426950408889634f, SCALE = 0.07216878364870322f;
constexpr int OSTR = 208;
constexpr int L_CNT = 0, L_Q = 8, L_REC = L_Q + 96, L_O = L_REC + 4 * 196, L_RED = L_O + 4 * OSTR, L_ST = L_RED + 8 * 16, L_X = L_ST + 16, L_HH = L_X + 324,
              L_XN = L_HH + 320, L_QR = L_XN + 16 * 132, L_MIX = L_QR + 4 * 256, L_WO = L_MIX + 3 * F / 2, L_Y = L_WO, L_TAB = L_WO + 2 * DH * 36, L_END = L_TAB + 4096;
constexpr int LDS_DW = 26624;
static_assert(L_END <= LDS_DW && L_REC % 4 == 0 && L_O % 4 == 0 && L_XN % 4 == 0 && L_QR % 4 == 0 && L_MIX % 4 == 0 && L_WO % 4 == 0, "lds map");

__device__ __forceinline__ float bflo(unsigned w) { return __uint_as_float(w << 16); }
__device__ __forceinline__ float bfhi(unsigned w) { return __uint_as_float(w & 0xffff0000u); }
__device__ __forceinline__ unsigned pk2(float lo, float hi) { unsigned r; asm("v_cvt_pk_bf16_f32 %0, %1, %2" : "=v"(r) : "v"(lo), "v"(hi)); return r; }
__device__ __forceinline__ float rbf(float f) { return (float)(__bf16)f; }
__device__ __forceinline__ f32x2 up2(unsigned w) { return f32x2{bflo(w), bfhi(w)}; }
__device__ __forceinline__ f32x2 rbf2(f32x2 v) { return up2(pk2(v[0], v[1])); }
__device__ __forceinline__ float dot2(unsigned a, unsigned b, float c) {
  return __builtin_amdgcn_fdot2_f32_bf16(__builtin_bit_cast(bf16x2, a), __builtin_bit_cast(bf16x2, b), c, false);
}
template <int CTRL> __device__ __forceinline__ float dpp(float x) {
  return __builtin_bit_cast(float, __builtin_amdgcn_mov_dpp(__builtin_bit_cast(int, x), CTRL, 0xf, 0xf, true));
}
constexpr int XOR1 = 0xB1, XOR2 = 0x4E, HMIR = 0x141, ROR8 = 0x128;
__device__ __forceinline__ float sw32(float a, float b) {
  const auto r = __builtin_amdgcn_permlane32_swap(__float_as_uint(a), __float_as_uint(b), false, false);
  return __uint_as_float(r[0]) + __uint_as_float(r[1]);
}
__device__ __forceinline__ float sw16(float a, float b) {
  const auto r = __builtin_amdgcn_permlane16_swap(__float_as_uint(a), __float_as_uint(b), false, false);
  return __uint_as_float(r[0]) + __uint_as_float(r[1]);
}
__device__ __forceinline__ float sum16(float v) { v += dpp<XOR1>(v); v += dpp<XOR2>(v); v += dpp<HMIR>(v); v += dpp<ROR8>(v); return v; }
__device__ __forceinline__ float sum64(float v) { v = sum16(v); v = sw16(v, v); return sw32(v, v); }
__device__ __forceinline__ float scatter8(const float* a, bool b3) {
  float y[4], z[2];
#pragma unroll
  for (int e = 0; e < 4; ++e) y[e] = sw32(a[e], a[e + 4]);
#pragma unroll
  for (int e = 0; e < 2; ++e) z[e] = sw16(y[e], y[e + 2]);
  return (b3 ? z[1] : z[0]) + dpp<ROR8>(b3 ? z[0] : z[1]);
}
typedef short s16x4 __attribute__((ext_vector_type(4)));
__device__ __forceinline__ bf16x8 tr8(LAS const unsigned* p, int step) {
  const s16x4 a = __builtin_amdgcn_ds_read_tr16_b64_v4i16((LAS s16x4*)p), b = __builtin_amdgcn_ds_read_tr16_b64_v4i16((LAS s16x4*)(p + step));
  return __builtin_bit_cast(bf16x8, __builtin_shufflevector(a, b, 0, 1, 2, 3, 4, 5, 6, 7));
}
__device__ __forceinline__ rsrc_t mk(const void* p, unsigned n) { return __builtin_amdgcn_make_buffer_rsrc((void*)p, 0, n, 0x00020000); }
__device__ __forceinline__ u32x4 ld_nt(rsrc_t r, unsigned v, unsigned s) { return __builtin_amdgcn_raw_buffer_load_b128(r, v, s, 2); }
__device__ __forceinline__ u32x4 ld_sc(rsrc_t r, unsigned v, unsigned s = 0) { return __builtin_amdgcn_raw_buffer_load_b128(r, v, s, 16); }
__device__ __forceinline__ void st_sc(rsrc_t r, unsigned v, u32x4 x) { __builtin_amdgcn_raw_buffer_store_b128(x, r, v, 0, 16); }
__device__ __forceinline__ unsigned now() { return (unsigned)__builtin_amdgcn_s_memrealtime(); }
__device__ __forceinline__ unsigned lds_peek(LAS unsigned* p) {
  return __builtin_amdgcn_readfirstlane(__hip_atomic_load(p, __ATOMIC_RELAXED, __HIP_MEMORY_SCOPE_WORKGROUP));
}
#define PIN() do { asm volatile("" ::: "memory"); __builtin_amdgcn_sched_barrier(0); } while (0)
#define WG_BAR() do { asm volatile("s_waitcnt lgkmcnt(0)" ::: "memory"); __builtin_amdgcn_s_barrier(); PIN(); } while (0)
#define STAMP(i) do { } while (0)
struct Tag { unsigned lo, hi, t32; };
__device__ __forceinline__ u32x4 enc2(float a, float b, const Tag& t) {
  return u32x4{(__float_as_uint(a) & 0xffffff00u) | t.lo, t.hi, (__float_as_uint(b) & 0xffffff00u) | t.lo, t.hi};
}
__device__ __forceinline__ unsigned bad2(u32x4 g, const Tag& t) { return (((g[0] ^ t.lo) | (g[2] ^ t.lo)) & 0xffu) | (g[1] ^ t.hi) | (g[3] ^ t.hi); }
__device__ __forceinline__ float dec(unsigned g) { return __uint_as_float(g & 0xffffff00u); }
__device__ __forceinline__ void helper_bar(LAS unsigned* cnt, unsigned target, int lane) {
  asm volatile("s_waitcnt lgkmcnt(0)" ::: "memory");
  if (lane == 0) __hip_atomic_fetch_add(cnt, 1u, __ATOMIC_RELAXED, __HIP_MEMORY_SCOPE_WORKGROUP);
  while (lds_peek(cnt) < target) { }
  asm volatile("" ::: "memory");
}
__device__ __forceinline__ float mix2(float t, f32x2 wa, f32x2 wb, unsigned wc, float acc) {
  constexpr float K0 = -2.f * L2E * 0.7978845608028654f, K1 = K0 * 0.044715f;
  const f32x2 u = rbf2(rbf2(wa * t) + wb);
  const f32x2 z = u * __builtin_elementwise_fma(u * u, (f32x2)K1, (f32x2)K0);
  const f32x2 d = f32x2{__builtin_amdgcn_exp2f(z[0]), __builtin_amdgcn_exp2f(z[1])} + 1.f;
  const f32x2 g = u * f32x2{__builtin_amdgcn_rcpf(d[0]), __builtin_amdgcn_rcpf(d[1])};
  return dot2(pk2(g[0], g[1]), wc, acc);
}
__device__ __forceinline__ float inv_sd(float var) { return __builtin_amdgcn_rcpf(rbf(__builtin_amdgcn_sqrtf(rbf(rbf(var) + 1e-5f)))); }

__global__ void __launch_bounds__(512, 2)
fused_decode(const unsigned short* x, const unsigned short* kc, const unsigned short* vc, const unsigned short* wq, const unsigned short* wo,
             const unsigned short* w1, const unsigned short* b1, const unsigned short* w2, const unsigned short* b2, unsigned short* out,
             unsigned char* ws) {
  __shared__ __attribute__((aligned(16))) unsigned lds_raw[LDS_DW];
  LAS unsigned* lds = (LAS unsigned*)lds_raw;
  LAS float* ldf = (LAS float*)lds_raw;
  const int tid = threadIdx.x, lane = tid & 63, w = __builtin_amdgcn_readfirstlane(tid >> 6);
  const int bid = blockIdx.x, h = bid >> 3, j = (bid + h) & 7, b = j >> 1, s = j & 1;
  const bool has_q = j >= 2 && s == 1, has_o = j < 2 || s == 0;
  const int hp = h >> 1, co = (h & 1) * 5 + (j < 2 ? j : b + 1);
  const int b2i = bid >> 6, cg = bid & 63;
  STAMP(0);
  const unsigned tstart = (QDELAY | SDELAY) ? now() : 0u;
  asm volatile("" :: "s"(x), "s"(kc), "s"(vc), "s"(wq), "s"(wo), "s"(w1), "s"(b1), "s"(w2), "s"(b2), "s"(out), "s"(ws));
  const rsrc_t wsr0 = mk(ws, WS_END);
  u32x2 ep = __builtin_amdgcn_raw_buffer_load_b64(wsr0, 0, 0, 16);
  bf16x8 Bf[12];

  if (w < 4) {
    if (PRIO) __builtin_amdgcn_s_setprio(2);
    const int r = lane >> 3, c = lane & 7;
    const int ntw = 256 + (j < 2 ? 0 : (s ? -SHIFT : SHIFT)) + ((bid & 1) ? -PSH : PSH), NT = (ntw + 3 - w) >> 2;
    const unsigned seq = (unsigned)(b * H + h) * SEQB;
    const int rot = __builtin_amdgcn_readfirstlane((ROTX) % ntw), y0 = s ? 512 - ntw : 0;
    const bool hi = r >> 2;
    const unsigned kb = PAIR == 1 ? (hi ? 171 : 0) : (hi ? 86 : 0), c1 = PAIR == 1 ? 171 : (hi ? 85 : 86), d1 = PAIR == 1 ? (hi ? 170 : 171) : 85,
                   c2 = PAIR == 1 ? (hi ? 342 : 341) : 171, d2 = PAIR == 1 ? 171 : (hi ? 85 : 86);
    constexpr int PB = PAIR == 1 ? 9 : 8;
    auto ty = [&](int i) -> int { const int y1 = i * 4 + w + rot; return y0 + (y1 >= ntw ? y1 - ntw : y1); };
    auto tv = [&](int i) -> unsigned {
      if (!PAIR) return (unsigned)(r * 384 + c * 16);
      const unsigned mm = ((4 * ty(i)) & ((1 << PB) - 1)) + (r & 3);
      return (mm + kb + (mm >= c1 ? d1 : 0) + (mm >= c2 ? d2 : 0)) * 384 + c * 16; };
    auto ts = [&](int i) -> unsigned {
      return __builtin_amdgcn_readfirstlane(PAIR ? seq + (unsigned)((4 * ty(i)) >> PB) * (768u << PB) : seq + ty(i) * TILEB); };
    const rsrc_t kr = mk(kc, KVB), vr = mk(vc, KVB);
    u32x4 K[NPRE][3], V[NPRE][3];
    WG_BAR();
    if (SDELAY) while (now() - tstart < SDELAY) __builtin_amdgcn_s_sleep(1);
#pragma unroll
    for (int p = 0; p < NPRE; ++p) {
      if (p) asm volatile("s_waitcnt vmcnt(3)" ::: "memory");
#pragma unroll
      for (int i = 0; i < 3; ++i) K[p][i] = ld_nt(kr, tv(p) + 128 * i, ts(p));
      PIN();
      if (p) asm volatile("s_waitcnt vmcnt(3)" ::: "memory");
#pragma unroll
      for (int i = 0; i < 3; ++i) V[p][i] = ld_nt(vr, tv(p) + 128 * i, ts(p));
      PIN();
    }
    WG_BAR();
    STAMP(1);
    u32x4 qf[3];
#pragma unroll
    for (int i = 0; i < 3; ++i) qf[i] = *(LAS const u32x4*)(lds + L_Q + (c + 8 * i) * 4);
    float m = __uint_as_float(0xff7f0000u), l = 0.f, acc[24];
#pragma unroll
    for (int k = 0; k < 24; ++k) acc[k] = 0.f;
    unsigned pace = 0;
    bool stamp3 = false;
    auto step_fn = [&](u32x4 (&Kc)[3], u32x4 (&Vc)[3], bool refill, unsigned nbytes, unsigned av, unsigned as) {
      float a0 = 0.f, a1 = 0.f, a2 = 0.f;
#pragma unroll
      for (int k = 0; k < 4; ++k) { a0 = dot2(Kc[0][k], qf[0][k], a0); a1 = dot2(Kc[1][k], qf[1][k], a1); a2 = dot2(Kc[2][k], qf[2][k], a2); }
      float a = (a0 + a1) + a2;
      asm volatile("" : "+v"(a));
      PIN();
      if (refill) {
        if (pace) { unsigned tn; while ((int)(((tn = now()) << 6) - pace) < 0) { }
                    if (stamp3) { if (lane == 0) lds[L_CNT + 3] = tn | 1u; stamp3 = false; } }
        const rsrc_t k2 = mk(kc, nbytes);
#pragma unroll
        for (int i = 0; i < 3; ++i) Kc[i] = ld_nt(k2, av + 128 * i, as);
      }
      PIN();
      f32x2 vf[12];
#pragma unroll
      for (int k = 0; k < 12; ++k) vf[k] = up2(Vc[k >> 2][k & 3]);
      if (EARLYV && refill) {
        asm volatile("" : "+v"(vf[0]), "+v"(vf[1]), "+v"(vf[2]), "+v"(vf[3]), "+v"(vf[4]), "+v"(vf[5]));
        asm volatile("" : "+v"(vf[6]), "+v"(vf[7]), "+v"(vf[8]), "+v"(vf[9]), "+v"(vf[10]), "+v"(vf[11]));
        PIN();
        const rsrc_t v2 = mk(vc, nbytes);
#pragma unroll
        for (int i = 0; i < 3; ++i) Vc[i] = ld_nt(v2, av + 128 * i, as);
        PIN();
      }
      a += dpp<XOR1>(a); a += dpp<XOR2>(a); a += dpp<HMIR>(a);
      const float sc = rbf(rbf(a) * SCALE);
      const float mn = fmaxf(m, sc);
      const float alpha = __builtin_amdgcn_exp2f((m - mn) * L2E), p = __builtin_amdgcn_exp2f((sc - mn) * L2E);
      l = fmaf(l, alpha, p); m = mn;
#pragma unroll
      for (int k = 0; k < 12; ++k) { acc[2 * k] = fmaf(p, vf[k][0], acc[2 * k] * alpha); acc[2 * k + 1] = fmaf(p, vf[k][1], acc[2 * k + 1] * alpha); }
      PIN();
      if (!EARLYV && refill) {
        const rsrc_t v2 = mk(vc, nbytes);
#pragma unroll
        for (int i = 0; i < 3; ++i) Vc[i] = ld_nt(v2, av + 128 * i, as);
      }
      PIN();
    };
#pragma unroll
    for (int p = 0; p < NPRE; ++p) step_fn(K[p], V[p], p == 0, KVB, tv(NPRE), ts(NPRE));
    int t = NPRE + 1;
    do {
      if (w == 1 && t == CKG && lane == 0) lds[L_CNT + 6] = 1;
      if (w == 0 && (t == CK1 || t == CK2)) { const unsigned tn = now() | 1u; if (lane == 0) lds[L_CNT + (t == CK1 ? 1 : 2)] = tn; }
      step_fn(K[0], V[0], true, KVB, tv(t), ts(t));
    } while (++t < SW);
    static_assert(NPRE < CK1 && CK1 < CK2 && CK2 < SW && NPRE < CKG && CKG < SW && SW + 2 <= CK3 && CK3 < 62 && (CK3 - SW) % 2 == 0 && SHIFT == 0 && PSH == 0, "every stamp the helpers wait for is reached");
    unsigned tend;
    while ((tend = lds_peek(lds + L_CNT + 4)) == 0) __builtin_amdgcn_s_sleep(1);
    unsigned tgt = now() << 6;
    int tau = (int)((tend << 6) - tgt) / (NT - 1 - SW);
#pragma unroll
    for (int i = 0; i < 3; ++i) K[1][i] = ld_nt(kr, tv(t) + 128 * i, ts(t));
    PIN();
#pragma unroll
    for (int i = 0; i < 3; ++i) V[1][i] = ld_nt(vr, tv(t) + 128 * i, ts(t));
    PIN();
    do {
      const unsigned t3 = lds_peek(lds + L_CNT + 5);
      if (t3 && t3 != tend) { tend = t3; tau = (int)((tend << 6) - tgt) / max(NT - 1 - t, 1); }
      tgt += tau;
      pace = t + 1 < NT ? tgt | 1u : 0u;
      step_fn(K[0], V[0], true, t + 1 < NT ? KVB : 0u, tv(t + 1), ts(t + 1));
      tgt += tau;
      pace = t + 2 < NT ? tgt | 1u : 0u;
      if (w == 0 && t + 2 == CK3) stamp3 = true;
      step_fn(K[1], V[1], true, t + 2 < NT ? KVB : 0u, tv(t + 2), ts(t + 2));
      t += 2;
    } while (t < NT);
    if (t == NT) step_fn(K[0], V[0], false, 0, 0, 0);
    float M = fmaxf(m, dpp<ROR8>(m));
    { const auto q16 = __builtin_amdgcn_permlane16_swap(__float_as_uint(M), __float_as_uint(M), false, false);
      M = fmaxf(__uint_as_float(q16[0]), __uint_as_float(q16[1]));
      const auto q32 = __builtin_amdgcn_permlane32_swap(__float_as_uint(M), __float_as_uint(M), false, false);
      M = fmaxf(__uint_as_float(q32[0]), __uint_as_float(q32[1])); }
    const float f = __builtin_amdgcn_exp2f((m - M) * L2E);
    l *= f; l += dpp<ROR8>(l); l = sw16(l, l); l = sw32(l, l);
#pragma unroll
    for (int k = 0; k < 24; ++k) acc[k] *= f;
#pragma unroll
    for (int i = 0; i < 3; ++i) ldf[L_REC + w * 196 + i * 64 + c * 8 + r] = scatter8(acc + i * 8, lane & 8);
    if (lane == 0) { ldf[L_REC + w * 196 + 192] = M; ldf[L_REC + w * 196 + 193] = l; }
  } else {
    const int hid = tid - 256, hw = w - 4;
    if (hid < 8) lds[L_CNT + hid] = 0;
    const int cq = (j - 2) >> 1, rg = hid >> 3, c8 = hid & 7;
    u32x4 W[20], X[5];
    const rsrc_t xr = mk(x, has_q ? 4 * D * 2 : 0), wqr = mk(wq, has_q ? WB : 0);
#pragma unroll
    for (int i = 0; i < 5; ++i) { const int ch = 20 * hw + (lane & 15) + 16 * i; X[i] = ld_nt(xr, (lane >> 4) * (D * 2) + (ch >= 80 ? ch - 80 : ch) * 16, 0); }
    PIN();
#pragma unroll
    for (int i = 0; i < 20; ++i) {
      W[i] = ld_nt(wqr, (unsigned)(lane >> 3) * (HD * 2) + c8 * 16, (unsigned)(160 * hw + 8 * i) * (HD * 2) + (h * DH + cq * 64) * 2);
      if ((i & 3) == 3) PIN();
    }
    if (WQ_WAIT < 25) asm volatile("s_waitcnt vmcnt(" STR(WQ_WAIT) ")" ::: "memory");
    WG_BAR();
    asm volatile("" : "+v"(ep));
    const unsigned long long E1 = ((unsigned long long)__builtin_amdgcn_readfirstlane(ep[1]) << 32 | (unsigned)__builtin_amdgcn_readfirstlane(ep[0])) + 1;
    const Tag tg{(unsigned)E1 & 0xffu, (unsigned)(E1 >> 8), (unsigned)E1};
    if (has_q) {
      float sa = 0.f, sb = 0.f;
#pragma unroll
      for (int i = 0; i < 20; i += 2) { sa = dot2(X[i >> 2][i & 3], 0x3f803f80u, sa); sb = dot2(X[i >> 2][(i & 3) + 1], 0x3f803f80u, sb); }
      const float mean = sum16(sa + sb) * (1.f / D);
      f32x2 s2 = {0.f, 0.f}, s3 = s2;
#pragma unroll
      for (int i = 0; i < 20; i += 2) {
        const f32x2 d = up2(X[i >> 2][i & 3]) - mean, e = up2(X[i >> 2][(i & 3) + 1]) - mean;
        s2 = __builtin_elementwise_fma(d, d, s2); s3 = __builtin_elementwise_fma(e, e, s3);
      }
      s2 += s3;
      const float inv = inv_sd(sum16(s2[0] + s2[1]) * (1.f / D)), mb = rbf(mean);
      LAS unsigned* xn = lds + L_XN + hw * 4 * 132;
#pragma unroll
      for (int i = 0; i < 2; ++i) {
        u32x4 o;
#pragma unroll
        for (int k = 0; k < 4; ++k) { const f32x2 y = rbf2(up2(X[i][k]) - mb) * inv; o[k] = pk2(y[0], y[1]); }
        *(LAS u32x4*)(xn + (lane >> 4) * 132 + ((lane & 15) + 16 * i) * 4) = o;
      }
      asm volatile("" ::: "memory");
      u32x4 af[5];
#pragma unroll
      for (int ks = 0; ks < 5; ++ks) af[ks] = *(LAS const u32x4*)(xn + (lane & 3) * 132 + ks * 16 + (lane >> 4) * 4);
      LAS unsigned* st = lds + L_WO + hw * 64 * 36;
      f32x4 dq[4];
#pragma unroll
      for (int nt = 0; nt < 4; ++nt) dq[nt] = f32x4{0.f, 0.f, 0.f, 0.f};
#pragma unroll
      for (int ks = 0; ks < 5; ++ks) {
        LAS unsigned* sk = st + (ks & 1) * 32 * 36;
#pragma unroll
        for (int i = 0; i < 4; ++i) *(LAS u32x4*)(sk + (8 * i + (lane >> 3)) * 36 + c8 * 4) = W[4 * ks + i];
        asm volatile("" ::: "memory");
        LAS const unsigned* rd = sk + (8 * (lane >> 4) + ((lane >> 2) & 3)) * 36 + (lane & 3) * 2;
#pragma unroll
        for (int nt = 0; nt < 4; ++nt) dq[nt] = __builtin_amdgcn_mfma_f32_16x16x32_bf16(__builtin_bit_cast(bf16x8, af[ks]), tr8(rd + nt * 8, 4 * 36), dq[nt], 0, 0, 0);
        asm volatile("" ::: "memory");
      }
      const bool g1 = lane & 16, g2 = lane & 32;
#pragma unroll
      for (int nt = 0; nt < 4; ++nt) {
        const float a01 = g1 ? dq[nt][1] : dq[nt][0], a23 = g1 ? dq[nt][3] : dq[nt][2];
        ldf[L_QR + hw * 256 + (lane >> 4) * 64 + nt * 16 + (lane & 15)] = g2 ? a23 : a01;
      }
      helper_bar(lds + L_CNT, 4, lane);
      if (hw == 0) {
        f32x4 a = *(LAS const f32x4*)(ldf + L_QR + 4 * lane);
#pragma unroll
        for (int ww = 1; ww < 4; ++ww) a += *(LAS const f32x4*)(ldf + L_QR + ww * 256 + 4 * lane);
        st_sc(wsr0, WS_QP + ((h * 4 + (lane >> 4)) * 96 + cq * 32 + (lane & 15) * 2) * 8, u32x4{pk2(a[0], a[1]), tg.t32, pk2(a[2], a[3]), tg.t32});
      }
    }
    STAMP(3);
    if (hw == 3 && !NOQ) {
      if (QDELAY) while (now() - tstart < QDELAY) __builtin_amdgcn_s_sleep(2);
      const unsigned o = lane < 48 ? WS_QP + (h * 4 + b) * 96 * 8 + lane * 16 : OOB, vm = lane < 48 ? ~0u : 0u;
      u32x4 g;
#pragma nounroll
      for (unsigned spin = 0; spin < SPIN_CAP; ++spin) {
        asm volatile("" ::: "memory");
        g = ld_sc(wsr0, o);
        if (__all((((g[1] ^ tg.t32) | (g[3] ^ tg.t32)) & vm) == 0)) break;
        __builtin_amdgcn_s_sleep(2);
      }
      if (lane < 48) *(LAS u32x2*)(lds + L_Q + 2 * lane) = u32x2{g[0], g[2]};
      STAMP(1);
    }
    WG_BAR();
    const rsrc_t wor = mk(wo, has_o ? WB : 0);
    u32x4 Wo[12];
#pragma unroll
    for (int i = 0; i < 12; ++i) Wo[i] = ld_nt(wor, (unsigned)rg * (D * 2) + c8 * 16, (unsigned)(hp * 2 * DH + 32 * i) * (D * 2) + co * 128);
    if (hid < 192) {
      *(LAS u32x4*)(lds + L_MIX + hid * 4) = __builtin_nontemporal_load((const u32x4*)(w1 + hid * 8));
      *(LAS u32x4*)(lds + L_MIX + 768 + hid * 4) = __builtin_nontemporal_load((const u32x4*)(b1 + hid * 8));
      *(LAS u32x4*)(lds + L_MIX + 1536 + hid * 4) = __builtin_nontemporal_load((const u32x4*)(w2 + hid * 8));
    }
    if (hid < 80) *(LAS u32x4*)(lds + L_X + hid * 4) = __builtin_nontemporal_load((const u32x4*)(x + b2i * D + hid * 8));
    if (hid == 80) lds[L_X + 320] = b2[0];
#pragma unroll
    for (int i = 0; i < 12; ++i) *(LAS u32x4*)(lds + L_WO + (rg + 32 * i) * 36 + c8 * 4) = Wo[i];
    helper_bar(lds + L_CNT, has_q ? 8 : 4, lane);
    LAS const unsigned* wl = lds + L_WO + (8 * (lane >> 4) + ((lane >> 2) & 3)) * 36 + hw * 8 + (lane & 3) * 2;
#pragma unroll
    for (int ks = 0; ks < 12; ++ks) Bf[ks] = tr8(wl + ks * 32 * 36, 4 * 36);
    if (hw != 2) {
      const int tw = hw == 3 ? 2 : hw, tl = tw * 64 + lane;
      f32x2 wa[4], wb[4];
      unsigned wc[4];
#pragma unroll
      for (int k = 0; k < 4; ++k) { wa[k] = up2(lds[L_MIX + tl + 192 * k]); wb[k] = up2(lds[L_MIX + 768 + tl + 192 * k]); wc[k] = lds[L_MIX + 1536 + tl + 192 * k]; }
      const unsigned bits0 = (TAB0 + ((bid * 32) & 4095)) | (bid >> 7) << 15;
#pragma nounroll
      for (int i0 = 0; i0 < 32; i0 += 8) {
        float ac[8];
#pragma unroll
        for (int i = 0; i < 8; ++i) {
          const float t = __uint_as_float((bits0 + i0 + i) << 16);
          ac[i] = 0.f;
#pragma unroll
          for (int k = 0; k < 4; ++k) ac[i] = mix2(t, wa[k], wb[k], wc[k], ac[i]);
        }
        float y = scatter8(ac, lane & 8);
        y += dpp<XOR1>(y); y += dpp<XOR2>(y); y += dpp<HMIR>(y);
        if ((lane & 7) == 0) ldf[L_QR + tw * 32 + i0 + (lane >> 3)] = y;
      }
      helper_bar(lds + L_CNT, has_q ? 11 : 7, lane);
      if (hw == 3 && lane < 8) {
        f32x4 y = *(LAS const f32x4*)(ldf + L_QR + 4 * lane);
#pragma unroll
        for (int ww = 1; ww < 3; ++ww) y += *(LAS const f32x4*)(ldf + L_QR + ww * 32 + 4 * lane);
        const float bias = bflo(lds[L_X + 320]);
        const f32x2 y0 = rbf2(f32x2{y[0], y[1]}) + bias, y1 = rbf2(f32x2{y[2], y[3]}) + bias;
        st_sc(wsr0, WS_TB + bid * 128 + lane * 16, u32x4{pk2(y0[0], y0[1]), tg.t32, pk2(y1[0], y1[1]), tg.t32});
      }
      if (hw < 2) {
        while (lds_peek(lds + L_CNT + 6) == 0) __builtin_amdgcn_s_sleep(16);
#pragma nounroll
        for (int half = 0; half < 2; ++half) {
          u32x4 g[8];
#pragma nounroll
          for (unsigned spin = 0; spin < SPIN_CAP; ++spin) {
            asm volatile("" ::: "memory");
            unsigned bad = 0;
#pragma unroll
            for (int i = 0; i < 8; ++i) g[i] = ld_sc(wsr0, WS_TB + hid * 16, (half * 8 + i) * 2048);
#pragma unroll
            for (int i = 0; i < 8; ++i) bad |= (g[i][1] ^ tg.t32) | (g[i][3] ^ tg.t32);
            if (__all(bad == 0)) break;
            __builtin_amdgcn_s_sleep(32);
          }
#pragma unroll
          for (int i = 0; i < 8; ++i) *(LAS u32x2*)(lds + L_TAB + ((half * 8 + i) * 128 + hid) * 2) = u32x2{g[i][0], g[i][2]};
        }
      }
    }
    if (hw == 2) {
      unsigned tm0 = 0;
#pragma nounroll
      for (int ck = 0; ck < 3; ++ck) {
        unsigned t2;
        while ((t2 = lds_peek(lds + L_CNT + 1 + ck)) == 0) __builtin_amdgcn_s_sleep(8);
        if ((bid & 3) == ((h >> 1) & 3) && lane == 0) __builtin_amdgcn_raw_buffer_store_b64(u32x2{t2, tg.t32}, wsr0, WS_TS + (ck * 64 + (bid >> 2)) * 128, 0, 16);
        while (now() - t2 < WAITT) __builtin_amdgcn_s_sleep(8);
        const u32x2 g = __builtin_amdgcn_raw_buffer_load_b64(wsr0, WS_TS + (ck * 64 + lane) * 128, 0, 16);
        const unsigned tm = t2 + (int)(sum64(g[1] == tg.t32 ? (float)min(max((int)(g[0] - t2), -2000), 2000) : (float)(WAITT + 30)) * (1.f / 64));
        const float left = ck == 1 ? (63.f - CK2) / (CK2 - CK1) * (1.f - EPS) : (63.f - CK3) / (CK3 - CK2) * (1.f - EPS3);
        if (ck && lane == 0) lds[L_CNT + 3 + ck] = (tm + (int)((float)(int)(tm - tm0) * left)) | 1u;
        tm0 = tm;
      }
    }
  }
  const unsigned long long E1 = ((unsigned long long)__builtin_amdgcn_readfirstlane(ep[1]) << 32 | (unsigned)__builtin_amdgcn_readfirstlane(ep[0])) + 1;
  const Tag tg{(unsigned)E1 & 0xffu, (unsigned)(E1 >> 8), (unsigned)E1};
  const int u = tid - 256, uu = u < 96 ? u : 96;
  unsigned a_p1 = u <= 96 ? WS_P1 + (h * 8 + j) * P1REC + u * 16 : OOB;
  unsigned a_p2 = (lane & 1) ? OOB : WS_P2 + (((lane >> 4) * 16 + hp) * D + co * 64 + (w - 4) * 16 + (lane & 15)) * 8;
  LAS const float* a_rec = ldf + L_REC + 2 * (w < 4 ? 0 : uu);
  LAS const unsigned* a_o = lds + L_O + (lane & 3) * OSTR + (lane >> 4) * 4;
  LAS const unsigned* own = lds + L_HH + cg * 5 + (lane < 5 ? lane : 0);
  unsigned a_out = lane < 5 ? (b2i * D + cg * 10 + 2 * lane) * 2 : OOB;
  asm volatile("" : "+v"(a_p1), "+v"(a_p2), "+v"(a_rec), "+v"(a_o), "+v"(own), "+v"(a_out));
  const rsrc_t wsr = wsr0;
  WG_BAR();
  STAMP(2);
  if (w == 4 || w == 5) {
    float mm[4];
    f32x2 rv[4];
#pragma unroll
    for (int i = 0; i < 4; ++i) { mm[i] = ldf[L_REC + i * 196 + 192]; rv[i] = *(LAS const f32x2*)(a_rec + i * 196); }
    __builtin_amdgcn_sched_barrier(0);
    const float M = fmaxf(fmaxf(mm[0], mm[1]), fmaxf(mm[2], mm[3]));
    f32x2 a = {0.f, 0.f};
#pragma unroll
    for (int i = 0; i < 4; ++i) a += __builtin_amdgcn_exp2f((mm[i] - M) * L2E) * rv[i];
    st_sc(wsr, a_p1, enc2(u < 96 ? a[0] : M, a[1], tg));
    if (w == 4) STAMP(1);
  }
  const unsigned tc5 = lds_peek(lds + L_CNT + 5), tcom = tc5 ? tc5 : lds_peek(lds + L_CNT + 4);
  if (has_o) {
    {
      const unsigned oa = WS_P1 + (hp * 16 + (w >> 2) * 8 + 2 * (w & 3)) * P1REC + lane * 16, ob = lane < 33 ? oa + 1024 : OOB;
      const unsigned vm = lane < 33 ? ~0u : 0u;
      LAS unsigned* da = lds + L_O + (w & 3) * OSTR + (w >> 2) * 96 + lane;
      LAS unsigned* db = lane < 32 ? da + 64 : lds + L_QR + lane;
      asm volatile("" : "+v"(da), "+v"(db));
      u32x4 a[2], c[2];
      unsigned pend = 3;
      if (C1) while ((int)(now() - tcom) < C1) { }
#pragma nounroll
      for (unsigned spin = 0; spin < SPIN_CAP; ++spin) {
        asm volatile("" ::: "memory");
#pragma unroll
        for (int i = 0; i < 2; ++i) if (pend >> i & 1) { a[i] = ld_sc(wsr, oa, i * P1REC); c[i] = ld_sc(wsr, ob, i * P1REC); }
        asm volatile("s_waitcnt vmcnt(0)" ::: "memory");
#pragma unroll
        for (int i = 0; i < 2; ++i) if (pend >> i & 1) { if (__all((bad2(a[i], tg) | (bad2(c[i], tg) & vm)) == 0)) pend &= ~(1u << i); }
        if (!pend) break;
        __builtin_amdgcn_s_sleep(H1SLP);
      }
      STAMP(4);
      const float m0 = dec(__builtin_amdgcn_readlane(c[0][0], 32)), m1 = dec(__builtin_amdgcn_readlane(c[1][0], 32)), M = fmaxf(m0, m1);
      const float l0 = dec(__builtin_amdgcn_readlane(c[0][2], 32)), l1 = dec(__builtin_amdgcn_readlane(c[1][2], 32));
      float w0 = __builtin_amdgcn_exp2f((m0 - M) * L2E), w1_ = __builtin_amdgcn_exp2f((m1 - M) * L2E);
      const float inv = __builtin_amdgcn_rcpf(fmaf(w0, l0, w1_ * l1));
      w0 *= inv; w1_ *= inv;
      const f32x2 oa_ = __builtin_elementwise_fma(f32x2{dec(a[0][0]), dec(a[0][2])}, (f32x2)w0, f32x2{dec(a[1][0]), dec(a[1][2])} * w1_);
      const f32x2 oc_ = __builtin_elementwise_fma(f32x2{dec(c[0][0]), dec(c[0][2])}, (f32x2)w0, f32x2{dec(c[1][0]), dec(c[1][2])} * w1_);
      *da = pk2(oa_[0], oa_[1]); *db = pk2(oc_[0], oc_[1]);
    }
    WG_BAR();
    if (w >= 4) {
      u32x4 af[12];
#pragma unroll
      for (int ks = 0; ks < 12; ++ks) af[ks] = *(LAS const u32x4*)(a_o + ks * 16);
      __builtin_amdgcn_sched_barrier(0);
      f32x4 d0 = {0.f, 0.f, 0.f, 0.f}, d1 = d0;
#pragma unroll
      for (int ks = 0; ks < 12; ks += 2) {
        d0 = __builtin_amdgcn_mfma_f32_16x16x32_bf16(__builtin_bit_cast(bf16x8, af[ks]), Bf[ks], d0, 0, 0, 0);
        d1 = __builtin_amdgcn_mfma_f32_16x16x32_bf16(__builtin_bit_cast(bf16x8, af[ks + 1]), Bf[ks + 1], d1, 0, 0, 0);
      }
      d0 += d1;
      const bool g1 = lane & 16, g2 = lane & 32;
      const float a01 = g1 ? d0[1] : d0[0], a23 = g1 ? d0[3] : d0[2], a = g2 ? a23 : a01;
      st_sc(wsr, a_p2, enc2(a, dpp<XOR1>(a), tg));
      if (w != 6) STAMP(3);
    }
  }
  if (w < 5) {
    const unsigned o = WS_P2 + b2i * 16 * D * 8 + tid * 16;
    f32x2 xr = up2(lds[L_X + tid]);
    asm volatile("" : "+v"(xr));
    u32x4 g[16];
    unsigned pend = 15;
    if (C2) while ((int)(now() - tcom) < C2) { }
#pragma nounroll
    for (unsigned spin = 0; spin < SPIN_CAP; ++spin) {
      asm volatile("" ::: "memory");
#pragma unroll
      for (int q = 0; q < 4; ++q) if (pend >> q & 1) {
#pragma unroll
        for (int i = 4 * q; i < 4 * q + 4; ++i) g[i] = ld_sc(wsr, o, i * D * 8);
      }
#pragma unroll
      for (int q = 0; q < 4; ++q) if (pend >> q & 1) {
        if (__all((bad2(g[4 * q], tg) | bad2(g[4 * q + 1], tg) | bad2(g[4 * q + 2], tg) | bad2(g[4 * q + 3], tg)) == 0)) pend &= ~(1u << q);
      }
      if (!pend) break;
      __builtin_amdgcn_s_sleep(H2SLP);
    }
    STAMP(5);
    f32x2 y0 = {0.f, 0.f}, y1 = y0;
#pragma unroll
    for (int i = 0; i < 16; i += 2) { y0 += f32x2{dec(g[i][0]), dec(g[i][2])}; y1 += f32x2{dec(g[i + 1][0]), dec(g[i + 1][2])}; }
    const f32x2 hs = xr + rbf2(y0 + y1);
    const unsigned hp2 = pk2(hs[0], hs[1]);
    lds[L_HH + tid] = hp2;
    const f32x2 hv = up2(hp2);
    const float piv = __uint_as_float(__builtin_amdgcn_readfirstlane(__float_as_uint(hv[0])));
    const f32x2 d = hv - piv, dd = d * d;
    const float sp = sw32(d[0] + d[1], dd[0] + dd[1]), sq = sum16(sw16(sp, sp));
    const float s1 = __uint_as_float(__builtin_amdgcn_readlane(__float_as_uint(sq), 0)), s2 = __uint_as_float(__builtin_amdgcn_readlane(__float_as_uint(sq), 32));
    *(LAS f32x2*)(ldf + L_ST + 2 * w) = f32x2{fmaf(s1, 1.f / 128, piv), fmaf(-s1, s1 * (1.f / 128), s2)};
  }
  WG_BAR();
  if (w == 0) {
    const f32x4 sa = *(LAS const f32x4*)(ldf + L_ST), sb = *(LAS const f32x4*)(ldf + L_ST + 4);
    const f32x2 sc = *(LAS const f32x2*)(ldf + L_ST + 8);
    const unsigned hw2 = *own;
    __builtin_amdgcn_sched_barrier(0);
    const f32x2 hv = up2(hw2);
    const float mean = (((sa[0] + sa[2]) + (sb[0] + sb[2])) + sc[0]) * 0.2f;
    const float e0 = sa[0] - mean, e1 = sa[2] - mean, e2 = sb[0] - mean, e3 = sb[2] - mean, e4 = sc[0] - mean;
    const float var = fmaf(fmaf(e0, e0, e1 * e1) + fmaf(e2, e2, e3 * e3) + e4 * e4, 0.2f, (((sa[1] + sa[3]) + (sb[1] + sb[3])) + sc[1]) * (1.f / D));
    const float inv = inv_sd(var), mb = rbf(mean);
    const float inv2 = inv_sd(var * inv * inv), mb2 = rbf((mean - mb) * inv);
    const f32x2 tv = rbf2(rbf2(rbf2(rbf2(hv - mb) * inv) - mb2) * inv2);
    STAMP(6);
    const unsigned tb = pk2(tv[0], tv[1]), i0 = (tb & 0x7fffu) - TAB0, i1 = ((tb >> 16) & 0x7fffu) - TAB0;
    const rsrc_t outr = mk(out, 4 * D * 2);
    LAS const unsigned short* tab = (LAS const unsigned short*)(lds + L_TAB);
    f32x2 y = {bflo(tab[(i0 & 4095u) + ((tb >> 3) & 4096u)]), bflo(tab[(i1 & 4095u) + ((tb >> 19) & 4096u)])};
    if (!__all((i0 | i1) < 4096u) || FORCE_FB) {
      const float bias = bflo(lds[L_X + 320]);
#pragma nounroll
      for (int i = 0; i < 10; ++i) {
        const float t = __shfl(i & 1 ? tv[1] : tv[0], i >> 1);
        float a = 0.f;
#pragma nounroll
        for (int k = 0; k < 12; ++k) a = mix2(t, up2(lds[L_MIX + lane + 64 * k]), up2(lds[L_MIX + 768 + lane + 64 * k]), lds[L_MIX + 1536 + lane + 64 * k], a);
        a = rbf(rbf(sum64(a)) + bias);
        if (lane == (i >> 1)) { if (i & 1) y[1] = a; else y[0] = a; }
      }
    }
    const f32x2 o = hv + y;
    __builtin_amdgcn_raw_buffer_store_b32(pk2(o[0], o[1]), outr, a_out, 0, OUTAUX);
  }
  if (bid == 0 && tid == 64) __builtin_amdgcn_raw_buffer_store_b64(u32x2{(unsigned)E1, (unsigned)(E1 >> 32)}, wsr, 0, 0, 16);
}

extern "C" void kernel_launch(void* const* d_in, const int* in_sizes, int n_in, void* d_out, int out_size, void* d_ws, size_t ws_size,
                              hipStream_t stream) {
  typedef const unsigned short* P;
  fused_decode<<<256, 512, 0, stream>>>((P)d_in[0], (P)d_in[1], (P)d_in[2], (P)d_in[3], (P)d_in[4], (P)d_in[6], (P)d_in[7], (P)d_in[8], (P)d_in[9],
                                        (unsigned short*)d_out, (unsigned char*)d_ws);
}
```

```cpp
#include <hip/hip_runtime.h>
#include <stdint.h>

typedef unsigned u32x4 __attribute__((ext_vector_type(4)));
typedef unsigned u32x2 __attribute__((ext_vector_type(2)));
typedef float f32x4 __attribute__((ext_vector_type(4)));
typedef float f32x2 __attribute__((ext_vector_type(2)));
typedef __bf16 bf16x2 __attribute__((ext_vector_type(2)));
typedef __bf16 bf16x8 __attribute__((ext_vector_type(8)));
typedef __amdgpu_buffer_rsrc_t rsrc_t;
#define LAS __attribute__((address_space(3)))

#define NPRE 6
#define QDELAY 600
#define PAIR 1
#define WAITT 400
#define CK1 14
#define CK2 26
#define SW 32
#define EPS 0.0f
#define C1 215
#define C2 415
#define CKG 28
#define FORCE_FB 0
#define STAG 85
#define STK 37
#define CK3 44
#define QSLP 8
#define EPS3 -0.02f
#define ROTX 0
#define STR2(x) #x
#define STR(x) STR2(x)

constexpr int D = 640, H = 32, SK = 4096, DH = 192, HD = H * DH, F = 1536;
constexpr unsigned SEQB = SK * DH * 2, TILEB = 8 * DH * 2, KVB = 4u * H * SEQB, WB = D * HD * 2;
constexpr unsigned P1REC = 896;
constexpr unsigned WS_TS = 4096, WS_TB = WS_TS + 4 * 64 * 128, WS_QP = WS_TB + 256 * 128, WS_P1 = WS_QP + H * 4 * 96 * 8, WS_P2 = WS_P1 + 256 * P1REC, WS_END = WS_P2 + 4 * 16 * D * 4;
constexpr unsigned OOB = 0x7ffffff0u;
constexpr unsigned SPIN_CAP = 1u << 17;
constexpr unsigned TAB0 = 100u << 7;
constexpr float L2E = 1.4426950408889634f, SCALE = 0.07216878364870322f;
constexpr int OSTR = 208;
constexpr int L_CNT = 0, L_Q = 12, L_REC = L_Q + 96, L_O = L_REC + 4 * 196, L_RED = L_O + 4 * OSTR, L_ST = L_RED + 8 * 16, L_X = L_ST + 16, L_HH = L_X + 324,
              L_XN = L_HH + 320, L_QR = L_XN + 16 * 132, L_MIX = L_QR + 4 * 256, L_WO = L_MIX + 3 * F / 2, L_Y = L_WO, L_TAB = L_WO + 2 * DH * 36, L_END = L_TAB + 4096;
constexpr int LDS_DW = 26624;
static_assert(L_END <= LDS_DW && L_REC % 4 == 0 && L_O % 4 == 0 && L_XN % 4 == 0 && L_QR % 4 == 0 && L_MIX % 4 == 0 && L_WO % 4 == 0, "lds map");

__device__ __forceinline__ float bflo(unsigned w) { return __uint_as_float(w << 16); }
__device__ __forceinline__ float bfhi(unsigned w) { return __uint_as_float(w & 0xffff0000u); }
__device__ __forceinline__ unsigned pk2(float lo, float hi) { unsigned r; asm("v_cvt_pk_bf16_f32 %0, %1, %2" : "=v"(r) : "v"(lo), "v"(hi)); return r; }
__device__ __forceinline__ float rbf(float f) { return (float)(__bf16)f; }
__device__ __forceinline__ f32x2 up2(unsigned w) { return f32x2{bflo(w), bfhi(w)}; }
__device__ __forceinline__ f32x2 rbf2(f32x2 v) { return up2(pk2(v[0], v[1])); }
__device__ __forceinline__ float dot2(unsigned a, unsigned b, float c) {
  return __builtin_amdgcn_fdot2_f32_bf16(__builtin_bit_cast(bf16x2, a), __builtin_bit_cast(bf16x2, b), c, false);
}
template <int CTRL> __device__ __forceinline__ float dpp(float x) {
  return __builtin_bit_cast(float, __builtin_amdgcn_mov_dpp(__builtin_bit_cast(int, x), CTRL, 0xf, 0xf, true));
}
constexpr int XOR1 = 0xB1, XOR2 = 0x4E, HMIR = 0x141, ROR8 = 0x128;
__device__ __forceinline__ float sw32(float a, float b) {
  const auto r = __builtin_amdgcn_permlane32_swap(__float_as_uint(a), __float_as_uint(b), false, false);
  return __uint_as_float(r[0]) + __uint_as_float(r[1]);
}
__device__ __forceinline__ float sw16(float a, float b) {
  const auto r = __builtin_amdgcn_permlane16_swap(__float_as_uint(a), __float_as_uint(b), false, false);
  return __uint_as_float(r[0]) + __uint_as_float(r[1]);
}
__device__ __forceinline__ float sum16(float v) { v += dpp<XOR1>(v); v += dpp<XOR2>(v); v += dpp<HMIR>(v); v += dpp<ROR8>(v); return v; }
__device__ __forceinline__ float sum64(float v) { v = sum16(v); v = sw16(v, v); return sw32(v, v); }
__device__ __forceinline__ float scatter8(const float* a, bool b3) {
  float y[4], z[2];
#pragma unroll
  for (int e = 0; e < 4; ++e) y[e] = sw32(a[e], a[e + 4]);
#pragma unroll
  for (int e = 0; e < 2; ++e) z[e] = sw16(y[e], y[e + 2]);
  return (b3 ? z[1] : z[0]) + dpp<ROR8>(b3 ? z[0] : z[1]);
}
typedef short s16x4 __attribute__((ext_vector_type(4)));
__device__ __forceinline__ bf16x8 tr8(LAS const unsigned* p, int step) {
  const s16x4 a = __builtin_amdgcn_ds_read_tr16_b64_v4i16((LAS s16x4*)p), b = __builtin_amdgcn_ds_read_tr16_b64_v4i16((LAS s16x4*)(p + step));
  return __builtin_bit_cast(bf16x8, __builtin_shufflevector(a, b, 0, 1, 2, 3, 4, 5, 6, 7));
}
__device__ __forceinline__ rsrc_t mk(const void* p, unsigned n) { return __builtin_amdgcn_make_buffer_rsrc((void*)p, 0, n, 0x00020000); }
__device__ __forceinline__ u32x4 ld_nt(rsrc_t r, unsigned v, unsigned s) { return __builtin_amdgcn_raw_buffer_load_b128(r, v, s, 2); }
__device__ __forceinline__ u32x4 ld_sc(rsrc_t r, unsigned v, unsigned s = 0) { return __builtin_amdgcn_raw_buffer_load_b128(r, v, s, 16); }
__device__ __forceinline__ void st_sc(rsrc_t r, unsigned v, u32x4 x) { __builtin_amdgcn_raw_buffer_store_b128(x, r, v, 0, 16); }
__device__ __forceinline__ unsigned now() { return (unsigned)__builtin_amdgcn_s_memrealtime(); }
__device__ __forceinline__ unsigned lds_peek(LAS unsigned* p) {
  return __builtin_amdgcn_readfirstlane(__hip_atomic_load(p, __ATOMIC_RELAXED, __HIP_MEMORY_SCOPE_WORKGROUP));
}
#define PIN() do { asm volatile("" ::: "memory"); __builtin_amdgcn_sched_barrier(0); } while (0)
#define WG_BAR() do { asm volatile("s_waitcnt lgkmcnt(0)" ::: "memory"); __builtin_amdgcn_s_barrier(); PIN(); } while (0)
#define STAMP(i) do { } while (0)
struct Tag { unsigned b0, b1, b2, b3, t32; };
__device__ __forceinline__ Tag make_tag(unsigned long long e) {
  const unsigned t = (unsigned)e;
  return Tag{t & 0xffu, (t + (t >> 8)) & 0xffu, (t + (t >> 16)) & 0xffu, (t + (t >> 24)) & 0xffu, t};
}
__device__ __forceinline__ u32x4 enc4(f32x4 v, const Tag& t) {
  return u32x4{(__float_as_uint(v[0]) & 0xffffff00u) | t.b0, (__float_as_uint(v[1]) & 0xffffff00u) | t.b1, (__float_as_uint(v[2]) & 0xffffff00u) | t.b2,
               (__float_as_uint(v[3]) & 0xffffff00u) | t.b3};
}
__device__ __forceinline__ unsigned bad4(u32x4 g, const Tag& t) { return ((g[0] ^ t.b0) | (g[1] ^ t.b1) | (g[2] ^ t.b2) | (g[3] ^ t.b3)) & 0xffu; }
__device__ __forceinline__ f32x4 dec4(u32x4 g) { return __builtin_bit_cast(f32x4, g & 0xffffff00u); }
__device__ __forceinline__ float dec(unsigned g) { return __uint_as_float(g & 0xffffff00u); }
__device__ __forceinline__ void helper_bar(LAS unsigned* cnt, unsigned target, int lane) {
  asm volatile("s_waitcnt lgkmcnt(0)" ::: "memory");
  if (lane == 0) __hip_atomic_fetch_add(cnt, 1u, __ATOMIC_RELAXED, __HIP_MEMORY_SCOPE_WORKGROUP);
  while (lds_peek(cnt) < target) { }
  asm volatile("" ::: "memory");
}
__device__ __forceinline__ float mix2(float t, f32x2 wa, f32x2 wb, unsigned wc, float acc) {
  constexpr float K0 = -2.f * L2E * 0.7978845608028654f, K1 = K0 * 0.044715f;
  const f32x2 u = rbf2(rbf2(wa * t) + wb);
  const f32x2 z = u * __builtin_elementwise_fma(u * u, (f32x2)K1, (f32x2)K0);
  const f32x2 d = f32x2{__builtin_amdgcn_exp2f(z[0]), __builtin_amdgcn_exp2f(z[1])} + 1.f;
  const f32x2 g = u * f32x2{__builtin_amdgcn_rcpf(d[0]), __builtin_amdgcn_rcpf(d[1])};
  return dot2(pk2(g[0], g[1]), wc, acc);
}
__device__ __forceinline__ float inv_sd(float var) { return __builtin_amdgcn_rcpf(rbf(__builtin_amdgcn_sqrtf(rbf(rbf(var) + 1e-5f)))); }

__global__ void __launch_bounds__(512, 2)
fused_decode(const unsigned short* x, const unsigned short* kc, const unsigned short* vc, const unsigned short* wq, const unsigned short* wo,
             const unsigned short* w1, const unsigned short* b1, const unsigned short* w2, const unsigned short* b2, unsigned short* out,
             unsigned char* ws) {
  __shared__ __attribute__((aligned(16))) unsigned lds_raw[LDS_DW];
  LAS unsigned* lds = (LAS unsigned*)lds_raw;
  LAS float* ldf = (LAS float*)lds_raw;
  const int tid = threadIdx.x, lane = tid & 63, w = __builtin_amdgcn_readfirstlane(tid >> 6);
  const int bid = blockIdx.x, h = bid >> 3, j = (bid + h) & 7, b = j >> 1, s = j & 1;
  const bool has_q = j >= 2 && s == 1, has_o = j < 2 || s == 0;
  const int hp = h >> 1, co = (h & 1) * 5 + (j < 2 ? j : b + 1);
  const int b2i = bid >> 6, cg = bid & 63;
  STAMP(0);
  const unsigned tstart = now();
  asm volatile("" :: "s"(x), "s"(kc), "s"(vc), "s"(wq), "s"(wo), "s"(w1), "s"(b1), "s"(w2), "s"(b2), "s"(out), "s"(ws));
  const rsrc_t wsr0 = mk(ws, WS_END);
  u32x2 ep = __builtin_amdgcn_raw_buffer_load_b64(wsr0, 0, 0, 16);
  bf16x8 Bf[12];

  if (w < 4) {
    __builtin_amdgcn_s_setprio(2);
    const int r = lane >> 3, c = lane & 7;
    constexpr int ntw = 256, NT = 64;
    const unsigned seq = (unsigned)(b * H + h) * SEQB;
    const int rot = __builtin_amdgcn_readfirstlane((ROTX) % ntw), y0 = s ? 512 - ntw : 0;
    const bool hi = r >> 2;
    const unsigned kb = PAIR == 1 ? (hi ? 171 : 0) : (hi ? 86 : 0), c1 = PAIR == 1 ? 171 : (hi ? 85 : 86), d1 = PAIR == 1 ? (hi ? 170 : 171) : 85,
                   c2 = PAIR == 1 ? (hi ? 342 : 341) : 171, d2 = PAIR == 1 ? 171 : (hi ? 85 : 86);
    constexpr int PB = PAIR == 1 ? 9 : 8;
    auto ty = [&](int i) -> int { const int y1 = i * 4 + w + rot; return y0 + (y1 >= ntw ? y1 - ntw : y1); };
    auto tv = [&](int i) -> unsigned {
      if (!PAIR) return (unsigned)(r * 384 + c * 16);
      const unsigned mm = ((4 * ty(i)) & ((1 << PB) - 1)) + (r & 3);
      return (mm + kb + (mm >= c1 ? d1 : 0) + (mm >= c2 ? d2 : 0)) * 384 + c * 16; };
    auto ts = [&](int i) -> unsigned {
      return __builtin_amdgcn_readfirstlane(PAIR ? seq + (unsigned)((4 * ty(i)) >> PB) * (768u << PB) : seq + ty(i) * TILEB); };
    const rsrc_t kr = mk(kc, KVB), vr = mk(vc, KVB);
    u32x4 K[NPRE][3], V[NPRE][3];
    WG_BAR();
#pragma unroll
    for (int p = 0; p < NPRE; ++p) {
      if (p) asm volatile("s_waitcnt vmcnt(3)" ::: "memory");
#pragma unroll
      for (int i = 0; i < 3; ++i) K[p][i] = ld_nt(kr, tv(p) + 128 * i, ts(p));
      PIN();
      if (p) asm volatile("s_waitcnt vmcnt(3)" ::: "memory");
#pragma unroll
      for (int i = 0; i < 3; ++i) V[p][i] = ld_nt(vr, tv(p) + 128 * i, ts(p));
      PIN();
    }
    while (lds_peek(lds + L_CNT + 7) == 0) __builtin_amdgcn_s_sleep(1);
    PIN();
    STAMP(1);
    u32x4 qf[3];
#pragma unroll
    for (int i = 0; i < 3; ++i) qf[i] = *(LAS const u32x4*)(lds + L_Q + (c + 8 * i) * 4);
    float m = __uint_as_float(0xff7f0000u), l = 0.f, acc[24];
#pragma unroll
    for (int k = 0; k < 24; ++k) acc[k] = 0.f;
    unsigned pace = 0;
    bool stamp3 = false;
    auto step_fn = [&](u32x4 (&Kc)[3], u32x4 (&Vc)[3], bool refill, unsigned nbytes, unsigned av, unsigned as) {
      float a0 = 0.f, a1 = 0.f, a2 = 0.f;
#pragma unroll
      for (int k = 0; k < 4; ++k) { a0 = dot2(Kc[0][k], qf[0][k], a0); a1 = dot2(Kc[1][k], qf[1][k], a1); a2 = dot2(Kc[2][k], qf[2][k], a2); }
      float a = (a0 + a1) + a2;
      asm volatile("" : "+v"(a));
      PIN();
      if (refill) {
        if (pace) { unsigned tn; while ((int)(((tn = now()) << 6) - pace) < 0) { }
                    if (stamp3) { if (lane == 0) lds[L_CNT + 8] = tn | 1u; stamp3 = false; } }
        const rsrc_t k2 = mk(kc, nbytes);
#pragma unroll
        for (int i = 0; i < 3; ++i) Kc[i] = ld_nt(k2, av + 128 * i, as);
      }
      PIN();
      f32x2 vf[12];
#pragma unroll
      for (int k = 0; k < 12; ++k) vf[k] = up2(Vc[k >> 2][k & 3]);
      if (refill) {
        asm volatile("" : "+v"(vf[0]), "+v"(vf[1]), "+v"(vf[2]), "+v"(vf[3]), "+v"(vf[4]), "+v"(vf[5]));
        asm volatile("" : "+v"(vf[6]), "+v"(vf[7]), "+v"(vf[8]), "+v"(vf[9]), "+v"(vf[10]), "+v"(vf[11]));
        PIN();
        const rsrc_t v2 = mk(vc, nbytes);
#pragma unroll
        for (int i = 0; i < 3; ++i) Vc[i] = ld_nt(v2, av + 128 * i, as);
        PIN();
      }
      a += dpp<XOR1>(a); a += dpp<XOR2>(a); a += dpp<HMIR>(a);
      const float sc = rbf(rbf(a) * SCALE);
      const float mn = fmaxf(m, sc);
      const float alpha = __builtin_amdgcn_exp2f((m - mn) * L2E), p = __builtin_amdgcn_exp2f((sc - mn) * L2E);
      l = fmaf(l, alpha, p); m = mn;
#pragma unroll
      for (int k = 0; k < 12; ++k) { acc[2 * k] = fmaf(p, vf[k][0], acc[2 * k] * alpha); acc[2 * k + 1] = fmaf(p, vf[k][1], acc[2 * k + 1] * alpha); }
      PIN();
    };
#pragma unroll
    for (int p = 0; p < NPRE; ++p) step_fn(K[p], V[p], p == 0, KVB, tv(NPRE), ts(NPRE));
    int t = NPRE + 1;
    do {
      if (w == 1 && t == CKG && lane == 0) lds[L_CNT + 6] = 1;
      if (w == 0 && (t == CK1 || t == CK2)) { const unsigned tn = now() | 1u; if (lane == 0) lds[L_CNT + (t == CK1 ? 1 : 2)] = tn; }
      step_fn(K[0], V[0], true, KVB, tv(t), ts(t));
    } while (++t < SW);
    static_assert(NPRE < CK1 && CK1 < CK2 && CK2 < SW && NPRE < CKG && CKG < SW && SW + 2 <= CK3 && CK3 < 62 && (CK3 - SW) % 2 == 0 && (NT - SW) % 2 == 0, "every stamp the helpers wait for is reached");
    unsigned tend;
    while ((tend = lds_peek(lds + L_CNT + 4)) == 0) __builtin_amdgcn_s_sleep(1);
    const int stag = (STAG * ((((bid * STK) & 63) + 64 * w) - 128) * 64) >> 8;
    unsigned tgt = now() << 6;
    int tau = (int)((tend << 6) + stag - tgt) / (NT - 1 - SW);
#pragma unroll
    for (int i = 0; i < 3; ++i) K[1][i] = ld_nt(kr, tv(t) + 128 * i, ts(t));
    PIN();
#pragma unroll
    for (int i = 0; i < 3; ++i) V[1][i] = ld_nt(vr, tv(t) + 128 * i, ts(t));
    PIN();
    do {
      const unsigned t3 = lds_peek(lds + L_CNT + 4);
      if (t3 != tend) { tend = t3; tau = (int)((tend << 6) + stag - tgt) / max(NT - 1 - t, 1); }
      tgt += tau;
      pace = tgt | 1u;
      step_fn(K[0], V[0], true, KVB, tv(t + 1), ts(t + 1));
      tgt += tau;
      pace = t + 2 < NT ? tgt | 1u : 0u;
      if (w == 0 && t + 2 == CK3) stamp3 = true;
      step_fn(K[1], V[1], true, t + 2 < NT ? KVB : 0u, tv(t + 2), ts(t + 2));
      t += 2;
    } while (t < NT);
    step_fn(K[0], V[0], false, 0, 0, 0);
    float M = fmaxf(m, dpp<ROR8>(m));
    { const auto q16 = __builtin_amdgcn_permlane16_swap(__float_as_uint(M), __float_as_uint(M), false, false);
      M = fmaxf(__uint_as_float(q16[0]), __uint_as_float(q16[1]));
      const auto q32 = __builtin_amdgcn_permlane32_swap(__float_as_uint(M), __float_as_uint(M), false, false);
      M = fmaxf(__uint_as_float(q32[0]), __uint_as_float(q32[1])); }
    const float f = __builtin_amdgcn_exp2f((m - M) * L2E);
    l *= f; l += dpp<ROR8>(l); l = sw16(l, l); l = sw32(l, l);
#pragma unroll
    for (int k = 0; k < 24; ++k) acc[k] *= f;
#pragma unroll
    for (int i = 0; i < 3; ++i) ldf[L_REC + w * 196 + i * 64 + c * 8 + r] = scatter8(acc + i * 8, lane & 8);
    if (lane == 0) { ldf[L_REC + w * 196 + 192] = M; ldf[L_REC + w * 196 + 193] = l; }
  } else {
    const int hid = tid - 256, hw = w - 4;
    if (hid < 12) lds[L_CNT + hid] = 0;
    const int cq = (j - 2) >> 1, rg = hid >> 3, c8 = hid & 7;
    u32x4 W[20], X[5];
    const rsrc_t xr = mk(x, has_q ? 4 * D * 2 : 0), wqr = mk(wq, has_q ? WB : 0);
#pragma unroll
    for (int i = 0; i < 5; ++i) { const int ch = 20 * hw + (lane & 15) + 16 * i; X[i] = ld_nt(xr, (lane >> 4) * (D * 2) + (ch >= 80 ? ch - 80 : ch) * 16, 0); }
    PIN();
#pragma unroll
    for (int i = 0; i < 20; ++i) {
      W[i] = ld_nt(wqr, (unsigned)(lane >> 3) * (HD * 2) + c8 * 16, (unsigned)(160 * hw + 8 * i) * (HD * 2) + (h * DH + cq * 64) * 2);
      if ((i & 3) == 3) PIN();
    }
    const unsigned mo = hid < 192 ? hid * 16 : OOB;
    const u32x4 Ma = ld_nt(mk(w1, F * 2), mo, 0), Mb = ld_nt(mk(b1, F * 2), mo, 0), Mc = ld_nt(mk(w2, F * 2), mo, 0);
    const unsigned b2v = __builtin_amdgcn_raw_buffer_load_b16(mk(b2, 2), 0, 0, 2);
    PIN();
    WG_BAR();
    asm volatile("" : "+v"(ep));
    const unsigned long long E1 = ((unsigned long long)__builtin_amdgcn_readfirstlane(ep[1]) << 32 | (unsigned)__builtin_amdgcn_readfirstlane(ep[0])) + 1;
    const Tag tg = make_tag(E1);
    if (has_q) {
      float sa = 0.f, sb = 0.f;
#pragma unroll
      for (int i = 0; i < 20; i += 2) { sa = dot2(X[i >> 2][i & 3], 0x3f803f80u, sa); sb = dot2(X[i >> 2][(i & 3) + 1], 0x3f803f80u, sb); }
      const float mean = sum16(sa + sb) * (1.f / D);
      f32x2 s2 = {0.f, 0.f}, s3 = s2;
#pragma unroll
      for (int i = 0; i < 20; i += 2) {
        const f32x2 d = up2(X[i >> 2][i & 3]) - mean, e = up2(X[i >> 2][(i & 3) + 1]) - mean;
        s2 = __builtin_elementwise_fma(d, d, s2); s3 = __builtin_elementwise_fma(e, e, s3);
      }
      s2 += s3;
      const float inv = inv_sd(sum16(s2[0] + s2[1]) * (1.f / D)), mb = rbf(mean);
      LAS unsigned* xn = lds + L_XN + hw * 4 * 132;
#pragma unroll
      for (int i = 0; i < 2; ++i) {
        u32x4 o;
#pragma unroll
        for (int k = 0; k < 4; ++k) { const f32x2 y = rbf2(up2(X[i][k]) - mb) * inv; o[k] = pk2(y[0], y[1]); }
        *(LAS u32x4*)(xn + (lane >> 4) * 132 + ((lane & 15) + 16 * i) * 4) = o;
      }
      asm volatile("" ::: "memory");
      u32x4 af[5];
#pragma unroll
      for (int ks = 0; ks < 5; ++ks) af[ks] = *(LAS const u32x4*)(xn + (lane & 3) * 132 + ks * 16 + (lane >> 4) * 4);
      LAS unsigned* st = lds + L_WO + hw * 64 * 36;
      f32x4 dq[4];
#pragma unroll
      for (int nt = 0; nt < 4; ++nt) dq[nt] = f32x4{0.f, 0.f, 0.f, 0.f};
#pragma unroll
      for (int ks = 0; ks < 5; ++ks) {
        LAS unsigned* sk = st + (ks & 1) * 32 * 36;
#pragma unroll
        for (int i = 0; i < 4; ++i) *(LAS u32x4*)(sk + (8 * i + (lane >> 3)) * 36 + c8 * 4) = W[4 * ks + i];
        asm volatile("" ::: "memory");
        LAS const unsigned* rd = sk + (8 * (lane >> 4) + ((lane >> 2) & 3)) * 36 + (lane & 3) * 2;
#pragma unroll
        for (int nt = 0; nt < 4; ++nt) dq[nt] = __builtin_amdgcn_mfma_f32_16x16x32_bf16(__builtin_bit_cast(bf16x8, af[ks]), tr8(rd + nt * 8, 4 * 36), dq[nt], 0, 0, 0);
        asm volatile("" ::: "memory");
      }
      const bool g1 = lane & 16, g2 = lane & 32;
#pragma unroll
      for (int nt = 0; nt < 4; ++nt) {
        const float a01 = g1 ? dq[nt][1] : dq[nt][0], a23 = g1 ? dq[nt][3] : dq[nt][2];
        ldf[L_QR + hw * 256 + (lane >> 4) * 64 + nt * 16 + (lane & 15)] = g2 ? a23 : a01;
      }
      helper_bar(lds + L_CNT, 4, lane);
      if (hw == 0) {
        f32x4 a = *(LAS const f32x4*)(ldf + L_QR + 4 * lane);
#pragma unroll
        for (int ww = 1; ww < 4; ++ww) a += *(LAS const f32x4*)(ldf + L_QR + ww * 256 + 4 * lane);
        st_sc(wsr0, WS_QP + ((h * 4 + (lane >> 4)) * 96 + cq * 32 + (lane & 15) * 2) * 8, u32x4{pk2(a[0], a[1]), tg.t32, pk2(a[2], a[3]), tg.t32});
      }
    }
    STAMP(3);
    if (hw == 3) {
      if (QDELAY) while (now() - tstart < QDELAY) __builtin_amdgcn_s_sleep(2);
      const unsigned o = lane < 48 ? WS_QP + (h * 4 + b) * 96 * 8 + lane * 16 : OOB, vm = lane < 48 ? ~0u : 0u;
      u32x4 g;
#pragma nounroll
      for (unsigned spin = 0; spin < SPIN_CAP; ++spin) {
        asm volatile("" ::: "memory");
        g = ld_sc(wsr0, o);
        if (__all((((g[1] ^ tg.t32) | (g[3] ^ tg.t32)) & vm) == 0)) break;
        __builtin_amdgcn_s_sleep(QSLP);
      }
      if (lane < 48) *(LAS u32x2*)(lds + L_Q + 2 * lane) = u32x2{g[0], g[2]};
      asm volatile("s_waitcnt lgkmcnt(0)" ::: "memory");
      if (lane == 0) lds[L_CNT + 7] = 1;
      STAMP(1);
    } else {
      *(LAS u32x4*)(lds + L_MIX + hid * 4) = Ma; *(LAS u32x4*)(lds + L_MIX + 768 + hid * 4) = Mb; *(LAS u32x4*)(lds + L_MIX + 1536 + hid * 4) = Mc;
      const unsigned bits0 = (TAB0 + ((bid * 32) & 4095)) | (bid >> 7) << 15;
#pragma nounroll
      for (int i0 = 0; i0 < 32; i0 += 8) {
        float ac[8];
#pragma unroll
        for (int i = 0; i < 8; ++i) {
          const float t = __uint_as_float((bits0 + i0 + i) << 16);
          ac[i] = 0.f;
#pragma unroll
          for (int k = 0; k < 4; ++k) ac[i] = mix2(t, up2(Ma[k]), up2(Mb[k]), Mc[k], ac[i]);
        }
        float y = scatter8(ac, lane & 8);
        y += dpp<XOR1>(y); y += dpp<XOR2>(y); y += dpp<HMIR>(y);
        if ((lane & 7) == 0) ldf[L_RED + hw * 32 + i0 + (lane >> 3)] = y;
      }
      helper_bar(lds + L_CNT + 3, 3, lane);
      if (hw == 2 && lane < 8) {
        f32x4 y = *(LAS const f32x4*)(ldf + L_RED + 4 * lane);
#pragma unroll
        for (int ww = 1; ww < 3; ++ww) y += *(LAS const f32x4*)(ldf + L_RED + ww * 32 + 4 * lane);
        const f32x2 y0 = rbf2(f32x2{y[0], y[1]}) + bflo(b2v), y1 = rbf2(f32x2{y[2], y[3]}) + bflo(b2v);
        st_sc(wsr0, WS_TB + bid * 128 + lane * 16, u32x4{pk2(y0[0], y0[1]), tg.t32, pk2(y1[0], y1[1]), tg.t32});
      }
    }
    const rsrc_t wor = mk(wo, has_o ? WB : 0);
    u32x4 Wo[12];
#pragma unroll
    for (int i = 0; i < 12; ++i) Wo[i] = ld_nt(wor, (unsigned)rg * (D * 2) + c8 * 16, (unsigned)(hp * 2 * DH + 32 * i) * (D * 2) + co * 128);
    if (hid < 80) *(LAS u32x4*)(lds + L_X + hid * 4) = __builtin_nontemporal_load((const u32x4*)(x + b2i * D + hid * 8));
    if (hid == 80) lds[L_X + 320] = b2v;
#pragma unroll
    for (int i = 0; i < 12; ++i) *(LAS u32x4*)(lds + L_WO + (rg + 32 * i) * 36 + c8 * 4) = Wo[i];
    helper_bar(lds + L_CNT, has_q ? 8 : 4, lane);
    LAS const unsigned* wl = lds + L_WO + (8 * (lane >> 4) + ((lane >> 2) & 3)) * 36 + hw * 8 + (lane & 3) * 2;
#pragma unroll
    for (int ks = 0; ks < 12; ++ks) Bf[ks] = tr8(wl + ks * 32 * 36, 4 * 36);
    if (hw < 2) {
      while (lds_peek(lds + L_CNT + 6) == 0) __builtin_amdgcn_s_sleep(16);
#pragma nounroll
      for (int half = 0; half < 2; ++half) {
        u32x4 g[8];
#pragma nounroll
        for (unsigned spin = 0; spin < SPIN_CAP; ++spin) {
          asm volatile("" ::: "memory");
          unsigned bad = 0;
#pragma unroll
          for (int i = 0; i < 8; ++i) g[i] = ld_sc(wsr0, WS_TB + hid * 16, (half * 8 + i) * 2048);
#pragma unroll
          for (int i = 0; i < 8; ++i) bad |= (g[i][1] ^ tg.t32) | (g[i][3] ^ tg.t32);
          if (__all(bad == 0)) break;
          __builtin_amdgcn_s_sleep(32);
        }
#pragma unroll
        for (int i = 0; i < 8; ++i) *(LAS u32x2*)(lds + L_TAB + ((half * 8 + i) * 128 + hid) * 2) = u32x2{g[i][0], g[i][2]};
      }
    }
    if (hw == 2) {
      unsigned tm0 = 0;
#pragma nounroll
      for (int ck = 0; ck < 3; ++ck) {
        unsigned t2;
        while ((t2 = lds_peek(lds + L_CNT + (ck < 2 ? 1 + ck : 8))) == 0) __builtin_amdgcn_s_sleep(8);
        if ((bid & 3) == ((h >> 1) & 3) && lane == 0) __builtin_amdgcn_raw_buffer_store_b64(u32x2{t2, tg.t32}, wsr0, WS_TS + (ck * 64 + (bid >> 2)) * 128, 0, 16);
        while (now() - t2 < WAITT) __builtin_amdgcn_s_sleep(8);
        const u32x2 g = __builtin_amdgcn_raw_buffer_load_b64(wsr0, WS_TS + (ck * 64 + lane) * 128, 0, 16);
        const unsigned tm = t2 + (int)(sum64(g[1] == tg.t32 ? (float)min(max((int)(g[0] - t2), -2000), 2000) : (float)(WAITT + 30)) * (1.f / 64));
        const float left = ck == 1 ? (63.f - CK2) / (CK2 - CK1) * (1.f - EPS) : (63.f - CK3) / (CK3 - CK2) * (1.f - EPS3);
        if (ck && lane == 0) lds[L_CNT + 4] = (tm + (int)((float)(int)(tm - tm0) * left)) | 1u;
        tm0 = tm;
      }
    }
  }
  const unsigned long long E1 = ((unsigned long long)__builtin_amdgcn_readfirstlane(ep[1]) << 32 | (unsigned)__builtin_amdgcn_readfirstlane(ep[0])) + 1;
  const Tag tg = make_tag(E1);
  unsigned a_p1 = lane <= 48 ? WS_P1 + (h * 8 + j) * P1REC + lane * 16 : OOB;
  unsigned a_p2 = (lane & 12) ? OOB : WS_P2 + (((lane & 3) * 16 + hp) * D + co * 64 + (w - 4) * 16 + (lane >> 4) * 4) * 4;
  LAS const float* a_rec = ldf + L_REC + 4 * (lane < 48 ? lane : 48);
  LAS const unsigned* a_o = lds + L_O + (lane & 3) * OSTR + (lane >> 4) * 4;
  LAS const unsigned* own = lds + L_HH + cg * 5 + (lane < 5 ? lane : 0);
  unsigned a_out = lane < 5 ? (b2i * D + cg * 10 + 2 * lane) * 2 : OOB;
  asm volatile("" : "+v"(a_p1), "+v"(a_p2), "+v"(a_rec), "+v"(a_o), "+v"(own), "+v"(a_out));
  const rsrc_t wsr = wsr0;
  WG_BAR();
  STAMP(2);
  if (w == 4) {
    float mm[4];
    f32x4 rv[4];
#pragma unroll
    for (int i = 0; i < 4; ++i) { mm[i] = ldf[L_REC + i * 196 + 192]; rv[i] = *(LAS const f32x4*)(a_rec + i * 196); }
    __builtin_amdgcn_sched_barrier(0);
    const float M = fmaxf(fmaxf(mm[0], mm[1]), fmaxf(mm[2], mm[3]));
    f32x4 a = {0.f, 0.f, 0.f, 0.f};
#pragma unroll
    for (int i = 0; i < 4; ++i) a += __builtin_amdgcn_exp2f((mm[i] - M) * L2E) * rv[i];
    if (lane >= 48) a = f32x4{M, a[1], 0.f, 0.f};
    st_sc(wsr, a_p1, enc4(a, tg));
    STAMP(1);
  }
  const unsigned tcom = lds_peek(lds + L_CNT + 4);
  if (has_o) {
    {
      const unsigned oa = lane <= 48 ? WS_P1 + (hp * 16 + (w >> 2) * 8 + 2 * (w & 3)) * P1REC + lane * 16 : OOB;
      const unsigned vm = lane <= 48 ? ~0u : 0u;
      LAS unsigned* da = lane < 48 ? lds + L_O + (w & 3) * OSTR + (w >> 2) * 96 + 2 * lane : lds + L_QR + 2 * lane;
      asm volatile("" : "+v"(da));
      u32x4 ga, gb;
      if (C1) while ((int)(now() - tcom) < C1) { }
#pragma nounroll
      for (unsigned spin = 0; spin < SPIN_CAP; ++spin) {
        asm volatile("" ::: "memory");
        ga = ld_sc(wsr, oa); gb = ld_sc(wsr, oa, P1REC);
        if (__all(((bad4(ga, tg) | bad4(gb, tg)) & vm) == 0)) break;
        __builtin_amdgcn_s_sleep(1);
      }
      STAMP(4);
      const float m0 = dec(__builtin_amdgcn_readlane(ga[0], 48)), m1 = dec(__builtin_amdgcn_readlane(gb[0], 48)), M = fmaxf(m0, m1);
      const float l0 = dec(__builtin_amdgcn_readlane(ga[1], 48)), l1 = dec(__builtin_amdgcn_readlane(gb[1], 48));
      float w0 = __builtin_amdgcn_exp2f((m0 - M) * L2E), w1_ = __builtin_amdgcn_exp2f((m1 - M) * L2E);
      const float inv = __builtin_amdgcn_rcpf(fmaf(w0, l0, w1_ * l1));
      w0 *= inv; w1_ *= inv;
      const f32x4 o = __builtin_elementwise_fma(dec4(ga), (f32x4)w0, dec4(gb) * w1_);
      *(LAS u32x2*)da = u32x2{pk2(o[0], o[1]), pk2(o[2], o[3])};
    }
    WG_BAR();
    if (w >= 4) {
      u32x4 af[12];
#pragma unroll
      for (int ks = 0; ks < 12; ++ks) af[ks] = *(LAS const u32x4*)(a_o + ks * 16);
      __builtin_amdgcn_sched_barrier(0);
      f32x4 d0 = {0.f, 0.f, 0.f, 0.f}, d1 = d0;
#pragma unroll
      for (int ks = 0; ks < 12; ks += 2) {
        d0 = __builtin_amdgcn_mfma_f32_16x16x32_bf16(Bf[ks], __builtin_bit_cast(bf16x8, af[ks]), d0, 0, 0, 0);
        d1 = __builtin_amdgcn_mfma_f32_16x16x32_bf16(Bf[ks + 1], __builtin_bit_cast(bf16x8, af[ks + 1]), d1, 0, 0, 0);
      }
      st_sc(wsr, a_p2, enc4(d0 + d1, tg));
      if (w != 6) STAMP(3);
    }
  }
  if (w < 5) {
    const unsigned o = WS_P2 + (b2i * 16 + (lane >> 5) * 8) * D * 4 + (w * 32 + (lane & 31)) * 16;
    const int pr = (w * 32 + (lane & 31)) * 2 + (lane >> 5);
    f32x2 xr = up2(lds[L_X + pr]);
    LAS unsigned* dh = lds + L_HH + pr;
    asm volatile("" : "+v"(xr), "+v"(dh));
    u32x4 g[8];
    if (C2) while ((int)(now() - tcom) < C2) { }
#pragma nounroll
    for (unsigned spin = 0; spin < SPIN_CAP; ++spin) {
      asm volatile("" ::: "memory");
      unsigned bad = 0;
#pragma unroll
      for (int i = 0; i < 8; ++i) g[i] = ld_sc(wsr, o, i * D * 4);
#pragma unroll
      for (int i = 0; i < 8; ++i) bad |= bad4(g[i], tg);
      if (__all(bad == 0)) break;
      __builtin_amdgcn_s_sleep(1);
    }
    STAMP(5);
    const f32x4 y = ((dec4(g[0]) + dec4(g[1])) + (dec4(g[2]) + dec4(g[3]))) + ((dec4(g[4]) + dec4(g[5])) + (dec4(g[6]) + dec4(g[7])));
    const f32x2 hs = xr + rbf2(f32x2{sw32(y[0], y[2]), sw32(y[1], y[3])});
    const unsigned hp2 = pk2(hs[0], hs[1]);
    *dh = hp2;
    const f32x2 hv = up2(hp2);
    const float piv = __uint_as_float(__builtin_amdgcn_readfirstlane(__float_as_uint(hv[0])));
    const f32x2 d = hv - piv, dd = d * d;
    const float sp = sw32(d[0] + d[1], dd[0] + dd[1]), sq = sum16(sw16(sp, sp));
    const float s1 = __uint_as_float(__builtin_amdgcn_readlane(__float_as_uint(sq), 0)), s2 = __uint_as_float(__builtin_amdgcn_readlane(__float_as_uint(sq), 32));
    *(LAS f32x2*)(ldf + L_ST + 2 * w) = f32x2{fmaf(s1, 1.f / 128, piv), fmaf(-s1, s1 * (1.f / 128), s2)};
  }
  WG_BAR();
  if (w == 0) {
    const f32x4 sa = *(LAS const f32x4*)(ldf + L_ST), sb = *(LAS const f32x4*)(ldf + L_ST + 4);
    const f32x2 sc = *(LAS const f32x2*)(ldf + L_ST + 8);
    const unsigned hw2 = *own;
    __builtin_amdgcn_sched_barrier(0);
    const f32x2 hv = up2(hw2);
    const float mean = (((sa[0] + sa[2]) + (sb[0] + sb[2])) + sc[0]) * 0.2f;
    const float e0 = sa[0] - mean, e1 = sa[2] - mean, e2 = sb[0] - mean, e3 = sb[2] - mean, e4 = sc[0] - mean;
    const float var = fmaf(fmaf(e0, e0, e1 * e1) + fmaf(e2, e2, e3 * e3) + e4 * e4, 0.2f, (((sa[1] + sa[3]) + (sb[1] + sb[3])) + sc[1]) * (1.f / D));
    const float inv = inv_sd(var), mb = rbf(mean);
    const float inv2 = inv_sd(var * inv * inv), mb2 = rbf((mean - mb) * inv);
    const f32x2 tv = rbf2(rbf2(rbf2(rbf2(hv - mb) * inv) - mb2) * inv2);
    STAMP(6);
    const unsigned tb = pk2(tv[0], tv[1]), i0 = (tb & 0x7fffu) - TAB0, i1 = ((tb >> 16) & 0x7fffu) - TAB0;
    const rsrc_t outr = mk(out, 4 * D * 2);
    LAS const unsigned short* tab = (LAS const unsigned short*)(lds + L_TAB);
    f32x2 y = {bflo(tab[(i0 & 4095u) + ((tb >> 3) & 4096u)]), bflo(tab[(i1 & 4095u) + ((tb >> 19) & 4096u)])};
    if (!__all((i0 | i1) < 4096u) || FORCE_FB) {
      const float bias = bflo(lds[L_X + 320]);
#pragma nounroll
      for (int i = 0; i < 10; ++i) {
        const float t = __shfl(i & 1 ? tv[1] : tv[0], i >> 1);
        float a = 0.f;
#pragma nounroll
        for (int k = 0; k < 12; ++k) a = mix2(t, up2(lds[L_MIX + lane + 64 * k]), up2(lds[L_MIX + 768 + lane + 64 * k]), lds[L_MIX + 1536 + lane + 64 * k], a);
        a = rbf(rbf(sum64(a)) + bias);
        if (lane == (i >> 1)) { if (i & 1) y[1] = a; else y[0] = a; }
      }
    }
    const f32x2 o = hv + y;
    __builtin_amdgcn_raw_buffer_store_b32(pk2(o[0], o[1]), outr, a_out, 0, 16);
  }
  if (bid == 0 && tid == 64) __builtin_amdgcn_raw_buffer_store_b64(u32x2{(unsigned)E1, (unsigned)(E1 >> 32)}, wsr, 0, 0, 16);
}

extern "C" void kernel_launch(void* const* d_in, const int* in_sizes, int n_in, void* d_out, int out_size, void* d_ws, size_t ws_size,
                              hipStream_t stream) {
  typedef const unsigned short* P;
  fused_decode<<<256, 512, 0, stream>>>((P)d_in[0], (P)d_in[1], (P)d_in[2], (P)d_in[3], (P)d_in[4], (P)d_in[6], (P)d_in[7], (P)d_in[8], (P)d_in[9],
                                        (unsigned short*)d_out, (unsigned char*)d_ws);
}
```

```cpp
#include <hip/hip_runtime.h>
#include <stdint.h>

typedef unsigned u32x4 __attribute__((ext_vector_type(4)));
typedef unsigned u32x2 __attribute__((ext_vector_type(2)));
typedef float f32x4 __attribute__((ext_vector_type(4)));
typedef float f32x2 __attribute__((ext_vector_type(2)));
typedef __bf16 bf16x2 __attribute__((ext_vector_type(2)));
typedef __bf16 bf16x8 __attribute__((ext_vector_type(8)));
typedef __amdgpu_buffer_rsrc_t rsrc_t;
#define LAS __attribute__((address_space(3)))

#define NPRE 6
#define QDELAY 600
#define PAIR 2
#define WAITT 400
#define CK1 14
#define CK2 26
#define SW 32
#define EPS 0.0f
#define C1 215
#define C2 415
#define CKG 28
#define FORCE_FB 0
#define STAG 85
#define STK 37
#define CK3 44
#define QSLP 8
#define EPS3 -0.02f
#define CODEPF 2
#define ROTX 0
#define STR2(x) #x
#define STR(x) STR2(x)

constexpr int D = 640, H = 32, SK = 4096, DH = 192, HD = H * DH, F = 1536;
constexpr unsigned SEQB = SK * DH * 2, TILEB = 8 * DH * 2, KVB = 4u * H * SEQB, WB = D * HD * 2;
constexpr unsigned P1REC = 896;
constexpr unsigned WS_TS = 4096, WS_TB = WS_TS + 4 * 64 * 128, WS_QP = WS_TB + 256 * 128, WS_P1 = WS_QP + H * 4 * 96 * 8, WS_P2 = WS_P1 + 256 * P1REC, WS_END = WS_P2 + 4 * 16 * D * 4;
constexpr unsigned OOB = 0x7ffffff0u;
constexpr unsigned SPIN_CAP = 1u << 17;
constexpr unsigned TAB0 = 100u << 7;
constexpr float L2E = 1.4426950408889634f, SCALE = 0.07216878364870322f;
constexpr int OSTR = 208;
constexpr int L_CNT = 0, L_Q = 12, L_REC = L_Q + 96, L_O = L_REC + 4 * 196, L_RED = L_O + 4 * OSTR, L_ST = L_RED + 8 * 16, L_X = L_ST + 16, L_HH = L_X + 324,
              L_XN = L_HH + 320, L_QR = L_XN + 16 * 132, L_MIX = L_QR + 4 * 256, L_WO = L_MIX + 3 * F / 2, L_Y = L_WO, L_TAB = L_WO + 2 * DH * 36, L_END = L_TAB + 4096;
constexpr int LDS_DW = 26624;
static_assert(L_END <= LDS_DW && L_REC % 4 == 0 && L_O % 4 == 0 && L_XN % 4 == 0 && L_QR % 4 == 0 && L_MIX % 4 == 0 && L_WO % 4 == 0, "lds map");

__device__ __forceinline__ float bflo(unsigned w) { return __uint_as_float(w << 16); }
__device__ __forceinline__ float bfhi(unsigned w) { return __uint_as_float(w & 0xffff0000u); }
__device__ __forceinline__ unsigned pk2(float lo, float hi) { unsigned r; asm("v_cvt_pk_bf16_f32 %0, %1, %2" : "=v"(r) : "v"(lo), "v"(hi)); return r; }
__device__ __forceinline__ float rbf(float f) { return (float)(__bf16)f; }
__device__ __forceinline__ f32x2 up2(unsigned w) { return f32x2{bflo(w), bfhi(w)}; }
__device__ __forceinline__ f32x2 rbf2(f32x2 v) { return up2(pk2(v[0], v[1])); }
__device__ __forceinline__ float dot2(unsigned a, unsigned b, float c) {
  return __builtin_amdgcn_fdot2_f32_bf16(__builtin_bit_cast(bf16x2, a), __builtin_bit_cast(bf16x2, b), c, false);
}
template <int CTRL> __device__ __forceinline__ float dpp(float x) {
  return __builtin_bit_cast(float, __builtin_amdgcn_mov_dpp(__builtin_bit_cast(int, x), CTRL, 0xf, 0xf, true));
}
constexpr int XOR1 = 0xB1, XOR2 = 0x4E, HMIR = 0x141, ROR8 = 0x128;
__device__ __forceinline__ float sw32(float a, float b) {
  const auto r = __builtin_amdgcn_permlane32_swap(__float_as_uint(a), __float_as_uint(b), false, false);
  return __uint_as_float(r[0]) + __uint_as_float(r[1]);
}
__device__ __forceinline__ float sw16(float a, float b) {
  const auto r = __builtin_amdgcn_permlane16_swap(__float_as_uint(a), __float_as_uint(b), false, false);
  return __uint_as_float(r[0]) + __uint_as_float(r[1]);
}
__device__ __forceinline__ float sum16(float v) { v += dpp<XOR1>(v); v += dpp<XOR2>(v); v += dpp<HMIR>(v); v += dpp<ROR8>(v); return v; }
__device__ __forceinline__ float sum64(float v) { v = sum16(v); v = sw16(v, v); return sw32(v, v); }
__device__ __forceinline__ float scatter8(const float* a, bool b3) {
  float y[4], z[2];
#pragma unroll
  for (int e = 0; e < 4; ++e) y[e] = sw32(a[e], a[e + 4]);
#pragma unroll
  for (int e = 0; e < 2; ++e) z[e] = sw16(y[e], y[e + 2]);
  return (b3 ? z[1] : z[0]) + dpp<ROR8>(b3 ? z[0] : z[1]);
}
typedef short s16x4 __attribute__((ext_vector_type(4)));
__device__ __forceinline__ bf16x8 tr8(LAS const unsigned* p, int step) {
  const s16x4 a = __builtin_amdgcn_ds_read_tr16_b64_v4i16((LAS s16x4*)p), b = __builtin_amdgcn_ds_read_tr16_b64_v4i16((LAS s16x4*)(p + step));
  return __builtin_bit_cast(bf16x8, __builtin_shufflevector(a, b, 0, 1, 2, 3, 4, 5, 6, 7));
}
__device__ __forceinline__ rsrc_t mk(const void* p, unsigned n) { return __builtin_amdgcn_make_buffer_rsrc((void*)p, 0, n, 0x00020000); }
__device__ __forceinline__ u32x4 ld_nt(rsrc_t r, unsigned v, unsigned s) { return __builtin_amdgcn_raw_buffer_load_b128(r, v, s, 2); }
__device__ __forceinline__ u32x4 ld_sc(rsrc_t r, unsigned v, unsigned s = 0) { return __builtin_amdgcn_raw_buffer_load_b128(r, v, s, 16); }
__device__ __forceinline__ void st_sc(rsrc_t r, unsigned v, u32x4 x) { __builtin_amdgcn_raw_buffer_store_b128(x, r, v, 0, 16); }
__device__ __forceinline__ unsigned now() { return (unsigned)__builtin_amdgcn_s_memrealtime(); }
__device__ __forceinline__ unsigned lds_peek(LAS unsigned* p) {
  return __builtin_amdgcn_readfirstlane(__hip_atomic_load(p, __ATOMIC_RELAXED, __HIP_MEMORY_SCOPE_WORKGROUP));
}
#define PIN() do { asm volatile("" ::: "memory"); __builtin_amdgcn_sched_barrier(0); } while (0)
#define WG_BAR() do { asm volatile("s_waitcnt lgkmcnt(0)" ::: "memory"); __builtin_amdgcn_s_barrier(); PIN(); } while (0)
#define STAMP(i) do { } while (0)
struct Tag { unsigned b0, b1, b2, b3, t32; };
__device__ __forceinline__ Tag make_tag(unsigned long long e) {
  const unsigned t = (unsigned)e;
  return Tag{t & 0xffu, (t + (t >> 8)) & 0xffu, (t + (t >> 16)) & 0xffu, (t + (t >> 24)) & 0xffu, t};
}
__device__ __forceinline__ u32x4 enc4(f32x4 v, const Tag& t) {
  return u32x4{(__float_as_uint(v[0]) & 0xffffff00u) | t.b0, (__float_as_uint(v[1]) & 0xffffff00u) | t.b1, (__float_as_uint(v[2]) & 0xffffff00u) | t.b2,
               (__float_as_uint(v[3]) & 0xffffff00u) | t.b3};
}
__device__ __forceinline__ unsigned bad4(u32x4 g, const Tag& t) { return ((g[0] ^ t.b0) | (g[1] ^ t.b1) | (g[2] ^ t.b2) | (g[3] ^ t.b3)) & 0xffu; }
__device__ __forceinline__ f32x4 dec4(u32x4 g) { return __builtin_bit_cast(f32x4, g & 0xffffff00u); }
__device__ __forceinline__ float dec(unsigned g) { return __uint_as_float(g & 0xffffff00u); }
__device__ __forceinline__ void helper_bar(LAS unsigned* cnt, unsigned target, int lane) {
  asm volatile("s_waitcnt lgkmcnt(0)" ::: "memory");
  if (lane == 0) __hip_atomic_fetch_add(cnt, 1u, __ATOMIC_RELAXED, __HIP_MEMORY_SCOPE_WORKGROUP);
  while (lds_peek(cnt) < target) { }
  asm volatile("" ::: "memory");
}
__device__ __forceinline__ float mix2(float t, f32x2 wa, f32x2 wb, unsigned wc, float acc) {
  constexpr float K0 = -2.f * L2E * 0.7978845608028654f, K1 = K0 * 0.044715f;
  const f32x2 u = rbf2(rbf2(wa * t) + wb);
  const f32x2 z = u * __builtin_elementwise_fma(u * u, (f32x2)K1, (f32x2)K0);
  const f32x2 d = f32x2{__builtin_amdgcn_exp2f(z[0]), __builtin_amdgcn_exp2f(z[1])} + 1.f;
  const f32x2 g = u * f32x2{__builtin_amdgcn_rcpf(d[0]), __builtin_amdgcn_rcpf(d[1])};
  return dot2(pk2(g[0], g[1]), wc, acc);
}
__device__ __forceinline__ float inv_sd(float var) { return __builtin_amdgcn_rcpf(rbf(__builtin_amdgcn_sqrtf(rbf(rbf(var) + 1e-5f)))); }

__global__ void __launch_bounds__(512, 2)
fused_decode(const unsigned short* x, const unsigned short* kc, const unsigned short* vc, const unsigned short* wq, const unsigned short* wo,
             const unsigned short* w1, const unsigned short* b1, const unsigned short* w2, const unsigned short* b2, unsigned short* out,
             unsigned char* ws) {
  __shared__ __attribute__((aligned(16))) unsigned lds_raw[LDS_DW];
  LAS unsigned* lds = (LAS unsigned*)lds_raw;
  LAS float* ldf = (LAS float*)lds_raw;
  const int tid = threadIdx.x, lane = tid & 63, w = __builtin_amdgcn_readfirstlane(tid >> 6);
  const int bid = blockIdx.x, h = bid >> 3, j = (bid + h) & 7, b = j >> 1, s = j & 1;
  const bool has_q = j >= 2 && s == 1, has_o = j < 2 || s == 0;
  const int hp = h >> 1, co = (h & 1) * 5 + (j < 2 ? j : b + 1);
  const int b2i = bid >> 6, cg = bid & 63;
  STAMP(0);
  const unsigned tstart = now();
  asm volatile("" :: "s"(x), "s"(kc), "s"(vc), "s"(wq), "s"(wo), "s"(w1), "s"(b1), "s"(w2), "s"(b2), "s"(out), "s"(ws));
  const rsrc_t wsr0 = mk(ws, WS_END);
  u32x2 ep = __builtin_amdgcn_raw_buffer_load_b64(wsr0, 0, 0, 16);
  bf16x8 Bf[12];

  if (w < 4) {
    __builtin_amdgcn_s_setprio(2);
    const int r = lane >> 3, c = lane & 7;
    constexpr int ntw = 256, NT = 64;
    const unsigned seq = (unsigned)(b * H + h) * SEQB;
    const int rot = __builtin_amdgcn_readfirstlane((ROTX) % ntw), y0 = s ? 512 - ntw : 0;
    const bool hi = r >> 2;
    const unsigned kb = PAIR == 1 ? (hi ? 171 : 0) : (hi ? 86 : 0), c1 = PAIR == 1 ? 171 : (hi ? 85 : 86), d1 = PAIR == 1 ? (hi ? 170 : 171) : 85,
                   c2 = PAIR == 1 ? (hi ? 342 : 341) : 171, d2 = PAIR == 1 ? 171 : (hi ? 85 : 86);
    constexpr int PB = PAIR == 1 ? 9 : 8;
    auto ty = [&](int i) -> int { const int y1 = i * 4 + w + rot; return y0 + (y1 >= ntw ? y1 - ntw : y1); };
    auto tv = [&](int i) -> unsigned {
      if (!PAIR) return (unsigned)(r * 384 + c * 16);
      const unsigned mm = ((4 * ty(i)) & ((1 << PB) - 1)) + (r & 3);
      return (mm + kb + (mm >= c1 ? d1 : 0) + (mm >= c2 ? d2 : 0)) * 384 + c * 16; };
    auto ts = [&](int i) -> unsigned {
      return __builtin_amdgcn_readfirstlane(PAIR ? seq + (unsigned)((4 * ty(i)) >> PB) * (768u << PB) : seq + ty(i) * TILEB); };
    const rsrc_t kr = mk(kc, KVB), vr = mk(vc, KVB);
    u32x4 K[NPRE][3], V[NPRE][3];
    WG_BAR();
#pragma unroll
    for (int p = 0; p < NPRE; ++p) {
      if (p) asm volatile("s_waitcnt vmcnt(3)" ::: "memory");
#pragma unroll
      for (int i = 0; i < 3; ++i) K[p][i] = ld_nt(kr, tv(p) + 128 * i, ts(p));
      PIN();
      if (p) asm volatile("s_waitcnt vmcnt(3)" ::: "memory");
#pragma unroll
      for (int i = 0; i < 3; ++i) V[p][i] = ld_nt(vr, tv(p) + 128 * i, ts(p));
      PIN();
    }
    while (lds_peek(lds + L_CNT + 7) == 0) __builtin_amdgcn_s_sleep(1);
    PIN();
    STAMP(1);
    u32x4 qf[3];
#pragma unroll
    for (int i = 0; i < 3; ++i) qf[i] = *(LAS const u32x4*)(lds + L_Q + (c + 8 * i) * 4);
    float m = __uint_as_float(0xff7f0000u), l = 0.f, acc[24];
#pragma unroll
    for (int k = 0; k < 24; ++k) acc[k] = 0.f;
    unsigned pace = 0;
    bool stamp3 = false;
    auto step_fn = [&](u32x4 (&Kc)[3], u32x4 (&Vc)[3], bool refill, unsigned nbytes, unsigned av, unsigned as) {
      float a0 = 0.f, a1 = 0.f, a2 = 0.f;
#pragma unroll
      for (int k = 0; k < 4; ++k) { a0 = dot2(Kc[0][k], qf[0][k], a0); a1 = dot2(Kc[1][k], qf[1][k], a1); a2 = dot2(Kc[2][k], qf[2][k], a2); }
      float a = (a0 + a1) + a2;
      asm volatile("" : "+v"(a));
      PIN();
      if (refill) {
        if (pace) { unsigned tn; while ((int)(((tn = now()) << 6) - pace) < 0) { }
                    if (stamp3) { if (lane == 0) lds[L_CNT + 8] = tn | 1u; stamp3 = false; } }
        const rsrc_t k2 = mk(kc, nbytes);
#pragma unroll
        for (int i = 0; i < 3; ++i) Kc[i] = ld_nt(k2, av + 128 * i, as);
      }
      PIN();
      f32x2 vf[12];
#pragma unroll
      for (int k = 0; k < 12; ++k) vf[k] = up2(Vc[k >> 2][k & 3]);
      if (refill) {
        asm volatile("" : "+v"(vf[0]), "+v"(vf[1]), "+v"(vf[2]), "+v"(vf[3]), "+v"(vf[4]), "+v"(vf[5]));
        asm volatile("" : "+v"(vf[6]), "+v"(vf[7]), "+v"(vf[8]), "+v"(vf[9]), "+v"(vf[10]), "+v"(vf[11]));
        PIN();
        const rsrc_t v2 = mk(vc, nbytes);
#pragma unroll
        for (int i = 0; i < 3; ++i) Vc[i] = ld_nt(v2, av + 128 * i, as);
        PIN();
      }
      a += dpp<XOR1>(a); a += dpp<XOR2>(a); a += dpp<HMIR>(a);
      const float sc = rbf(rbf(a) * SCALE);
      const float mn = fmaxf(m, sc);
      const float alpha = __builtin_amdgcn_exp2f((m - mn) * L2E), p = __builtin_amdgcn_exp2f((sc - mn) * L2E);
      l = fmaf(l, alpha, p); m = mn;
#pragma unroll
      for (int k = 0; k < 12; ++k) { acc[2 * k] = fmaf(p, vf[k][0], acc[2 * k] * alpha); acc[2 * k + 1] = fmaf(p, vf[k][1], acc[2 * k + 1] * alpha); }
      PIN();
    };
#pragma unroll
    for (int p = 0; p < NPRE; ++p) step_fn(K[p], V[p], p == 0, KVB, tv(NPRE), ts(NPRE));
    int t = NPRE + 1;
    do {
      if (w == 1 && t == CKG && lane == 0) lds[L_CNT + 6] = 1;
      if (w == 0 && (t == CK1 || t == CK2)) { const unsigned tn = now() | 1u; if (lane == 0) lds[L_CNT + (t == CK1 ? 1 : 2)] = tn; }
      step_fn(K[0], V[0], true, KVB, tv(t), ts(t));
    } while (++t < SW);
    static_assert(NPRE < CK1 && CK1 < CK2 && CK2 < SW && NPRE < CKG && CKG < SW && SW + 2 <= CK3 && CK3 < 62 && (CK3 - SW) % 2 == 0 && (NT - SW) % 2 == 0, "every stamp the helpers wait for is reached");
    unsigned tend;
    while ((tend = lds_peek(lds + L_CNT + 4)) == 0) __builtin_amdgcn_s_sleep(1);
    const int stag = (STAG * ((((bid * STK) & 63) + 64 * w) - 128) * 64) >> 8;
    unsigned tgt = now() << 6;
    int tau = (int)((tend << 6) + stag - tgt) / (NT - 1 - SW);
#pragma unroll
    for (int i = 0; i < 3; ++i) K[1][i] = ld_nt(kr, tv(t) + 128 * i, ts(t));
    PIN();
#pragma unroll
    for (int i = 0; i < 3; ++i) V[1][i] = ld_nt(vr, tv(t) + 128 * i, ts(t));
    PIN();
    do {
      const unsigned t3 = lds_peek(lds + L_CNT + 4);
      if (t3 != tend) { tend = t3; tau = (int)((tend << 6) + stag - tgt) / max(NT - 1 - t, 1); }
      tgt += tau;
      pace = tgt | 1u;
      step_fn(K[0], V[0], true, KVB, tv(t + 1), ts(t + 1));
      tgt += tau;
      pace = t + 2 < NT ? tgt | 1u : 0u;
      if (w == 0 && t + 2 == CK3) stamp3 = true;
      step_fn(K[1], V[1], true, t + 2 < NT ? KVB : 0u, tv(t + 2), ts(t + 2));
      t += 2;
    } while (t < NT);
    step_fn(K[0], V[0], false, 0, 0, 0);
    float M = fmaxf(m, dpp<ROR8>(m));
    { const auto q16 = __builtin_amdgcn_permlane16_swap(__float_as_uint(M), __float_as_uint(M), false, false);
      M = fmaxf(__uint_as_float(q16[0]), __uint_as_float(q16[1]));
      const auto q32 = __builtin_amdgcn_permlane32_swap(__float_as_uint(M), __float_as_uint(M), false, false);
      M = fmaxf(__uint_as_float(q32[0]), __uint_as_float(q32[1])); }
    const float f = __builtin_amdgcn_exp2f((m - M) * L2E);
    l *= f; l += dpp<ROR8>(l); l = sw16(l, l); l = sw32(l, l);
#pragma unroll
    for (int k = 0; k < 24; ++k) acc[k] *= f;
#pragma unroll
    for (int i = 0; i < 3; ++i) ldf[L_REC + w * 196 + i * 64 + c * 8 + r] = scatter8(acc + i * 8, lane & 8);
    if (lane == 0) { ldf[L_REC + w * 196 + 192] = M; ldf[L_REC + w * 196 + 193] = l; }
  } else {
    const int hid = tid - 256, hw = w - 4;
    if (hid < 12) lds[L_CNT + hid] = 0;
    const int cq = (j - 2) >> 1, rg = hid >> 3, c8 = hid & 7;
    u32x4 W[20], X[5];
    const rsrc_t xr = mk(x, has_q ? 4 * D * 2 : 0), wqr = mk(wq, has_q ? WB : 0);
#pragma unroll
    for (int i = 0; i < 5; ++i) { const int ch = 20 * hw + (lane & 15) + 16 * i; X[i] = ld_nt(xr, (lane >> 4) * (D * 2) + (ch >= 80 ? ch - 80 : ch) * 16, 0); }
    PIN();
#pragma unroll
    for (int i = 0; i < 20; ++i) {
      W[i] = ld_nt(wqr, (unsigned)(lane >> 3) * (HD * 2) + c8 * 16, (unsigned)(160 * hw + 8 * i) * (HD * 2) + (h * DH + cq * 64) * 2);
      if ((i & 3) == 3) PIN();
    }
    const unsigned mo = hid < 192 ? hid * 16 : OOB;
    const u32x4 Ma = ld_nt(mk(w1, F * 2), mo, 0), Mb = ld_nt(mk(b1, F * 2), mo, 0), Mc = ld_nt(mk(w2, F * 2), mo, 0);
    const unsigned b2v = __builtin_amdgcn_raw_buffer_load_b16(mk(b2, 2), 0, 0, 2);
    PIN();
    WG_BAR();
    asm volatile("" : "+v"(ep));
    const unsigned long long E1 = ((unsigned long long)__builtin_amdgcn_readfirstlane(ep[1]) << 32 | (unsigned)__builtin_amdgcn_readfirstlane(ep[0])) + 1;
    const Tag tg = make_tag(E1);
    if (has_q) {
      float sa = 0.f, sb = 0.f;
#pragma unroll
      for (int i = 0; i < 20; i += 2) { sa = dot2(X[i >> 2][i & 3], 0x3f803f80u, sa); sb = dot2(X[i >> 2][(i & 3) + 1], 0x3f803f80u, sb); }
      const float mean = sum16(sa + sb) * (1.f / D);
      f32x2 s2 = {0.f, 0.f}, s3 = s2;
#pragma unroll
      for (int i = 0; i < 20; i += 2) {
        const f32x2 d = up2(X[i >> 2][i & 3]) - mean, e = up2(X[i >> 2][(i & 3) + 1]) - mean;
        s2 = __builtin_elementwise_fma(d, d, s2); s3 = __builtin_elementwise_fma(e, e, s3);
      }
      s2 += s3;
      const float inv = inv_sd(sum16(s2[0] + s2[1]) * (1.f / D)), mb = rbf(mean);
      LAS unsigned* xn = lds + L_XN + hw * 4 * 132;
#pragma unroll
      for (int i = 0; i < 2; ++i) {
        u32x4 o;
#pragma unroll
        for (int k = 0; k < 4; ++k) { const f32x2 y = rbf2(up2(X[i][k]) - mb) * inv; o[k] = pk2(y[0], y[1]); }
        *(LAS u32x4*)(xn + (lane >> 4) * 132 + ((lane & 15) + 16 * i) * 4) = o;
      }
      asm volatile("" ::: "memory");
      u32x4 af[5];
#pragma unroll
      for (int ks = 0; ks < 5; ++ks) af[ks] = *(LAS const u32x4*)(xn + (lane & 3) * 132 + ks * 16 + (lane >> 4) * 4);
      LAS unsigned* st = lds + L_WO + hw * 64 * 36;
      f32x4 dq[4];
#pragma unroll
      for (int nt = 0; nt < 4; ++nt) dq[nt] = f32x4{0.f, 0.f, 0.f, 0.f};
#pragma unroll
      for (int ks = 0; ks < 5; ++ks) {
        LAS unsigned* sk = st + (ks & 1) * 32 * 36;
#pragma unroll
        for (int i = 0; i < 4; ++i) *(LAS u32x4*)(sk + (8 * i + (lane >> 3)) * 36 + c8 * 4) = W[4 * ks + i];
        asm volatile("" ::: "memory");
        LAS const unsigned* rd = sk + (8 * (lane >> 4) + ((lane >> 2) & 3)) * 36 + (lane & 3) * 2;
#pragma unroll
        for (int nt = 0; nt < 4; ++nt) dq[nt] = __builtin_amdgcn_mfma_f32_16x16x32_bf16(__builtin_bit_cast(bf16x8, af[ks]), tr8(rd + nt * 8, 4 * 36), dq[nt], 0, 0, 0);
        asm volatile("" ::: "memory");
      }
      const bool g1 = lane & 16, g2 = lane & 32;
#pragma unroll
      for (int nt = 0; nt < 4; ++nt) {
        const float a01 = g1 ? dq[nt][1] : dq[nt][0], a23 = g1 ? dq[nt][3] : dq[nt][2];
        ldf[L_QR + hw * 256 + (lane >> 4) * 64 + nt * 16 + (lane & 15)] = g2 ? a23 : a01;
      }
      helper_bar(lds + L_CNT, 4, lane);
      if (hw == 0) {
        f32x4 a = *(LAS const f32x4*)(ldf + L_QR + 4 * lane);
#pragma unroll
        for (int ww = 1; ww < 4; ++ww) a += *(LAS const f32x4*)(ldf + L_QR + ww * 256 + 4 * lane);
        st_sc(wsr0, WS_QP + ((h * 4 + (lane >> 4)) * 96 + cq * 32 + (lane & 15) * 2) * 8, u32x4{pk2(a[0], a[1]), tg.t32, pk2(a[2], a[3]), tg.t32});
      }
    }
    STAMP(3);
    if (hw == 3) {
      if (QDELAY) while (now() - tstart < QDELAY) __builtin_amdgcn_s_sleep(2);
      const unsigned o = lane < 48 ? WS_QP + (h * 4 + b) * 96 * 8 + lane * 16 : OOB, vm = lane < 48 ? ~0u : 0u;
      u32x4 g;
#pragma nounroll
      for (unsigned spin = 0; spin < SPIN_CAP; ++spin) {
        asm volatile("" ::: "memory");
        g = ld_sc(wsr0, o);
        if (__all((((g[1] ^ tg.t32) | (g[3] ^ tg.t32)) & vm) == 0)) break;
        __builtin_amdgcn_s_sleep(QSLP);
      }
      if (lane < 48) *(LAS u32x2*)(lds + L_Q + 2 * lane) = u32x2{g[0], g[2]};
      asm volatile("s_waitcnt lgkmcnt(0)" ::: "memory");
      if (lane == 0) lds[L_CNT + 7] = 1;
      STAMP(1);
    } else {
      *(LAS u32x4*)(lds + L_MIX + hid * 4) = Ma; *(LAS u32x4*)(lds + L_MIX + 768 + hid * 4) = Mb; *(LAS u32x4*)(lds + L_MIX + 1536 + hid * 4) = Mc;
      const unsigned bits0 = (TAB0 + ((bid * 32) & 4095)) | (bid >> 7) << 15;
#pragma nounroll
      for (int i0 = 0; i0 < 32; i0 += 8) {
        float ac[8];
#pragma unroll
        for (int i = 0; i < 8; ++i) {
          const float t = __uint_as_float((bits0 + i0 + i) << 16);
          ac[i] = 0.f;
#pragma unroll
          for (int k = 0; k < 4; ++k) ac[i] = mix2(t, up2(Ma[k]), up2(Mb[k]), Mc[k], ac[i]);
        }
        float y = scatter8(ac, lane & 8);
        y += dpp<XOR1>(y); y += dpp<XOR2>(y); y += dpp<HMIR>(y);
        if ((lane & 7) == 0) ldf[L_RED + hw * 32 + i0 + (lane >> 3)] = y;
      }
      helper_bar(lds + L_CNT + 3, 3, lane);
      if (hw == 2 && lane < 8) {
        f32x4 y = *(LAS const f32x4*)(ldf + L_RED + 4 * lane);
#pragma unroll
        for (int ww = 1; ww < 3; ++ww) y += *(LAS const f32x4*)(ldf + L_RED + ww * 32 + 4 * lane);
        const f32x2 y0 = rbf2(f32x2{y[0], y[1]}) + bflo(b2v), y1 = rbf2(f32x2{y[2], y[3]}) + bflo(b2v);
        st_sc(wsr0, WS_TB + bid * 128 + lane * 16, u32x4{pk2(y0[0], y0[1]), tg.t32, pk2(y1[0], y1[1]), tg.t32});
      }
    }
    const rsrc_t wor = mk(wo, has_o ? WB : 0);
    u32x4 Wo[12];
#pragma unroll
    for (int i = 0; i < 12; ++i) Wo[i] = ld_nt(wor, (unsigned)rg * (D * 2) + c8 * 16, (unsigned)(hp * 2 * DH + 32 * i) * (D * 2) + co * 128);
    if (hid < 80) *(LAS u32x4*)(lds + L_X + hid * 4) = __builtin_nontemporal_load((const u32x4*)(x + b2i * D + hid * 8));
    if (hid == 80) lds[L_X + 320] = b2v;
#pragma unroll
    for (int i = 0; i < 12; ++i) *(LAS u32x4*)(lds + L_WO + (rg + 32 * i) * 36 + c8 * 4) = Wo[i];
    helper_bar(lds + L_CNT, has_q ? 8 : 4, lane);
    LAS const unsigned* wl = lds + L_WO + (8 * (lane >> 4) + ((lane >> 2) & 3)) * 36 + hw * 8 + (lane & 3) * 2;
#pragma unroll
    for (int ks = 0; ks < 12; ++ks) Bf[ks] = tr8(wl + ks * 32 * 36, 4 * 36);
    if (hw < 2) {
      while (lds_peek(lds + L_CNT + 6) == 0) __builtin_amdgcn_s_sleep(16);
#pragma nounroll
      for (int half = 0; half < 2; ++half) {
        u32x4 g[8];
#pragma nounroll
        for (unsigned spin = 0; spin < SPIN_CAP; ++spin) {
          asm volatile("" ::: "memory");
          unsigned bad = 0;
#pragma unroll
          for (int i = 0; i < 8; ++i) g[i] = ld_sc(wsr0, WS_TB + hid * 16, (half * 8 + i) * 2048);
#pragma unroll
          for (int i = 0; i < 8; ++i) bad |= (g[i][1] ^ tg.t32) | (g[i][3] ^ tg.t32);
          if (__all(bad == 0)) break;
          __builtin_amdgcn_s_sleep(32);
        }
#pragma unroll
        for (int i = 0; i < 8; ++i) *(LAS u32x2*)(lds + L_TAB + ((half * 8 + i) * 128 + hid) * 2) = u32x2{g[i][0], g[i][2]};
      }
    }
    if (CODEPF && hw == 3) {
      while (lds_peek(lds + L_CNT + 8) == 0) __builtin_amdgcn_s_sleep(16);
      unsigned long long pc;
      int len;
      asm volatile("s_getpc_b64 %0\n.Lfd_here:\n\ts_mov_b32 %1, .Lfd_end-.Lfd_here" : "=s"(pc), "=s"(len));
      const rsrc_t cr = mk((const void*)pc, len > 0 ? len : 0);
      unsigned v = 0;
#pragma unroll
      for (int i = 0; i < CODEPF; ++i) v |= __builtin_amdgcn_raw_buffer_load_b32(cr, lane * 128, i * 8192, 0);
      asm volatile("" :: "v"(v));
    }
    if (hw == 2) {
      unsigned tm0 = 0;
#pragma nounroll
      for (int ck = 0; ck < 3; ++ck) {
        unsigned t2;
        while ((t2 = lds_peek(lds + L_CNT + (ck < 2 ? 1 + ck : 8))) == 0) __builtin_amdgcn_s_sleep(8);
        if ((bid & 3) == ((h >> 1) & 3) && lane == 0) __builtin_amdgcn_raw_buffer_store_b64(u32x2{t2, tg.t32}, wsr0, WS_TS + (ck * 64 + (bid >> 2)) * 128, 0, 16);
        while (now() - t2 < WAITT) __builtin_amdgcn_s_sleep(8);
        const u32x2 g = __builtin_amdgcn_raw_buffer_load_b64(wsr0, WS_TS + (ck * 64 + lane) * 128, 0, 16);
        const unsigned tm = t2 + (int)(sum64(g[1] == tg.t32 ? (float)min(max((int)(g[0] - t2), -2000), 2000) : (float)(WAITT + 30)) * (1.f / 64));
        const float left = ck == 1 ? (63.f - CK2) / (CK2 - CK1) * (1.f - EPS) : (63.f - CK3) / (CK3 - CK2) * (1.f - EPS3);
        if (ck && lane == 0) lds[L_CNT + 4] = (tm + (int)((float)(int)(tm - tm0) * left)) | 1u;
        tm0 = tm;
      }
    }
  }
  const unsigned long long E1 = ((unsigned long long)__builtin_amdgcn_readfirstlane(ep[1]) << 32 | (unsigned)__builtin_amdgcn_readfirstlane(ep[0])) + 1;
  const Tag tg = make_tag(E1);
  unsigned a_p1 = lane <= 48 ? WS_P1 + (h * 8 + j) * P1REC + lane * 16 : OOB;
  unsigned a_p2 = (lane & 12) ? OOB : WS_P2 + (((lane & 3) * 16 + hp) * D + co * 64 + (w - 4) * 16 + (lane >> 4) * 4) * 4;
  LAS const float* a_rec = ldf + L_REC + 4 * (lane < 48 ? lane : 48);
  LAS const unsigned* a_o = lds + L_O + (lane & 3) * OSTR + (lane >> 4) * 4;
  LAS const unsigned* own = lds + L_HH + cg * 5 + (lane < 5 ? lane : 0);
  unsigned a_out = lane < 5 ? (b2i * D + cg * 10 + 2 * lane) * 2 : OOB;
  asm volatile("" : "+v"(a_p1), "+v"(a_p2), "+v"(a_rec), "+v"(a_o), "+v"(own), "+v"(a_out));
  const rsrc_t wsr = wsr0;
  WG_BAR();
  STAMP(2);
  if (w == 4) {
    float mm[4];
    f32x4 rv[4];
#pragma unroll
    for (int i = 0; i < 4; ++i) { mm[i] = ldf[L_REC + i * 196 + 192]; rv[i] = *(LAS const f32x4*)(a_rec + i * 196); }
    __builtin_amdgcn_sched_barrier(0);
    const float M = fmaxf(fmaxf(mm[0], mm[1]), fmaxf(mm[2], mm[3]));
    f32x4 a = {0.f, 0.f, 0.f, 0.f};
#pragma unroll
    for (int i = 0; i < 4; ++i) a += __builtin_amdgcn_exp2f((mm[i] - M) * L2E) * rv[i];
    if (lane >= 48) a = f32x4{M, a[1], 0.f, 0.f};
    st_sc(wsr, a_p1, enc4(a, tg));
    STAMP(1);
  }
  const unsigned tcom = lds_peek(lds + L_CNT + 4);
  if (has_o) {
    {
      const unsigned oa = lane <= 48 ? WS_P1 + (hp * 16 + (w >> 2) * 8 + 2 * (w & 3)) * P1REC + lane * 16 : OOB;
      const unsigned vm = lane <= 48 ? ~0u : 0u;
      LAS unsigned* da = lane < 48 ? lds + L_O + (w & 3) * OSTR + (w >> 2) * 96 + 2 * lane : lds + L_QR + 2 * lane;
      asm volatile("" : "+v"(da));
      u32x4 ga, gb;
      if (C1) while ((int)(now() - tcom) < C1) { }
#pragma nounroll
      for (unsigned spin = 0; spin < SPIN_CAP; ++spin) {
        asm volatile("" ::: "memory");
        ga = ld_sc(wsr, oa); gb = ld_sc(wsr, oa, P1REC);
        if (__all(((bad4(ga, tg) | bad4(gb, tg)) & vm) == 0)) break;
        __builtin_amdgcn_s_sleep(1);
      }
      STAMP(4);
      const float m0 = dec(__builtin_amdgcn_readlane(ga[0], 48)), m1 = dec(__builtin_amdgcn_readlane(gb[0], 48)), M = fmaxf(m0, m1);
      const float l0 = dec(__builtin_amdgcn_readlane(ga[1], 48)), l1 = dec(__builtin_amdgcn_readlane(gb[1], 48));
      float w0 = __builtin_amdgcn_exp2f((m0 - M) * L2E), w1_ = __builtin_amdgcn_exp2f((m1 - M) * L2E);
      const float inv = __builtin_amdgcn_rcpf(fmaf(w0, l0, w1_ * l1));
      w0 *= inv; w1_ *= inv;
      const f32x4 o = __builtin_elementwise_fma(dec4(ga), (f32x4)w0, dec4(gb) * w1_);
      *(LAS u32x2*)da = u32x2{pk2(o[0], o[1]), pk2(o[2], o[3])};
    }
    WG_BAR();
    if (w >= 4) {
      u32x4 af[12];
#pragma unroll
      for (int ks = 0; ks < 12; ++ks) af[ks] = *(LAS const u32x4*)(a_o + ks * 16);
      __builtin_amdgcn_sched_barrier(0);
      f32x4 d0 = {0.f, 0.f, 0.f, 0.f}, d1 = d0;
#pragma unroll
      for (int ks = 0; ks < 12; ks += 2) {
        d0 = __builtin_amdgcn_mfma_f32_16x16x32_bf16(Bf[ks], __builtin_bit_cast(bf16x8, af[ks]), d0, 0, 0, 0);
        d1 = __builtin_amdgcn_mfma_f32_16x16x32_bf16(Bf[ks + 1], __builtin_bit_cast(bf16x8, af[ks + 1]), d1, 0, 0, 0);
      }
      st_sc(wsr, a_p2, enc4(d0 + d1, tg));
      if (w != 6) STAMP(3);
    }
  }
  if (w < 5) {
    const unsigned o = WS_P2 + (b2i * 16 + (lane >> 5) * 8) * D * 4 + (w * 32 + (lane & 31)) * 16;
    const int pr = (w * 32 + (lane & 31)) * 2 + (lane >> 5);
    f32x2 xr = up2(lds[L_X + pr]);
    LAS unsigned* dh = lds + L_HH + pr;
    asm volatile("" : "+v"(xr), "+v"(dh));
    u32x4 g[8];
    if (C2) while ((int)(now() - tcom) < C2) { }
#pragma nounroll
    for (unsigned spin = 0; spin < SPIN_CAP; ++spin) {
      asm volatile("" ::: "memory");
      unsigned bad = 0;
#pragma unroll
      for (int i = 0; i < 8; ++i) g[i] = ld_sc(wsr, o, i * D * 4);
#pragma unroll
      for (int i = 0; i < 8; ++i) bad |= bad4(g[i], tg);
      if (__all(bad == 0)) break;
      __builtin_amdgcn_s_sleep(1);
    }
    STAMP(5);
    const f32x4 y = ((dec4(g[0]) + dec4(g[1])) + (dec4(g[2]) + dec4(g[3]))) + ((dec4(g[4]) + dec4(g[5])) + (dec4(g[6]) + dec4(g[7])));
    const f32x2 hs = xr + rbf2(f32x2{sw32(y[0], y[2]), sw32(y[1], y[3])});
    const unsigned hp2 = pk2(hs[0], hs[1]);
    *dh = hp2;
    const f32x2 hv = up2(hp2);
    const float piv = __uint_as_float(__builtin_amdgcn_readfirstlane(__float_as_uint(hv[0])));
    const f32x2 d = hv - piv, dd = d * d;
    const float sp = sw32(d[0] + d[1], dd[0] + dd[1]), sq = sum16(sw16(sp, sp));
    const float s1 = __uint_as_float(__builtin_amdgcn_readlane(__float_as_uint(sq), 0)), s2 = __uint_as_float(__builtin_amdgcn_readlane(__float_as_uint(sq), 32));
    *(LAS f32x2*)(ldf + L_ST + 2 * w) = f32x2{fmaf(s1, 1.f / 128, piv), fmaf(-s1, s1 * (1.f / 128), s2)};
  }
  WG_BAR();
  if (w == 0) {
    const f32x4 sa = *(LAS const f32x4*)(ldf + L_ST), sb = *(LAS const f32x4*)(ldf + L_ST + 4);
    const f32x2 sc = *(LAS const f32x2*)(ldf + L_ST + 8);
    const unsigned hw2 = *own;
    __builtin_amdgcn_sched_barrier(0);
    const f32x2 hv = up2(hw2);
    const float mean = (((sa[0] + sa[2]) + (sb[0] + sb[2])) + sc[0]) * 0.2f;
    const float e0 = sa[0] - mean, e1 = sa[2] - mean, e2 = sb[0] - mean, e3 = sb[2] - mean, e4 = sc[0] - mean;
    const float var = fmaf(fmaf(e0, e0, e1 * e1) + fmaf(e2, e2, e3 * e3) + e4 * e4, 0.2f, (((sa[1] + sa[3]) + (sb[1] + sb[3])) + sc[1]) * (1.f / D));
    const float inv = inv_sd(var), mb = rbf(mean);
    const float inv2 = inv_sd(var * inv * inv), mb2 = rbf((mean - mb) * inv);
    const f32x2 tv = rbf2(rbf2(rbf2(rbf2(hv - mb) * inv) - mb2) * inv2);
    STAMP(6);
    const unsigned tb = pk2(tv[0], tv[1]), i0 = (tb & 0x7fffu) - TAB0, i1 = ((tb >> 16) & 0x7fffu) - TAB0;
    const rsrc_t outr = mk(out, 4 * D * 2);
    LAS const unsigned short* tab = (LAS const unsigned short*)(lds + L_TAB);
    f32x2 y = {bflo(tab[(i0 & 4095u) + ((tb >> 3) & 4096u)]), bflo(tab[(i1 & 4095u) + ((tb >> 19) & 4096u)])};
    if (!__all((i0 | i1) < 4096u) || FORCE_FB) {
      const float bias = bflo(lds[L_X + 320]);
#pragma nounroll
      for (int i = 0; i < 10; ++i) {
        const float t = __shfl(i & 1 ? tv[1] : tv[0], i >> 1);
        float a = 0.f;
#pragma nounroll
        for (int k = 0; k < 12; ++k) a = mix2(t, up2(lds[L_MIX + lane + 64 * k]), up2(lds[L_MIX + 768 + lane + 64 * k]), lds[L_MIX + 1536 + lane + 64 * k], a);
        a = rbf(rbf(sum64(a)) + bias);
        if (lane == (i >> 1)) { if (i & 1) y[1] = a; else y[0] = a; }
      }
    }
    const f32x2 o = hv + y;
    __builtin_amdgcn_raw_buffer_store_b32(pk2(o[0], o[1]), outr, a_out, 0, 16);
  }
  if (bid == 0 && tid == 64) __builtin_amdgcn_raw_buffer_store_b64(u32x2{(unsigned)E1, (unsigned)(E1 >> 32)}, wsr, 0, 0, 16);
  if (CODEPF) asm volatile(".Lfd_end:");
}

extern "C" void kernel_launch(void* const* d_in, const int* in_sizes, int n_in, void* d_out, int out_size, void* d_ws, size_t ws_size,
                              hipStream_t stream) {
  typedef const unsigned short* P;
  fused_decode<<<256, 512, 0, stream>>>((P)d_in[0], (P)d_in[1], (P)d_in[2], (P)d_in[3], (P)d_in[4], (P)d_in[6], (P)d_in[7], (P)d_in[8], (P)d_in[9],
                                        (unsigned short*)d_out, (unsigned char*)d_ws);
}
```

```cpp
#include <hip/hip_runtime.h>
#include <stdint.h>

typedef unsigned u32x4 __attribute__((ext_vector_type(4)));
typedef unsigned u32x2 __attribute__((ext_vector_type(2)));
typedef float f32x4 __attribute__((ext_vector_type(4)));
typedef float f32x2 __attribute__((ext_vector_type(2)));
typedef __bf16 bf16x2 __attribute__((ext_vector_type(2)));
typedef __bf16 bf16x8 __attribute__((ext_vector_type(8)));
typedef __amdgpu_buffer_rsrc_t rsrc_t;
#define LAS __attribute__((address_space(3)))

#define NPRE 6
#define QDELAY 600
#define PAIR 2
#define WAITT 400
#define CK1 14
#define CK2 26
#define SW 32
#define EPS 0.0f
#define C1 215
#define C2 415
#define CKG 28
#define FORCE_FB 0
#define STAG 85
#define STK 37
#define CK3 44
#define QSLP 8
#define EPS3 -0.02f
#define ROTX 0
#define STR2(x) #x
#define STR(x) STR2(x)

constexpr int D = 640, H = 32, SK = 4096, DH = 192, HD = H * DH, F = 1536;
constexpr unsigned SEQB = SK * DH * 2, TILEB = 8 * DH * 2, KVB = 4u * H * SEQB, WB = D * HD * 2;
constexpr unsigned P1REC = 896;
constexpr unsigned WS_TS = 4096, WS_TB = WS_TS + 4 * 64 * 128, WS_QP = WS_TB + 256 * 128, WS_P1 = WS_QP + H * 4 * 96 * 8, WS_P2 = WS_P1 + 256 * P1REC, WS_END = WS_P2 + 4 * 16 * D * 4;
constexpr unsigned OOB = 0x7ffffff0u;
constexpr unsigned SPIN_CAP = 1u << 17;
constexpr unsigned TAB0 = 100u << 7;
constexpr float L2E = 1.4426950408889634f, SCALE = 0.07216878364870322f;
constexpr int OSTR = 208;
constexpr int L_CNT = 0, L_Q = 12, L_REC = L_Q + 96, L_O = L_REC + 4 * 196, L_RED = L_O + 4 * OSTR, L_ST = L_RED + 8 * 16, L_X = L_ST + 16, L_HH = L_X + 324,
              L_XN = L_HH + 320, L_QR = L_XN + 16 * 132, L_MIX = L_QR + 4 * 256, L_WO = L_MIX + 3 * F / 2, L_Y = L_WO, L_TAB = L_WO + 2 * DH * 36, L_END = L_TAB + 4096;
constexpr int LDS_DW = 26624;
static_assert(L_END <= LDS_DW && L_REC % 4 == 0 && L_O % 4 == 0 && L_XN % 4 == 0 && L_QR % 4 == 0 && L_MIX % 4 == 0 && L_WO % 4 == 0, "lds map");

__device__ __forceinline__ float bflo(unsigned w) { return __uint_as_float(w << 16); }
__device__ __forceinline__ float bfhi(unsigned w) { return __uint_as_float(w & 0xffff0000u); }
__device__ __forceinline__ unsigned pk2(float lo, float hi) { unsigned r; asm("v_cvt_pk_bf16_f32 %0, %1, %2" : "=v"(r) : "v"(lo), "v"(hi)); return r; }
__device__ __forceinline__ float rbf(float f) { return (float)(__bf16)f; }
__device__ __forceinline__ f32x2 up2(unsigned w) { return f32x2{bflo(w), bfhi(w)}; }
__device__ __forceinline__ f32x2 rbf2(f32x2 v) { return up2(pk2(v[0], v[1])); }
__device__ __forceinline__ float dot2(unsigned a, unsigned b, float c) {
  return __builtin_amdgcn_fdot2_f32_bf16(__builtin_bit_cast(bf16x2, a), __builtin_bit_cast(bf16x2, b), c, false);
}
template <int CTRL> __device__ __forceinline__ float dpp(float x) {
  return __builtin_bit_cast(float, __builtin_amdgcn_mov_dpp(__builtin_bit_cast(int, x), CTRL, 0xf, 0xf, true));
}
constexpr int XOR1 = 0xB1, XOR2 = 0x4E, HMIR = 0x141, ROR8 = 0x128;
__device__ __forceinline__ float sw32(float a, float b) {
  const auto r = __builtin_amdgcn_permlane32_swap(__float_as_uint(a), __float_as_uint(b), false, false);
  return __uint_as_float(r[0]) + __uint_as_float(r[1]);
}
__device__ __forceinline__ float sw16(float a, float b) {
  const auto r = __builtin_amdgcn_permlane16_swap(__float_as_uint(a), __float_as_uint(b), false, false);
  return __uint_as_float(r[0]) + __uint_as_float(r[1]);
}
__device__ __forceinline__ float sum16(float v) { v += dpp<XOR1>(v); v += dpp<XOR2>(v); v += dpp<HMIR>(v); v += dpp<ROR8>(v); return v; }
__device__ __forceinline__ float sum64(float v) { v = sum16(v); v = sw16(v, v); return sw32(v, v); }
__device__ __forceinline__ float scatter8(const float* a, bool b3) {
  float y[4], z[2];
#pragma unroll
  for (int e = 0; e < 4; ++e) y[e] = sw32(a[e], a[e + 4]);
#pragma unroll
  for (int e = 0; e < 2; ++e) z[e] = sw16(y[e], y[e + 2]);
  return (b3 ? z[1] : z[0]) + dpp<ROR8>(b3 ? z[0] : z[1]);
}
typedef short s16x4 __attribute__((ext_vector_type(4)));
__device__ __forceinline__ bf16x8 tr8(LAS const unsigned* p, int step) {
  const s16x4 a = __builtin_amdgcn_ds_read_tr16_b64_v4i16((LAS s16x4*)p), b = __builtin_amdgcn_ds_read_tr16_b64_v4i16((LAS s16x4*)(p + step));
  return __builtin_bit_cast(bf16x8, __builtin_shufflevector(a, b, 0, 1, 2, 3, 4, 5, 6, 7));
}
__device__ __forceinline__ rsrc_t mk(const void* p, unsigned n) { return __builtin_amdgcn_make_buffer_rsrc((void*)p, 0, n, 0x00020000); }
__device__ __forceinline__ u32x4 ld_nt(rsrc_t r, unsigned v, unsigned s) { return __builtin_amdgcn_raw_buffer_load_b128(r, v, s, 2); }
__device__ __forceinline__ u32x4 ld_sc(rsrc_t r, unsigned v, unsigned s = 0) { return __builtin_amdgcn_raw_buffer_load_b128(r, v, s, 16); }
__device__ __forceinline__ void st_sc(rsrc_t r, unsigned v, u32x4 x) { __builtin_amdgcn_raw_buffer_store_b128(x, r, v, 0, 16); }
__device__ __forceinline__ unsigned now() { return (unsigned)__builtin_amdgcn_s_memrealtime(); }
__device__ __forceinline__ unsigned lds_peek(LAS unsigned* p) {
  return __builtin_amdgcn_readfirstlane(__hip_atomic_load(p, __ATOMIC_RELAXED, __HIP_MEMORY_SCOPE_WORKGROUP));
}
#define PIN() do { asm volatile("" ::: "memory"); __builtin_amdgcn_sched_barrier(0); } while (0)
#define WG_BAR() do { asm volatile("s_waitcnt lgkmcnt(0)" ::: "memory"); __builtin_amdgcn_s_barrier(); PIN(); } while (0)
#define STAMP(i) do { } while (0)
struct Tag { unsigned b0, b1, b2, b3, t32; };
__device__ __forceinline__ Tag make_tag(unsigned long long e) {
  const unsigned t = (unsigned)e;
  return Tag{t & 0xffu, (t + (t >> 8)) & 0xffu, (t + (t >> 16)) & 0xffu, (t + (t >> 24)) & 0xffu, t};
}
__device__ __forceinline__ u32x4 enc4(f32x4 v, const Tag& t) {
  return u32x4{(__float_as_uint(v[0]) & 0xffffff00u) | t.b0, (__float_as_uint(v[1]) & 0xffffff00u) | t.b1, (__float_as_uint(v[2]) & 0xffffff00u) | t.b2,
               (__float_as_uint(v[3]) & 0xffffff00u) | t.b3};
}
__device__ __forceinline__ unsigned bad4(u32x4 g, const Tag& t) { return ((g[0] ^ t.b0) | (g[1] ^ t.b1) | (g[2] ^ t.b2) | (g[3] ^ t.b3)) & 0xffu; }
__device__ __forceinline__ f32x4 dec4(u32x4 g) { return __builtin_bit_cast(f32x4, g & 0xffffff00u); }
__device__ __forceinline__ float dec(unsigned g) { return __uint_as_float(g & 0xffffff00u); }
__device__ __forceinline__ void helper_bar(LAS unsigned* cnt, unsigned target, int lane) {
  asm volatile("s_waitcnt lgkmcnt(0)" ::: "memory");
  if (lane == 0) __hip_atomic_fetch_add(cnt, 1u, __ATOMIC_RELAXED, __HIP_MEMORY_SCOPE_WORKGROUP);
  while (lds_peek(cnt) < target) { }
  asm volatile("" ::: "memory");
}
__device__ __forceinline__ float mix2(float t, f32x2 wa, f32x2 wb, unsigned wc, float acc) {
  constexpr float K0 = -2.f * L2E * 0.7978845608028654f, K1 = K0 * 0.044715f;
  const f32x2 u = rbf2(rbf2(wa * t) + wb);
  const f32x2 z = u * __builtin_elementwise_fma(u * u, (f32x2)K1, (f32x2)K0);
  const f32x2 d = f32x2{__builtin_amdgcn_exp2f(z[0]), __builtin_amdgcn_exp2f(z[1])} + 1.f;
  const f32x2 g = u * f32x2{__builtin_amdgcn_rcpf(d[0]), __builtin_amdgcn_rcpf(d[1])};
  return dot2(pk2(g[0], g[1]), wc, acc);
}
__device__ __forceinline__ float inv_sd(float var) { return __builtin_amdgcn_rcpf(rbf(__builtin_amdgcn_sqrtf(rbf(rbf(var) + 1e-5f)))); }

__global__ void __launch_bounds__(512, 2)
fused_decode(const unsigned short* x, const unsigned short* kc, const unsigned short* vc, const unsigned short* wq, const unsigned short* wo,
             const unsigned short* w1, const unsigned short* b1, const unsigned short* w2, const unsigned short* b2, unsigned short* out,
             unsigned char* ws) {
  __shared__ __attribute__((aligned(16))) unsigned lds_raw[LDS_DW];
  LAS unsigned* lds = (LAS unsigned*)lds_raw;
  LAS float* ldf = (LAS float*)lds_raw;
  const int tid = threadIdx.x, lane = tid & 63, w = __builtin_amdgcn_readfirstlane(tid >> 6);
  const int bid = blockIdx.x, h = bid >> 3, j = (bid + h) & 7, b = j >> 1, s = j & 1;
  const bool has_q = j >= 2 && s == 1, has_o = j < 2 || s == 0;
  const int hp = h >> 1, co = (h & 1) * 5 + (j < 2 ? j : b + 1);
  const int b2i = bid >> 6, cg = bid & 63;
  STAMP(0);
  const unsigned tstart = now();
  asm volatile("" :: "s"(x), "s"(kc), "s"(vc), "s"(wq), "s"(wo), "s"(w1), "s"(b1), "s"(w2), "s"(b2), "s"(out), "s"(ws));
  const rsrc_t wsr0 = mk(ws, WS_END);
  u32x2 ep = __builtin_amdgcn_raw_buffer_load_b64(wsr0, 0, 0, 16);
  bf16x8 Bf[12];

  if (w < 4) {
    __builtin_amdgcn_s_setprio(2);
    const int r = lane >> 3, c = lane & 7;
    constexpr int ntw = 256, NT = 64;
    const unsigned seq = (unsigned)(b * H + h) * SEQB;
    const int rot = __builtin_amdgcn_readfirstlane((ROTX) % ntw), y0 = s ? 512 - ntw : 0;
    const bool hi = r >> 2;
    const unsigned kb = PAIR == 1 ? (hi ? 171 : 0) : (hi ? 86 : 0), c1 = PAIR == 1 ? 171 : (hi ? 85 : 86), d1 = PAIR == 1 ? (hi ? 170 : 171) : 85,
                   c2 = PAIR == 1 ? (hi ? 342 : 341) : 171, d2 = PAIR == 1 ? 171 : (hi ? 85 : 86);
    constexpr int PB = PAIR == 1 ? 9 : 8;
    auto ty = [&](int i) -> int { const int y1 = i * 4 + w + rot; return y0 + (y1 >= ntw ? y1 - ntw : y1); };
    auto tv = [&](int i) -> unsigned {
      if (!PAIR) return (unsigned)(r * 384 + c * 16);
      const unsigned mm = ((4 * ty(i)) & ((1 << PB) - 1)) + (r & 3);
      return (mm + kb + (mm >= c1 ? d1 : 0) + (mm >= c2 ? d2 : 0)) * 384 + c * 16; };
    auto ts = [&](int i) -> unsigned {
      return __builtin_amdgcn_readfirstlane(PAIR ? seq + (unsigned)((4 * ty(i)) >> PB) * (768u << PB) : seq + ty(i) * TILEB); };
    const rsrc_t kr = mk(kc, KVB), vr = mk(vc, KVB);
    u32x4 K[NPRE][3], V[NPRE][3];
    WG_BAR();
#pragma unroll
    for (int p = 0; p < NPRE; ++p) {
      if (p) asm volatile("s_waitcnt vmcnt(3)" ::: "memory");
#pragma unroll
      for (int i = 0; i < 3; ++i) K[p][i] = ld_nt(kr, tv(p) + 128 * i, ts(p));
      PIN();
      if (p) asm volatile("s_waitcnt vmcnt(3)" ::: "memory");
#pragma unroll
      for (int i = 0; i < 3; ++i) V[p][i] = ld_nt(vr, tv(p) + 128 * i, ts(p));
      PIN();
    }
    while (lds_peek(lds + L_CNT + 7) == 0) __builtin_amdgcn_s_sleep(1);
    PIN();
    STAMP(1);
    u32x4 qf[3];
#pragma unroll
    for (int i = 0; i < 3; ++i) qf[i] = *(LAS const u32x4*)(lds + L_Q + (c + 8 * i) * 4);
    float m = __uint_as_float(0xff7f0000u), l = 0.f, acc[24];
#pragma unroll
    for (int k = 0; k < 24; ++k) acc[k] = 0.f;
    unsigned pace = 0;
    bool stamp3 = false;
    auto step_fn = [&](u32x4 (&Kc)[3], u32x4 (&Vc)[3], bool refill, unsigned nbytes, unsigned av, unsigned as) {
      float a0 = 0.f, a1 = 0.f, a2 = 0.f;
#pragma unroll
      for (int k = 0; k < 4; ++k) { a0 = dot2(Kc[0][k], qf[0][k], a0); a1 = dot2(Kc[1][k], qf[1][k], a1); a2 = dot2(Kc[2][k], qf[2][k], a2); }
      float a = (a0 + a1) + a2;
      asm volatile("" : "+v"(a));
      PIN();
      if (refill) {
        if (pace) { unsigned tn; while ((int)(((tn = now()) << 6) - pace) < 0) { }
                    if (stamp3) { if (lane == 0) lds[L_CNT + 8] = tn | 1u; stamp3 = false; } }
        const rsrc_t k2 = mk(kc, nbytes);
#pragma unroll
        for (int i = 0; i < 3; ++i) Kc[i] = ld_nt(k2, av + 128 * i, as);
      }
      PIN();
      f32x2 vf[12];
#pragma unroll
      for (int k = 0; k < 12; ++k) vf[k] = up2(Vc[k >> 2][k & 3]);
      if (refill) {
        asm volatile("" : "+v"(vf[0]), "+v"(vf[1]), "+v"(vf[2]), "+v"(vf[3]), "+v"(vf[4]), "+v"(vf[5]));
        asm volatile("" : "+v"(vf[6]), "+v"(vf[7]), "+v"(vf[8]), "+v"(vf[9]), "+v"(vf[10]), "+v"(vf[11]));
        PIN();
        const rsrc_t v2 = mk(vc, nbytes);
#pragma unroll
        for (int i = 0; i < 3; ++i) Vc[i] = ld_nt(v2, av + 128 * i, as);
        PIN();
      }
      a += dpp<XOR1>(a); a += dpp<XOR2>(a); a += dpp<HMIR>(a);
      const float sc = rbf(rbf(a) * SCALE);
      const float mn = fmaxf(m, sc);
      const float alpha = __builtin_amdgcn_exp2f((m - mn) * L2E), p = __builtin_amdgcn_exp2f((sc - mn) * L2E);
      l = fmaf(l, alpha, p); m = mn;
#pragma unroll
      for (int k = 0; k < 12; ++k) { acc[2 * k] = fmaf(p, vf[k][0], acc[2 * k] * alpha); acc[2 * k + 1] = fmaf(p, vf[k][1], acc[2 * k + 1] * alpha); }
      PIN();
    };
#pragma unroll
    for (int p = 0; p < NPRE; ++p) step_fn(K[p], V[p], p == 0, KVB, tv(NPRE), ts(NPRE));
    int t = NPRE + 1;
    do {
      if (w == 1 && t == CKG && lane == 0) lds[L_CNT + 6] = 1;
      if (w == 0 && (t == CK1 || t == CK2)) { const unsigned tn = now() | 1u; if (lane == 0) lds[L_CNT + (t == CK1 ? 1 : 2)] = tn; }
      step_fn(K[0], V[0], true, KVB, tv(t), ts(t));
    } while (++t < SW);
    static_assert(NPRE < CK1 && CK1 < CK2 && CK2 < SW && NPRE < CKG && CKG < SW && SW + 2 <= CK3 && CK3 < 62 && (CK3 - SW) % 2 == 0 && (NT - SW) % 2 == 0, "every stamp the helpers wait for is reached");
    unsigned tend;
    while ((tend = lds_peek(lds + L_CNT + 4)) == 0) __builtin_amdgcn_s_sleep(1);
    const int stag = (STAG * ((((bid * STK) & 63) + 64 * w) - 128) * 64) >> 8;
    unsigned tgt = now() << 6;
    int tau = (int)((tend << 6) + stag - tgt) / (NT - 1 - SW);
#pragma unroll
    for (int i = 0; i < 3; ++i) K[1][i] = ld_nt(kr, tv(t) + 128 * i, ts(t));
    PIN();
#pragma unroll
    for (int i = 0; i < 3; ++i) V[1][i] = ld_nt(vr, tv(t) + 128 * i, ts(t));
    PIN();
    do {
      const unsigned t3 = lds_peek(lds + L_CNT + 4);
      if (t3 != tend) { tend = t3; tau = (int)((tend << 6) + stag - tgt) / max(NT - 1 - t, 1); }
      tgt += tau;
      pace = tgt | 1u;
      step_fn(K[0], V[0], true, KVB, tv(t + 1), ts(t + 1));
      tgt += tau;
      pace = t + 2 < NT ? tgt | 1u : 0u;
      if (w == 0 && t + 2 == CK3) stamp3 = true;
      step_fn(K[1], V[1], true, t + 2 < NT ? KVB : 0u, tv(t + 2), ts(t + 2));
      t += 2;
    } while (t < NT);
    step_fn(K[0], V[0], false, 0, 0, 0);
    float M = fmaxf(m, dpp<ROR8>(m));
    { const auto q16 = __builtin_amdgcn_permlane16_swap(__float_as_uint(M), __float_as_uint(M), false, false);
      M = fmaxf(__uint_as_float(q16[0]), __uint_as_float(q16[1]));
      const auto q32 = __builtin_amdgcn_permlane32_swap(__float_as_uint(M), __float_as_uint(M), false, false);
      M = fmaxf(__uint_as_float(q32[0]), __uint_as_float(q32[1])); }
    const float f = __builtin_amdgcn_exp2f((m - M) * L2E);
    l *= f; l += dpp<ROR8>(l); l = sw16(l, l); l = sw32(l, l);
#pragma unroll
    for (int k = 0; k < 24; ++k) acc[k] *= f;
#pragma unroll
    for (int i = 0; i < 3; ++i) ldf[L_REC + w * 196 + i * 64 + c * 8 + r] = scatter8(acc + i * 8, lane & 8);
    if (lane == 0) { ldf[L_REC + w * 196 + 192] = M; ldf[L_REC + w * 196 + 193] = l; }
  } else {
    const int hid = tid - 256, hw = w - 4;
    if (hid < 12) lds[L_CNT + hid] = 0;
    const int cq = (j - 2) >> 1, rg = hid >> 3, c8 = hid & 7;
    u32x4 W[20], X[5];
    const rsrc_t xr = mk(x, has_q ? 4 * D * 2 : 0), wqr = mk(wq, has_q ? WB : 0);
#pragma unroll
    for (int i = 0; i < 5; ++i) { const int ch = 20 * hw + (lane & 15) + 16 * i; X[i] = ld_nt(xr, (lane >> 4) * (D * 2) + (ch >= 80 ? ch - 80 : ch) * 16, 0); }
    PIN();
#pragma unroll
    for (int i = 0; i < 20; ++i) {
      W[i] = ld_nt(wqr, (unsigned)(lane >> 3) * (HD * 2) + c8 * 16, (unsigned)(160 * hw + 8 * i) * (HD * 2) + (h * DH + cq * 64) * 2);
      if ((i & 3) == 3) PIN();
    }
    const unsigned mo = hid < 192 ? hid * 16 : OOB;
    const u32x4 Ma = ld_nt(mk(w1, F * 2), mo, 0), Mb = ld_nt(mk(b1, F * 2), mo, 0), Mc = ld_nt(mk(w2, F * 2), mo, 0);
    const unsigned b2v = __builtin_amdgcn_raw_buffer_load_b16(mk(b2, 2), 0, 0, 2);
    PIN();
    WG_BAR();
    asm volatile("" : "+v"(ep));
    const unsigned long long E1 = ((unsigned long long)__builtin_amdgcn_readfirstlane(ep[1]) << 32 | (unsigned)__builtin_amdgcn_readfirstlane(ep[0])) + 1;
    const Tag tg = make_tag(E1);
    if (has_q) {
      float sa = 0.f, sb = 0.f;
#pragma unroll
      for (int i = 0; i < 20; i += 2) { sa = dot2(X[i >> 2][i & 3], 0x3f803f80u, sa); sb = dot2(X[i >> 2][(i & 3) + 1], 0x3f803f80u, sb); }
      const float mean = sum16(sa + sb) * (1.f / D);
      f32x2 s2 = {0.f, 0.f}, s3 = s2;
#pragma unroll
      for (int i = 0; i < 20; i += 2) {
        const f32x2 d = up2(X[i >> 2][i & 3]) - mean, e = up2(X[i >> 2][(i & 3) + 1]) - mean;
        s2 = __builtin_elementwise_fma(d, d, s2); s3 = __builtin_elementwise_fma(e, e, s3);
      }
      s2 += s3;
      const float inv = inv_sd(sum16(s2[0] + s2[1]) * (1.f / D)), mb = rbf(mean);
      LAS unsigned* xn = lds + L_XN + hw * 4 * 132;
#pragma unroll
      for (int i = 0; i < 2; ++i) {
        u32x4 o;
#pragma unroll
        for (int k = 0; k < 4; ++k) { const f32x2 y = rbf2(up2(X[i][k]) - mb) * inv; o[k] = pk2(y[0], y[1]); }
        *(LAS u32x4*)(xn + (lane >> 4) * 132 + ((lane & 15) + 16 * i) * 4) = o;
      }
      asm volatile("" ::: "memory");
      u32x4 af[5];
#pragma unroll
      for (int ks = 0; ks < 5; ++ks) af[ks] = *(LAS const u32x4*)(xn + (lane & 3) * 132 + ks * 16 + (lane >> 4) * 4);
      LAS unsigned* st = lds + L_WO + hw * 64 * 36;
      f32x4 dq[4];
#pragma unroll
      for (int nt = 0; nt < 4; ++nt) dq[nt] = f32x4{0.f, 0.f, 0.f, 0.f};
#pragma unroll
      for (int ks = 0; ks < 5; ++ks) {
        LAS unsigned* sk = st + (ks & 1) * 32 * 36;
#pragma unroll
        for (int i = 0; i < 4; ++i) *(LAS u32x4*)(sk + (8 * i + (lane >> 3)) * 36 + c8 * 4) = W[4 * ks + i];
        asm volatile("" ::: "memory");
        LAS const unsigned* rd = sk + (8 * (lane >> 4) + ((lane >> 2) & 3)) * 36 + (lane & 3) * 2;
#pragma unroll
        for (int nt = 0; nt < 4; ++nt) dq[nt] = __builtin_amdgcn_mfma_f32_16x16x32_bf16(__builtin_bit_cast(bf16x8, af[ks]), tr8(rd + nt * 8, 4 * 36), dq[nt], 0, 0, 0);
        asm volatile("" ::: "memory");
      }
      const bool g1 = lane & 16, g2 = lane & 32;
#pragma unroll
      for (int nt = 0; nt < 4; ++nt) {
        const float a01 = g1 ? dq[nt][1] : dq[nt][0], a23 = g1 ? dq[nt][3] : dq[nt][2];
        ldf[L_QR + hw * 256 + (lane >> 4) * 64 + nt * 16 + (lane & 15)] = g2 ? a23 : a01;
      }
      helper_bar(lds + L_CNT, 4, lane);
      if (hw == 0) {
        f32x4 a = *(LAS const f32x4*)(ldf + L_QR + 4 * lane);
#pragma unroll
        for (int ww = 1; ww < 4; ++ww) a += *(LAS const f32x4*)(ldf + L_QR + ww * 256 + 4 * lane);
        st_sc(wsr0, WS_QP + ((h * 4 + (lane >> 4)) * 96 + cq * 32 + (lane & 15) * 2) * 8, u32x4{pk2(a[0], a[1]), tg.t32, pk2(a[2], a[3]), tg.t32});
      }
    }
    STAMP(3);
    if (hw == 3) {
      if (QDELAY) while (now() - tstart < QDELAY) __builtin_amdgcn_s_sleep(2);
      const unsigned o = lane < 48 ? WS_QP + (h * 4 + b) * 96 * 8 + lane * 16 : OOB, vm = lane < 48 ? ~0u : 0u;
      u32x4 g;
#pragma nounroll
      for (unsigned spin = 0; spin < SPIN_CAP; ++spin) {
        asm volatile("" ::: "memory");
        g = ld_sc(wsr0, o);
        if (__all((((g[1] ^ tg.t32) | (g[3] ^ tg.t32)) & vm) == 0)) break;
        __builtin_amdgcn_s_sleep(QSLP);
      }
      if (lane < 48) *(LAS u32x2*)(lds + L_Q + 2 * lane) = u32x2{g[0], g[2]};
      asm volatile("s_waitcnt lgkmcnt(0)" ::: "memory");
      if (lane == 0) lds[L_CNT + 7] = 1;
      STAMP(1);
    } else {
      *(LAS u32x4*)(lds + L_MIX + hid * 4) = Ma; *(LAS u32x4*)(lds + L_MIX + 768 + hid * 4) = Mb; *(LAS u32x4*)(lds + L_MIX + 1536 + hid * 4) = Mc;
      const unsigned bits0 = (TAB0 + ((bid * 32) & 4095)) | (bid >> 7) << 15;
#pragma nounroll
      for (int i0 = 0; i0 < 32; i0 += 8) {
        float ac[8];
#pragma unroll
        for (int i = 0; i < 8; ++i) {
          const float t = __uint_as_float((bits0 + i0 + i) << 16);
          ac[i] = 0.f;
#pragma unroll
          for (int k = 0; k < 4; ++k) ac[i] = mix2(t, up2(Ma[k]), up2(Mb[k]), Mc[k], ac[i]);
        }
        float y = scatter8(ac, lane & 8);
        y += dpp<XOR1>(y); y += dpp<XOR2>(y); y += dpp<HMIR>(y);
        if ((lane & 7) == 0) ldf[L_RED + hw * 32 + i0 + (lane >> 3)] = y;
      }
      helper_bar(lds + L_CNT + 3, 3, lane);
      if (hw == 2 && lane < 8) {
        f32x4 y = *(LAS const f32x4*)(ldf + L_RED + 4 * lane);
#pragma unroll
        for (int ww = 1; ww < 3; ++ww) y += *(LAS const f32x4*)(ldf + L_RED + ww * 32 + 4 * lane);
        const f32x2 y0 = rbf2(f32x2{y[0], y[1]}) + bflo(b2v), y1 = rbf2(f32x2{y[2], y[3]}) + bflo(b2v);
        st_sc(wsr0, WS_TB + bid * 128 + lane * 16, u32x4{pk2(y0[0], y0[1]), tg.t32, pk2(y1[0], y1[1]), tg.t32});
      }
    }
    const rsrc_t wor = mk(wo, has_o ? WB : 0);
    u32x4 Wo[12];
#pragma unroll
    for (int i = 0; i < 12; ++i) Wo[i] = ld_nt(wor, (unsigned)rg * (D * 2) + c8 * 16, (unsigned)(hp * 2 * DH + 32 * i) * (D * 2) + co * 128);
    if (hid < 80) *(LAS u32x4*)(lds + L_X + hid * 4) = __builtin_nontemporal_load((const u32x4*)(x + b2i * D + hid * 8));
    if (hid == 80) lds[L_X + 320] = b2v;
#pragma unroll
    for (int i = 0; i < 12; ++i) *(LAS u32x4*)(lds + L_WO + (rg + 32 * i) * 36 + c8 * 4) = Wo[i];
    helper_bar(lds + L_CNT, has_q ? 8 : 4, lane);
    LAS const unsigned* wl = lds + L_WO + (8 * (lane >> 4) + ((lane >> 2) & 3)) * 36 + hw * 8 + (lane & 3) * 2;
#pragma unroll
    for (int ks = 0; ks < 12; ++ks) Bf[ks] = tr8(wl + ks * 32 * 36, 4 * 36);
    if (hw < 2) {
      while (lds_peek(lds + L_CNT + 6) == 0) __builtin_amdgcn_s_sleep(16);
#pragma nounroll
      for (int half = 0; half < 2; ++half) {
        u32x4 g[8];
#pragma nounroll
        for (unsigned spin = 0; spin < SPIN_CAP; ++spin) {
          asm volatile("" ::: "memory");
          unsigned bad = 0;
#pragma unroll
          for (int i = 0; i < 8; ++i) g[i] = ld_sc(wsr0, WS_TB + hid * 16, (half * 8 + i) * 2048);
#pragma unroll
          for (int i = 0; i < 8; ++i) bad |= (g[i][1] ^ tg.t32) | (g[i][3] ^ tg.t32);
          if (__all(bad == 0)) break;
          __builtin_amdgcn_s_sleep(32);
        }
#pragma unroll
        for (int i = 0; i < 8; ++i) *(LAS u32x2*)(lds + L_TAB + ((half * 8 + i) * 128 + hid) * 2) = u32x2{g[i][0], g[i][2]};
      }
    }
    if (hw == 2) {
      unsigned tm0 = 0;
#pragma nounroll
      for (int ck = 0; ck < 3; ++ck) {
        unsigned t2;
        while ((t2 = lds_peek(lds + L_CNT + (ck < 2 ? 1 + ck : 8))) == 0) __builtin_amdgcn_s_sleep(8);
        if ((bid & 3) == ((h >> 1) & 3) && lane == 0) __builtin_amdgcn_raw_buffer_store_b64(u32x2{t2, tg.t32}, wsr0, WS_TS + (ck * 64 + (bid >> 2)) * 128, 0, 16);
        while (now() - t2 < WAITT) __builtin_amdgcn_s_sleep(8);
        const u32x2 g = __builtin_amdgcn_raw_buffer_load_b64(wsr0, WS_TS + (ck * 64 + lane) * 128, 0, 16);
        const unsigned tm = t2 + (int)(sum64(g[1] == tg.t32 ? (float)min(max((int)(g[0] - t2), -2000), 2000) : (float)(WAITT + 30)) * (1.f / 64));
        const float left = ck == 1 ? (63.f - CK2) / (CK2 - CK1) * (1.f - EPS) : (63.f - CK3) / (CK3 - CK2) * (1.f - EPS3);
        if (ck && lane == 0) lds[L_CNT + 4] = (tm + (int)((float)(int)(tm - tm0) * left)) | 1u;
        tm0 = tm;
      }
    }
  }
  const unsigned long long E1 = ((unsigned long long)__builtin_amdgcn_readfirstlane(ep[1]) << 32 | (unsigned)__builtin_amdgcn_readfirstlane(ep[0])) + 1;
  const Tag tg = make_tag(E1);
  unsigned a_p1 = lane <= 48 ? WS_P1 + (h * 8 + j) * P1REC + lane * 16 : OOB;
  unsigned a_p2 = (lane & 12) ? OOB : WS_P2 + (((lane & 3) * 16 + hp) * D + co * 64 + (w - 4) * 16 + (lane >> 4) * 4) * 4;
  LAS const float* a_rec = ldf + L_REC + 4 * (lane < 48 ? lane : 48);
  LAS const unsigned* a_o = lds + L_O + (lane & 3) * OSTR + (lane >> 4) * 4;
  LAS const unsigned* own = lds + L_HH + cg * 5 + (lane < 5 ? lane : 0);
  unsigned a_out = lane < 5 ? (b2i * D + cg * 10 + 2 * lane) * 2 : OOB;
  asm volatile("" : "+v"(a_p1), "+v"(a_p2), "+v"(a_rec), "+v"(a_o), "+v"(own), "+v"(a_out));
  const rsrc_t wsr = wsr0;
  WG_BAR();
  STAMP(2);
  if (w == 4) {
    float mm[4];
    f32x4 rv[4];
#pragma unroll
    for (int i = 0; i < 4; ++i) { mm[i] = ldf[L_REC + i * 196 + 192]; rv[i] = *(LAS const f32x4*)(a_rec + i * 196); }
    __builtin_amdgcn_sched_barrier(0);
    const float M = fmaxf(fmaxf(mm[0], mm[1]), fmaxf(mm[2], mm[3]));
    f32x4 a = {0.f, 0.f, 0.f, 0.f};
#pragma unroll
    for (int i = 0; i < 4; ++i) a += __builtin_amdgcn_exp2f((mm[i] - M) * L2E) * rv[i];
    if (lane >= 48) a = f32x4{M, a[1], 0.f, 0.f};
    st_sc(wsr, a_p1, enc4(a, tg));
    STAMP(1);
  }
  const unsigned tcom = lds_peek(lds + L_CNT + 4);
  if (has_o) {
    {
      const unsigned oa = lane <= 48 ? WS_P1 + (hp * 16 + (w >> 2) * 8 + 2 * (w & 3)) * P1REC + lane * 16 : OOB;
      const unsigned vm = lane <= 48 ? ~0u : 0u;
      LAS unsigned* da = lane < 48 ? lds + L_O + (w & 3) * OSTR + (w >> 2) * 96 + 2 * lane : lds + L_QR + 2 * lane;
      asm volatile("" : "+v"(da));
      u32x4 ga, gb;
      if (C1) while ((int)(now() - tcom) < C1) { }
#pragma nounroll
      for (unsigned spin = 0; spin < SPIN_CAP; ++spin) {
        asm volatile("" ::: "memory");
        ga = ld_sc(wsr, oa); gb = ld_sc(wsr, oa, P1REC);
        if (__all(((bad4(ga, tg) | bad4(gb, tg)) & vm) == 0)) break;
        __builtin_amdgcn_s_sleep(1);
      }
      STAMP(4);
      const float m0 = dec(__builtin_amdgcn_readlane(ga[0], 48)), m1 = dec(__builtin_amdgcn_readlane(gb[0], 48)), M = fmaxf(m0, m1);
      const float l0 = dec(__builtin_amdgcn_readlane(ga[1], 48)), l1 = dec(__builtin_amdgcn_readlane(gb[1], 48));
      float w0 = __builtin_amdgcn_exp2f((m0 - M) * L2E), w1_ = __builtin_amdgcn_exp2f((m1 - M) * L2E);
      const float inv = __builtin_amdgcn_rcpf(fmaf(w0, l0, w1_ * l1));
      w0 *= inv; w1_ *= inv;
      const f32x4 o = __builtin_elementwise_fma(dec4(ga), (f32x4)w0, dec4(gb) * w1_);
      *(LAS u32x2*)da = u32x2{pk2(o[0], o[1]), pk2(o[2], o[3])};
    }
    WG_BAR();
    if (w >= 4) {
      u32x4 af[12];
#pragma unroll
      for (int ks = 0; ks < 12; ++ks) af[ks] = *(LAS const u32x4*)(a_o + ks * 16);
      __builtin_amdgcn_sched_barrier(0);
      f32x4 d0 = {0.f, 0.f, 0.f, 0.f}, d1 = d0;
#pragma unroll
      for (int ks = 0; ks < 12; ks += 2) {
        d0 = __builtin_amdgcn_mfma_f32_16x16x32_bf16(Bf[ks], __builtin_bit_cast(bf16x8, af[ks]), d0, 0, 0, 0);
        d1 = __builtin_amdgcn_mfma_f32_16x16x32_bf16(Bf[ks + 1], __builtin_bit_cast(bf16x8, af[ks + 1]), d1, 0, 0, 0);
      }
      st_sc(wsr, a_p2, enc4(d0 + d1, tg));
      if (w != 6) STAMP(3);
    }
  }
  if (w < 5) {
    const unsigned o = WS_P2 + (b2i * 16 + (lane >> 5) * 8) * D * 4 + (w * 32 + (lane & 31)) * 16;
    const int pr = (w * 32 + (lane & 31)) * 2 + (lane >> 5);
    f32x2 xr = up2(lds[L_X + pr]);
    LAS unsigned* dh = lds + L_HH + pr;
    asm volatile("" : "+v"(xr), "+v"(dh));
    u32x4 g[8];
    if (C2) while ((int)(now() - tcom) < C2) { }
#pragma nounroll
    for (unsigned spin = 0; spin < SPIN_CAP; ++spin) {
      asm volatile("" ::: "memory");
      unsigned bad = 0;
#pragma unroll
      for (int i = 0; i < 8; ++i) g[i] = ld_sc(wsr, o, i * D * 4);
#pragma unroll
      for (int i = 0; i < 8; ++i) bad |= bad4(g[i], tg);
      if (__all(bad == 0)) break;
      __builtin_amdgcn_s_sleep(1);
    }
    STAMP(5);
    const f32x4 y = ((dec4(g[0]) + dec4(g[1])) + (dec4(g[2]) + dec4(g[3]))) + ((dec4(g[4]) + dec4(g[5])) + (dec4(g[6]) + dec4(g[7])));
    const f32x2 hs = xr + rbf2(f32x2{sw32(y[0], y[2]), sw32(y[1], y[3])});
    const unsigned hp2 = pk2(hs[0], hs[1]);
    *dh = hp2;
    const f32x2 hv = up2(hp2);
    const float piv = __uint_as_float(__builtin_amdgcn_readfirstlane(__float_as_uint(hv[0])));
    const f32x2 d = hv - piv, dd = d * d;
    const float sp = sw32(d[0] + d[1], dd[0] + dd[1]), sq = sum16(sw16(sp, sp));
    const float s1 = __uint_as_float(__builtin_amdgcn_readlane(__float_as_uint(sq), 0)), s2 = __uint_as_float(__builtin_amdgcn_readlane(__float_as_uint(sq), 32));
    *(LAS f32x2*)(ldf + L_ST + 2 * w) = f32x2{fmaf(s1, 1.f / 128, piv), fmaf(-s1, s1 * (1.f / 128), s2)};
  }
  WG_BAR();
  if (w == 0) {
    const f32x4 sa = *(LAS const f32x4*)(ldf + L_ST), sb = *(LAS const f32x4*)(ldf + L_ST + 4);
    const f32x2 sc = *(LAS const f32x2*)(ldf + L_ST + 8);
    const unsigned hw2 = *own;
    __builtin_amdgcn_sched_barrier(0);
    const f32x2 hv = up2(hw2);
    const float mean = (((sa[0] + sa[2]) + (sb[0] + sb[2])) + sc[0]) * 0.2f;
    const float e0 = sa[0] - mean, e1 = sa[2] - mean, e2 = sb[0] - mean, e3 = sb[2] - mean, e4 = sc[0] - mean;
    const float var = fmaf(fmaf(e0, e0, e1 * e1) + fmaf(e2, e2, e3 * e3) + e4 * e4, 0.2f, (((sa[1] + sa[3]) + (sb[1] + sb[3])) + sc[1]) * (1.f / D));
    const float inv = inv_sd(var), mb = rbf(mean);
    const float inv2 = inv_sd(var * inv * inv), mb2 = rbf((mean - mb) * inv);
    const f32x2 tv = rbf2(rbf2(rbf2(rbf2(hv - mb) * inv) - mb2) * inv2);
    STAMP(6);
    const unsigned tb = pk2(tv[0], tv[1]), i0 = (tb & 0x7fffu) - TAB0, i1 = ((tb >> 16) & 0x7fffu) - TAB0;
    const rsrc_t outr = mk(out, 4 * D * 2);
    LAS const unsigned short* tab = (LAS const unsigned short*)(lds + L_TAB);
    f32x2 y = {bflo(tab[(i0 & 4095u) + ((tb >> 3) & 4096u)]), bflo(tab[(i1 & 4095u) + ((tb >> 19) & 4096u)])};
    if (!__all((i0 | i1) < 4096u) || FORCE_FB) {
      const float bias = bflo(lds[L_X + 320]);
#pragma nounroll
      for (int i = 0; i < 10; ++i) {
        const float t = __shfl(i & 1 ? tv[1] : tv[0], i >> 1);
        float a = 0.f;
#pragma nounroll
        for (int k = 0; k < 12; ++k) a = mix2(t, up2(lds[L_MIX + lane + 64 * k]), up2(lds[L_MIX + 768 + lane + 64 * k]), lds[L_MIX + 1536 + lane + 64 * k], a);
        a = rbf(rbf(sum64(a)) + bias);
        if (lane == (i >> 1)) { if (i & 1) y[1] = a; else y[0] = a; }
      }
    }
    const f32x2 o = hv + y;
    __builtin_amdgcn_raw_buffer_store_b32(pk2(o[0], o[1]), outr, a_out, 0, 16);
  }
  if (bid == 0 && tid == 64) __builtin_amdgcn_raw_buffer_store_b64(u32x2{(unsigned)E1, (unsigned)(E1 >> 32)}, wsr, 0, 0, 16);
}

extern "C" void kernel_launch(void* const* d_in, const int* in_sizes, int n_in, void* d_out, int out_size, void* d_ws, size_t ws_size,
                              hipStream_t stream) {
  typedef const unsigned short* P;
  fused_decode<<<256, 512, 0, stream>>>((P)d_in[0], (P)d_in[1], (P)d_in[2], (P)d_in[3], (P)d_in[4], (P)d_in[6], (P)d_in[7], (P)d_in[8], (P)d_in[9],
                                        (unsigned short*)d_out, (unsigned char*)d_ws);
}
```

```cpp
#include <hip/hip_runtime.h>
#include <stdint.h>

typedef unsigned u32x4 __attribute__((ext_vector_type(4)));
typedef unsigned u32x2 __attribute__((ext_vector_type(2)));
typedef float f32x4 __attribute__((ext_vector_type(4)));
typedef float f32x2 __attribute__((ext_vector_type(2)));
typedef __bf16 bf16x2 __attribute__((ext_vector_type(2)));
typedef __bf16 bf16x8 __attribute__((ext_vector_type(8)));
typedef __amdgpu_buffer_rsrc_t rsrc_t;
#define LAS __attribute__((address_space(3)))

#define NPRE 6
#define QDELAY 600
#define PAIR 2
#define WAITT 400
#define CK1 14
#define CK2 26
#define SW 32
#define EPS 0.0f
#define C1 215
#define C2 415
#define CKG 28
#define FORCE_FB 0
#define STAG 85
#define STK 37
#define CK3 44
#define QSLP 8
#define EPS3 -0.02f
#define XMAP 1
#define ROTX 0
#define STR2(x) #x
#define STR(x) STR2(x)

constexpr int D = 640, H = 32, SK = 4096, DH = 192, HD = H * DH, F = 1536;
constexpr unsigned SEQB = SK * DH * 2, TILEB = 8 * DH * 2, KVB = 4u * H * SEQB, WB = D * HD * 2;
constexpr unsigned P1REC = 896;
constexpr unsigned WS_TS = 4096, WS_TB = WS_TS + 4 * 64 * 128, WS_QP = WS_TB + 256 * 128, WS_P1 = WS_QP + H * 4 * 96 * 8, WS_P2 = WS_P1 + 256 * P1REC, WS_END = WS_P2 + 4 * 16 * D * 4;
constexpr unsigned OOB = 0x7ffffff0u;
constexpr unsigned SPIN_CAP = 1u << 17;
constexpr unsigned TAB0 = 100u << 7;
constexpr float L2E = 1.4426950408889634f, SCALE = 0.07216878364870322f;
constexpr int OSTR = 208;
constexpr int L_CNT = 0, L_Q = 12, L_REC = L_Q + 96, L_O = L_REC + 4 * 196, L_RED = L_O + 4 * OSTR, L_ST = L_RED + 8 * 16, L_X = L_ST + 16, L_HH = L_X + 324,
              L_XN = L_HH + 320, L_QR = L_XN + 16 * 132, L_MIX = L_QR + 4 * 256, L_WO = L_MIX + 3 * F / 2, L_Y = L_WO, L_TAB = L_WO + 2 * DH * 36, L_END = L_TAB + 4096;
constexpr int LDS_DW = 26624;
static_assert(L_END <= LDS_DW && L_REC % 4 == 0 && L_O % 4 == 0 && L_XN % 4 == 0 && L_QR % 4 == 0 && L_MIX % 4 == 0 && L_WO % 4 == 0, "lds map");

__device__ __forceinline__ float bflo(unsigned w) { return __uint_as_float(w << 16); }
__device__ __forceinline__ float bfhi(unsigned w) { return __uint_as_float(w & 0xffff0000u); }
__device__ __forceinline__ unsigned pk2(float lo, float hi) { unsigned r; asm("v_cvt_pk_bf16_f32 %0, %1, %2" : "=v"(r) : "v"(lo), "v"(hi)); return r; }
__device__ __forceinline__ float rbf(float f) { return (float)(__bf16)f; }
__device__ __forceinline__ f32x2 up2(unsigned w) { return f32x2{bflo(w), bfhi(w)}; }
__device__ __forceinline__ f32x2 rbf2(f32x2 v) { return up2(pk2(v[0], v[1])); }
__device__ __forceinline__ float dot2(unsigned a, unsigned b, float c) {
  return __builtin_amdgcn_fdot2_f32_bf16(__builtin_bit_cast(bf16x2, a), __builtin_bit_cast(bf16x2, b), c, false);
}
template <int CTRL> __device__ __forceinline__ float dpp(float x) {
  return __builtin_bit_cast(float, __builtin_amdgcn_mov_dpp(__builtin_bit_cast(int, x), CTRL, 0xf, 0xf, true));
}
constexpr int XOR1 = 0xB1, XOR2 = 0x4E, HMIR = 0x141, ROR8 = 0x128;
__device__ __forceinline__ float sw32(float a, float b) {
  const auto r = __builtin_amdgcn_permlane32_swap(__float_as_uint(a), __float_as_uint(b), false, false);
  return __uint_as_float(r[0]) + __uint_as_float(r[1]);
}
__device__ __forceinline__ float sw16(float a, float b) {
  const auto r = __builtin_amdgcn_permlane16_swap(__float_as_uint(a), __float_as_uint(b), false, false);
  return __uint_as_float(r[0]) + __uint_as_float(r[1]);
}
__device__ __forceinline__ float sum16(float v) { v += dpp<XOR1>(v); v += dpp<XOR2>(v); v += dpp<HMIR>(v); v += dpp<ROR8>(v); return v; }
__device__ __forceinline__ float sum64(float v) { v = sum16(v); v = sw16(v, v); return sw32(v, v); }
__device__ __forceinline__ float scatter8(const float* a, bool b3) {
  float y[4], z[2];
#pragma unroll
  for (int e = 0; e < 4; ++e) y[e] = sw32(a[e], a[e + 4]);
#pragma unroll
  for (int e = 0; e < 2; ++e) z[e] = sw16(y[e], y[e + 2]);
  return (b3 ? z[1] : z[0]) + dpp<ROR8>(b3 ? z[0] : z[1]);
}
typedef short s16x4 __attribute__((ext_vector_type(4)));
__device__ __forceinline__ bf16x8 tr8(LAS const unsigned* p, int step) {
  const s16x4 a = __builtin_amdgcn_ds_read_tr16_b64_v4i16((LAS s16x4*)p), b = __builtin_amdgcn_ds_read_tr16_b64_v4i16((LAS s16x4*)(p + step));
  return __builtin_bit_cast(bf16x8, __builtin_shufflevector(a, b, 0, 1, 2, 3, 4, 5, 6, 7));
}
__device__ __forceinline__ rsrc_t mk(const void* p, unsigned n) { return __builtin_amdgcn_make_buffer_rsrc((void*)p, 0, n, 0x00020000); }
__device__ __forceinline__ u32x4 ld_nt(rsrc_t r, unsigned v, unsigned s) { return __builtin_amdgcn_raw_buffer_load_b128(r, v, s, 2); }
__device__ __forceinline__ u32x4 ld_sc(rsrc_t r, unsigned v, unsigned s = 0) { return __builtin_amdgcn_raw_buffer_load_b128(r, v, s, 16); }
__device__ __forceinline__ void st_sc(rsrc_t r, unsigned v, u32x4 x) { __builtin_amdgcn_raw_buffer_store_b128(x, r, v, 0, 16); }
__device__ __forceinline__ unsigned now() { return (unsigned)__builtin_amdgcn_s_memrealtime(); }
__device__ __forceinline__ unsigned lds_peek(LAS unsigned* p) {
  return __builtin_amdgcn_readfirstlane(__hip_atomic_load(p, __ATOMIC_RELAXED, __HIP_MEMORY_SCOPE_WORKGROUP));
}
#define PIN() do { asm volatile("" ::: "memory"); __builtin_amdgcn_sched_barrier(0); } while (0)
#define WG_BAR() do { asm volatile("s_waitcnt lgkmcnt(0)" ::: "memory"); __builtin_amdgcn_s_barrier(); PIN(); } while (0)
#define STAMP(i) do { } while (0)
struct Tag { unsigned b0, b1, b2, b3, t32; };
__device__ __forceinline__ Tag make_tag(unsigned long long e) {
  const unsigned t = (unsigned)e;
  return Tag{t & 0xffu, (t + (t >> 8)) & 0xffu, (t + (t >> 16)) & 0xffu, (t + (t >> 24)) & 0xffu, t};
}
__device__ __forceinline__ u32x4 enc4(f32x4 v, const Tag& t) {
  return u32x4{(__float_as_uint(v[0]) & 0xffffff00u) | t.b0, (__float_as_uint(v[1]) & 0xffffff00u) | t.b1, (__float_as_uint(v[2]) & 0xffffff00u) | t.b2,
               (__float_as_uint(v[3]) & 0xffffff00u) | t.b3};
}
__device__ __forceinline__ unsigned bad4(u32x4 g, const Tag& t) { return ((g[0] ^ t.b0) | (g[1] ^ t.b1) | (g[2] ^ t.b2) | (g[3] ^ t.b3)) & 0xffu; }
__device__ __forceinline__ f32x4 dec4(u32x4 g) { return __builtin_bit_cast(f32x4, g & 0xffffff00u); }
__device__ __forceinline__ float dec(unsigned g) { return __uint_as_float(g & 0xffffff00u); }
__device__ __forceinline__ void helper_bar(LAS unsigned* cnt, unsigned target, int lane) {
  asm volatile("s_waitcnt lgkmcnt(0)" ::: "memory");
  if (lane == 0) __hip_atomic_fetch_add(cnt, 1u, __ATOMIC_RELAXED, __HIP_MEMORY_SCOPE_WORKGROUP);
  while (lds_peek(cnt) < target) { }
  asm volatile("" ::: "memory");
}
__device__ __forceinline__ float mix2(float t, f32x2 wa, f32x2 wb, unsigned wc, float acc) {
  constexpr float K0 = -2.f * L2E * 0.7978845608028654f, K1 = K0 * 0.044715f;
  const f32x2 u = rbf2(rbf2(wa * t) + wb);
  const f32x2 z = u * __builtin_elementwise_fma(u * u, (f32x2)K1, (f32x2)K0);
  const f32x2 d = f32x2{__builtin_amdgcn_exp2f(z[0]), __builtin_amdgcn_exp2f(z[1])} + 1.f;
  const f32x2 g = u * f32x2{__builtin_amdgcn_rcpf(d[0]), __builtin_amdgcn_rcpf(d[1])};
  return dot2(pk2(g[0], g[1]), wc, acc);
}
__device__ __forceinline__ float inv_sd(float var) { return __builtin_amdgcn_rcpf(rbf(__builtin_amdgcn_sqrtf(rbf(rbf(var) + 1e-5f)))); }

__global__ void __launch_bounds__(512, 2)
fused_decode(const unsigned short* x, const unsigned short* kc, const unsigned short* vc, const unsigned short* wq, const unsigned short* wo,
             const unsigned short* w1, const unsigned short* b1, const unsigned short* w2, const unsigned short* b2, unsigned short* out,
             unsigned char* ws) {
  __shared__ __attribute__((aligned(16))) unsigned lds_raw[LDS_DW];
  LAS unsigned* lds = (LAS unsigned*)lds_raw;
  LAS float* ldf = (LAS float*)lds_raw;
  const int tid = threadIdx.x, lane = tid & 63, w = __builtin_amdgcn_readfirstlane(tid >> 6);
  const int bid = blockIdx.x, h = XMAP ? (bid & 7) * 4 + ((bid >> 3) & 3) : bid >> 3, j = XMAP ? bid >> 5 : (bid + h) & 7, b = j >> 1, s = j & 1;
  const bool has_q = j >= 2 && s == 1, has_o = j < 2 || s == 0;
  const int hp = h >> 1, co = (h & 1) * 5 + (j < 2 ? j : b + 1);
  const int b2i = bid >> 6, cg = bid & 63;
  STAMP(0);
  const unsigned tstart = now();
  asm volatile("" :: "s"(x), "s"(kc), "s"(vc), "s"(wq), "s"(wo), "s"(w1), "s"(b1), "s"(w2), "s"(b2), "s"(out), "s"(ws));
  const rsrc_t wsr0 = mk(ws, WS_END);
  bf16x8 Bf[12];
  unsigned long long E1;

  if (__builtin_expect(w < 4, 1)) {
    __builtin_amdgcn_s_setprio(2);
    const int r = lane >> 3, c = lane & 7;
    constexpr int ntw = 256, NT = 64;
    const unsigned seq = (unsigned)(b * H + h) * SEQB;
    const int rot = __builtin_amdgcn_readfirstlane((ROTX) % ntw), y0 = s ? 512 - ntw : 0;
    const bool hi = r >> 2;
    const unsigned kb = PAIR == 1 ? (hi ? 171 : 0) : (hi ? 86 : 0), c1 = PAIR == 1 ? 171 : (hi ? 85 : 86), d1 = PAIR == 1 ? (hi ? 170 : 171) : 85,
                   c2 = PAIR == 1 ? (hi ? 342 : 341) : 171, d2 = PAIR == 1 ? 171 : (hi ? 85 : 86);
    constexpr int PB = PAIR == 1 ? 9 : 8;
    auto ty = [&](int i) -> int { const int y1 = i * 4 + w + rot; return y0 + (y1 >= ntw ? y1 - ntw : y1); };
    auto tv = [&](int i) -> unsigned {
      if (!PAIR) return (unsigned)(r * 384 + c * 16);
      const unsigned mm = ((4 * ty(i)) & ((1 << PB) - 1)) + (r & 3);
      return (mm + kb + (mm >= c1 ? d1 : 0) + (mm >= c2 ? d2 : 0)) * 384 + c * 16; };
    auto ts = [&](int i) -> unsigned {
      return __builtin_amdgcn_readfirstlane(PAIR ? seq + (unsigned)((4 * ty(i)) >> PB) * (768u << PB) : seq + ty(i) * TILEB); };
    const rsrc_t kr = mk(kc, KVB), vr = mk(vc, KVB);
    u32x4 K[NPRE][3], V[NPRE][3];
#pragma unroll
    for (int p = 0; p < NPRE; ++p) {
      if (p == 1) WG_BAR();
      if (p) asm volatile("s_waitcnt vmcnt(3)" ::: "memory");
#pragma unroll
      for (int i = 0; i < 3; ++i) K[p][i] = ld_nt(kr, tv(p) + 128 * i, ts(p));
      PIN();
      if (p) asm volatile("s_waitcnt vmcnt(3)" ::: "memory");
#pragma unroll
      for (int i = 0; i < 3; ++i) V[p][i] = ld_nt(vr, tv(p) + 128 * i, ts(p));
      PIN();
    }
    while (lds_peek(lds + L_CNT + 7) == 0) __builtin_amdgcn_s_sleep(1);
    PIN();
    STAMP(1);
    u32x4 qf[3];
#pragma unroll
    for (int i = 0; i < 3; ++i) qf[i] = *(LAS const u32x4*)(lds + L_Q + (c + 8 * i) * 4);
    float m = __uint_as_float(0xff7f0000u), l = 0.f, acc[24];
#pragma unroll
    for (int k = 0; k < 24; ++k) acc[k] = 0.f;
    unsigned pace = 0;
    bool stamp3 = false;
    auto step_fn = [&](u32x4 (&Kc)[3], u32x4 (&Vc)[3], bool refill, unsigned nbytes, unsigned av, unsigned as) {
      float a0 = 0.f, a1 = 0.f, a2 = 0.f;
#pragma unroll
      for (int k = 0; k < 4; ++k) { a0 = dot2(Kc[0][k], qf[0][k], a0); a1 = dot2(Kc[1][k], qf[1][k], a1); a2 = dot2(Kc[2][k], qf[2][k], a2); }
      float a = (a0 + a1) + a2;
      asm volatile("" : "+v"(a));
      PIN();
      if (refill) {
        if (pace) { unsigned tn; while ((int)(((tn = now()) << 6) - pace) < 0) { }
                    if (stamp3) { if (lane == 0) lds[L_CNT + 8] = tn | 1u; stamp3 = false; } }
        const rsrc_t k2 = mk(kc, nbytes);
#pragma unroll
        for (int i = 0; i < 3; ++i) Kc[i] = ld_nt(k2, av + 128 * i, as);
      }
      PIN();
      f32x2 vf[12];
#pragma unroll
      for (int k = 0; k < 12; ++k) vf[k] = up2(Vc[k >> 2][k & 3]);
      if (refill) {
        asm volatile("" : "+v"(vf[0]), "+v"(vf[1]), "+v"(vf[2]), "+v"(vf[3]), "+v"(vf[4]), "+v"(vf[5]));
        asm volatile("" : "+v"(vf[6]), "+v"(vf[7]), "+v"(vf[8]), "+v"(vf[9]), "+v"(vf[10]), "+v"(vf[11]));
        PIN();
        const rsrc_t v2 = mk(vc, nbytes);
#pragma unroll
        for (int i = 0; i < 3; ++i) Vc[i] = ld_nt(v2, av + 128 * i, as);
        PIN();
      }
      a += dpp<XOR1>(a); a += dpp<XOR2>(a); a += dpp<HMIR>(a);
      const float sc = rbf(rbf(a) * SCALE);
      const float mn = fmaxf(m, sc);
      const float alpha = __builtin_amdgcn_exp2f((m - mn) * L2E), p = __builtin_amdgcn_exp2f((sc - mn) * L2E);
      l = fmaf(l, alpha, p); m = mn;
#pragma unroll
      for (int k = 0; k < 12; ++k) { acc[2 * k] = fmaf(p, vf[k][0], acc[2 * k] * alpha); acc[2 * k + 1] = fmaf(p, vf[k][1], acc[2 * k + 1] * alpha); }
      PIN();
    };
#pragma unroll
    for (int p = 0; p < NPRE; ++p) step_fn(K[p], V[p], p == 0, KVB, tv(NPRE), ts(NPRE));
    int t = NPRE + 1;
    do {
      if (w == 1 && t == CKG && lane == 0) lds[L_CNT + 6] = 1;
      if (w == 0 && (t == CK1 || t == CK2)) { const unsigned tn = now() | 1u; if (lane == 0) lds[L_CNT + (t == CK1 ? 1 : 2)] = tn; }
      step_fn(K[0], V[0], true, KVB, tv(t), ts(t));
    } while (++t < SW);
    static_assert(NPRE < CK1 && CK1 < CK2 && CK2 < SW && NPRE < CKG && CKG < SW && SW + 2 <= CK3 && CK3 < 62 && (CK3 - SW) % 2 == 0 && (NT - SW) % 2 == 0, "every stamp the helpers wait for is reached");
    unsigned tend;
    while ((tend = lds_peek(lds + L_CNT + 4)) == 0) __builtin_amdgcn_s_sleep(1);
    E1 = (unsigned long long)lds_peek(lds + L_CNT + 11) << 32 | lds_peek(lds + L_CNT + 10);
    const int stag = (STAG * ((((bid * STK) & 63) + 64 * w) - 128) * 64) >> 8;
    unsigned tgt = now() << 6;
    int tau = (int)((tend << 6) + stag - tgt) / (NT - 1 - SW);
#pragma unroll
    for (int i = 0; i < 3; ++i) K[1][i] = ld_nt(kr, tv(t) + 128 * i, ts(t));
    PIN();
#pragma unroll
    for (int i = 0; i < 3; ++i) V[1][i] = ld_nt(vr, tv(t) + 128 * i, ts(t));
    PIN();
    do {
      const unsigned t3 = lds_peek(lds + L_CNT + 4);
      if (t3 != tend) { tend = t3; tau = (int)((tend << 6) + stag - tgt) / max(NT - 1 - t, 1); }
      tgt += tau;
      pace = tgt | 1u;
      step_fn(K[0], V[0], true, KVB, tv(t + 1), ts(t + 1));
      tgt += tau;
      pace = t + 2 < NT ? tgt | 1u : 0u;
      if (w == 0 && t + 2 == CK3) stamp3 = true;
      step_fn(K[1], V[1], true, t + 2 < NT ? KVB : 0u, tv(t + 2), ts(t + 2));
      t += 2;
    } while (t < NT);
    step_fn(K[0], V[0], false, 0, 0, 0);
    float M = fmaxf(m, dpp<ROR8>(m));
    { const auto q16 = __builtin_amdgcn_permlane16_swap(__float_as_uint(M), __float_as_uint(M), false, false);
      M = fmaxf(__uint_as_float(q16[0]), __uint_as_float(q16[1]));
      const auto q32 = __builtin_amdgcn_permlane32_swap(__float_as_uint(M), __float_as_uint(M), false, false);
      M = fmaxf(__uint_as_float(q32[0]), __uint_as_float(q32[1])); }
    const float f = __builtin_amdgcn_exp2f((m - M) * L2E);
    l *= f; l += dpp<ROR8>(l); l = sw16(l, l); l = sw32(l, l);
#pragma unroll
    for (int k = 0; k < 24; ++k) acc[k] *= f;
#pragma unroll
    for (int i = 0; i < 3; ++i) ldf[L_REC + w * 196 + i * 64 + c * 8 + r] = scatter8(acc + i * 8, lane & 8);
    if (lane == 0) { ldf[L_REC + w * 196 + 192] = M; ldf[L_REC + w * 196 + 193] = l; }
  } else {
    const int hid = tid - 256, hw = w - 4;
    u32x2 ep = __builtin_amdgcn_raw_buffer_load_b64(wsr0, 0, 0, 16);
    if (hid < 12) lds[L_CNT + hid] = 0;
    const int cq = (j - 2) >> 1, rg = hid >> 3, c8 = hid & 7;
    u32x4 W[20], X[5];
    const rsrc_t xr = mk(x, has_q ? 4 * D * 2 : 0), wqr = mk(wq, has_q ? WB : 0);
#pragma unroll
    for (int i = 0; i < 5; ++i) { const int ch = 20 * hw + (lane & 15) + 16 * i; X[i] = ld_nt(xr, (lane >> 4) * (D * 2) + (ch >= 80 ? ch - 80 : ch) * 16, 0); }
    PIN();
#pragma unroll
    for (int i = 0; i < 20; ++i) {
      W[i] = ld_nt(wqr, (unsigned)(lane >> 3) * (HD * 2) + c8 * 16, (unsigned)(160 * hw + 8 * i) * (HD * 2) + (h * DH + cq * 64) * 2);
      if ((i & 3) == 3) PIN();
    }
    const unsigned mo = hid < 192 ? hid * 16 : OOB;
    const u32x4 Ma = ld_nt(mk(w1, F * 2), mo, 0), Mb = ld_nt(mk(b1, F * 2), mo, 0), Mc = ld_nt(mk(w2, F * 2), mo, 0);
    const unsigned b2v = __builtin_amdgcn_raw_buffer_load_b16(mk(b2, 2), 0, 0, 2);
    PIN();
    WG_BAR();
    asm volatile("" : "+v"(ep));
    E1 = ((unsigned long long)__builtin_amdgcn_readfirstlane(ep[1]) << 32 | (unsigned)__builtin_amdgcn_readfirstlane(ep[0])) + 1;
    const Tag tg = make_tag(E1);
    if (hid == 0) *(LAS u32x2*)(lds + L_CNT + 10) = u32x2{(unsigned)E1, (unsigned)(E1 >> 32)};
    if (has_q) {
      float sa = 0.f, sb = 0.f;
#pragma unroll
      for (int i = 0; i < 20; i += 2) { sa = dot2(X[i >> 2][i & 3], 0x3f803f80u, sa); sb = dot2(X[i >> 2][(i & 3) + 1], 0x3f803f80u, sb); }
      const float mean = sum16(sa + sb) * (1.f / D);
      f32x2 s2 = {0.f, 0.f}, s3 = s2;
#pragma unroll
      for (int i = 0; i < 20; i += 2) {
        const f32x2 d = up2(X[i >> 2][i & 3]) - mean, e = up2(X[i >> 2][(i & 3) + 1]) - mean;
        s2 = __builtin_elementwise_fma(d, d, s2); s3 = __builtin_elementwise_fma(e, e, s3);
      }
      s2 += s3;
      const float inv = inv_sd(sum16(s2[0] + s2[1]) * (1.f / D)), mb = rbf(mean);
      LAS unsigned* xn = lds + L_XN + hw * 4 * 132;
#pragma unroll
      for (int i = 0; i < 2; ++i) {
        u32x4 o;
#pragma unroll
        for (int k = 0; k < 4; ++k) { const f32x2 y = rbf2(up2(X[i][k]) - mb) * inv; o[k] = pk2(y[0], y[1]); }
        *(LAS u32x4*)(xn + (lane >> 4) * 132 + ((lane & 15) + 16 * i) * 4) = o;
      }
      asm volatile("" ::: "memory");
      u32x4 af[5];
#pragma unroll
      for (int ks = 0; ks < 5; ++ks) af[ks] = *(LAS const u32x4*)(xn + (lane & 3) * 132 + ks * 16 + (lane >> 4) * 4);
      LAS unsigned* st = lds + L_WO + hw * 64 * 36;
      f32x4 dq[4];
#pragma unroll
      for (int nt = 0; nt < 4; ++nt) dq[nt] = f32x4{0.f, 0.f, 0.f, 0.f};
#pragma unroll
      for (int ks = 0; ks < 5; ++ks) {
        LAS unsigned* sk = st + (ks & 1) * 32 * 36;
#pragma unroll
        for (int i = 0; i < 4; ++i) *(LAS u32x4*)(sk + (8 * i + (lane >> 3)) * 36 + c8 * 4) = W[4 * ks + i];
        asm volatile("" ::: "memory");
        LAS const unsigned* rd = sk + (8 * (lane >> 4) + ((lane >> 2) & 3)) * 36 + (lane & 3) * 2;
#pragma unroll
        for (int nt = 0; nt < 4; ++nt) dq[nt] = __builtin_amdgcn_mfma_f32_16x16x32_bf16(__builtin_bit_cast(bf16x8, af[ks]), tr8(rd + nt * 8, 4 * 36), dq[nt], 0, 0, 0);
        asm volatile("" ::: "memory");
      }
      const bool g1 = lane & 16, g2 = lane & 32;
#pragma unroll
      for (int nt = 0; nt < 4; ++nt) {
        const float a01 = g1 ? dq[nt][1] : dq[nt][0], a23 = g1 ? dq[nt][3] : dq[nt][2];
        ldf[L_QR + hw * 256 + (lane >> 4) * 64 + nt * 16 + (lane & 15)] = g2 ? a23 : a01;
      }
      helper_bar(lds + L_CNT, 4, lane);
      if (hw == 0) {
        f32x4 a = *(LAS const f32x4*)(ldf + L_QR + 4 * lane);
#pragma unroll
        for (int ww = 1; ww < 4; ++ww) a += *(LAS const f32x4*)(ldf + L_QR + ww * 256 + 4 * lane);
        st_sc(wsr0, WS_QP + ((h * 4 + (lane >> 4)) * 96 + cq * 32 + (lane & 15) * 2) * 8, u32x4{pk2(a[0], a[1]), tg.t32, pk2(a[2], a[3]), tg.t32});
      }
    }
    STAMP(3);
    if (hw == 3) {
      if (QDELAY) while (now() - tstart < QDELAY) __builtin_amdgcn_s_sleep(2);
      const unsigned o = lane < 48 ? WS_QP + (h * 4 + b) * 96 * 8 + lane * 16 : OOB, vm = lane < 48 ? ~0u : 0u;
      u32x4 g;
#pragma nounroll
      for (unsigned spin = 0; spin < SPIN_CAP; ++spin) {
        asm volatile("" ::: "memory");
        g = ld_sc(wsr0, o);
        if (__all((((g[1] ^ tg.t32) | (g[3] ^ tg.t32)) & vm) == 0)) break;
        __builtin_amdgcn_s_sleep(QSLP);
      }
      if (lane < 48) *(LAS u32x2*)(lds + L_Q + 2 * lane) = u32x2{g[0], g[2]};
      asm volatile("s_waitcnt lgkmcnt(0)" ::: "memory");
      if (lane == 0) lds[L_CNT + 7] = 1;
      STAMP(1);
    } else {
      *(LAS u32x4*)(lds + L_MIX + hid * 4) = Ma; *(LAS u32x4*)(lds + L_MIX + 768 + hid * 4) = Mb; *(LAS u32x4*)(lds + L_MIX + 1536 + hid * 4) = Mc;
      const unsigned bits0 = (TAB0 + ((bid * 32) & 4095)) | (bid >> 7) << 15;
#pragma nounroll
      for (int i0 = 0; i0 < 32; i0 += 8) {
        float ac[8];
#pragma unroll
        for (int i = 0; i < 8; ++i) {
          const float t = __uint_as_float((bits0 + i0 + i) << 16);
          ac[i] = 0.f;
#pragma unroll
          for (int k = 0; k < 4; ++k) ac[i] = mix2(t, up2(Ma[k]), up2(Mb[k]), Mc[k], ac[i]);
        }
        float y = scatter8(ac, lane & 8);
        y += dpp<XOR1>(y); y += dpp<XOR2>(y); y += dpp<HMIR>(y);
        if ((lane & 7) == 0) ldf[L_RED + hw * 32 + i0 + (lane >> 3)] = y;
      }
      helper_bar(lds + L_CNT + 3, 3, lane);
      if (hw == 2 && lane < 8) {
        f32x4 y = *(LAS const f32x4*)(ldf + L_RED + 4 * lane);
#pragma unroll
        for (int ww = 1; ww < 3; ++ww) y += *(LAS const f32x4*)(ldf + L_RED + ww * 32 + 4 * lane);
        const f32x2 y0 = rbf2(f32x2{y[0], y[1]}) + bflo(b2v), y1 = rbf2(f32x2{y[2], y[3]}) + bflo(b2v);
        st_sc(wsr0, WS_TB + bid * 128 + lane * 16, u32x4{pk2(y0[0], y0[1]), tg.t32, pk2(y1[0], y1[1]), tg.t32});
      }
    }
    const rsrc_t wor = mk(wo, has_o ? WB : 0);
    u32x4 Wo[12];
#pragma unroll
    for (int i = 0; i < 12; ++i) Wo[i] = ld_nt(wor, (unsigned)rg * (D * 2) + c8 * 16, (unsigned)(hp * 2 * DH + 32 * i) * (D * 2) + co * 128);
    if (hid < 80) *(LAS u32x4*)(lds + L_X + hid * 4) = __builtin_nontemporal_load((const u32x4*)(x + b2i * D + hid * 8));
    if (hid == 80) lds[L_X + 320] = b2v;
#pragma unroll
    for (int i = 0; i < 12; ++i) *(LAS u32x4*)(lds + L_WO + (rg + 32 * i) * 36 + c8 * 4) = Wo[i];
    helper_bar(lds + L_CNT, has_q ? 8 : 4, lane);
    LAS const unsigned* wl = lds + L_WO + (8 * (lane >> 4) + ((lane >> 2) & 3)) * 36 + hw * 8 + (lane & 3) * 2;
#pragma unroll
    for (int ks = 0; ks < 12; ++ks) Bf[ks] = tr8(wl + ks * 32 * 36, 4 * 36);
    if (hw < 2) {
      while (lds_peek(lds + L_CNT + 6) == 0) __builtin_amdgcn_s_sleep(16);
#pragma nounroll
      for (int half = 0; half < 2; ++half) {
        u32x4 g[8];
#pragma nounroll
        for (unsigned spin = 0; spin < SPIN_CAP; ++spin) {
          asm volatile("" ::: "memory");
          unsigned bad = 0;
#pragma unroll
          for (int i = 0; i < 8; ++i) g[i] = ld_sc(wsr0, WS_TB + hid * 16, (half * 8 + i) * 2048);
#pragma unroll
          for (int i = 0; i < 8; ++i) bad |= (g[i][1] ^ tg.t32) | (g[i][3] ^ tg.t32);
          if (__all(bad == 0)) break;
          __builtin_amdgcn_s_sleep(32);
        }
#pragma unroll
        for (int i = 0; i < 8; ++i) *(LAS u32x2*)(lds + L_TAB + ((half * 8 + i) * 128 + hid) * 2) = u32x2{g[i][0], g[i][2]};
      }
    }
    if (hw == 2) {
      unsigned tm0 = 0;
#pragma nounroll
      for (int ck = 0; ck < 3; ++ck) {
        unsigned t2;
        while ((t2 = lds_peek(lds + L_CNT + (ck < 2 ? 1 + ck : 8))) == 0) __builtin_amdgcn_s_sleep(8);
        if ((XMAP ? (h & 3) == (j & 3) : (bid & 3) == ((h >> 1) & 3)) && lane == 0)
          __builtin_amdgcn_raw_buffer_store_b64(u32x2{t2, tg.t32}, wsr0, WS_TS + (ck * 64 + (XMAP ? (bid & 7) * 8 + j : bid >> 2)) * 128, 0, 16);
        while (now() - t2 < WAITT) __builtin_amdgcn_s_sleep(8);
        const u32x2 g = __builtin_amdgcn_raw_buffer_load_b64(wsr0, WS_TS + (ck * 64 + lane) * 128, 0, 16);
        const unsigned tm = t2 + (int)(sum64(g[1] == tg.t32 ? (float)min(max((int)(g[0] - t2), -2000), 2000) : (float)(WAITT + 30)) * (1.f / 64));
        const float left = ck == 1 ? (63.f - CK2) / (CK2 - CK1) * (1.f - EPS) : (63.f - CK3) / (CK3 - CK2) * (1.f - EPS3);
        if (ck && lane == 0) lds[L_CNT + 4] = (tm + (int)((float)(int)(tm - tm0) * left)) | 1u;
        tm0 = tm;
      }
    }
  }
  const Tag tg = make_tag(E1);
  unsigned a_p1 = lane <= 48 ? WS_P1 + (h * 8 + j) * P1REC + lane * 16 : OOB;
  unsigned a_p2 = (lane & 12) ? OOB : WS_P2 + (((lane & 3) * 16 + hp) * D + co * 64 + (w - 4) * 16 + (lane >> 4) * 4) * 4;
  LAS const float* a_rec = ldf + L_REC + 4 * (lane < 48 ? lane : 48);
  LAS const unsigned* a_o = lds + L_O + (lane & 3) * OSTR + (lane >> 4) * 4;
  LAS const unsigned* own = lds + L_HH + cg * 5 + (lane < 5 ? lane : 0);
  unsigned a_out = lane < 5 ? (b2i * D + cg * 10 + 2 * lane) * 2 : OOB;
  asm volatile("" : "+v"(a_p1), "+v"(a_p2), "+v"(a_rec), "+v"(a_o), "+v"(own), "+v"(a_out));
  const rsrc_t wsr = wsr0;
  WG_BAR();
  STAMP(2);
  if (w == 4) {
    float mm[4];
    f32x4 rv[4];
#pragma unroll
    for (int i = 0; i < 4; ++i) { mm[i] = ldf[L_REC + i * 196 + 192]; rv[i] = *(LAS const f32x4*)(a_rec + i * 196); }
    __builtin_amdgcn_sched_barrier(0);
    const float M = fmaxf(fmaxf(mm[0], mm[1]), fmaxf(mm[2], mm[3]));
    f32x4 a = {0.f, 0.f, 0.f, 0.f};
#pragma unroll
    for (int i = 0; i < 4; ++i) a += __builtin_amdgcn_exp2f((mm[i] - M) * L2E) * rv[i];
    if (lane >= 48) a = f32x4{M, a[1], 0.f, 0.f};
    st_sc(wsr, a_p1, enc4(a, tg));
    STAMP(1);
  }
  const unsigned tcom = lds_peek(lds + L_CNT + 4);
  if (has_o) {
    {
      const unsigned oa = lane <= 48 ? WS_P1 + (hp * 16 + (w >> 2) * 8 + 2 * (w & 3)) * P1REC + lane * 16 : OOB;
      const unsigned vm = lane <= 48 ? ~0u : 0u;
      LAS unsigned* da = lane < 48 ? lds + L_O + (w & 3) * OSTR + (w >> 2) * 96 + 2 * lane : lds + L_QR + 2 * lane;
      asm volatile("" : "+v"(da));
      u32x4 ga, gb;
      if (C1) while ((int)(now() - tcom) < C1) { }
#pragma nounroll
      for (unsigned spin = 0; spin < SPIN_CAP; ++spin) {
        asm volatile("" ::: "memory");
        ga = ld_sc(wsr, oa); gb = ld_sc(wsr, oa, P1REC);
        if (__all(((bad4(ga, tg) | bad4(gb, tg)) & vm) == 0)) break;
        __builtin_amdgcn_s_sleep(1);
      }
      STAMP(4);
      const float m0 = dec(__builtin_amdgcn_readlane(ga[0], 48)), m1 = dec(__builtin_amdgcn_readlane(gb[0], 48)), M = fmaxf(m0, m1);
      const float l0 = dec(__builtin_amdgcn_readlane(ga[1], 48)), l1 = dec(__builtin_amdgcn_readlane(gb[1], 48));
      float w0 = __builtin_amdgcn_exp2f((m0 - M) * L2E), w1_ = __builtin_amdgcn_exp2f((m1 - M) * L2E);
      const float inv = __builtin_amdgcn_rcpf(fmaf(w0, l0, w1_ * l1));
      w0 *= inv; w1_ *= inv;
      const f32x4 o = __builtin_elementwise_fma(dec4(ga), (f32x4)w0, dec4(gb) * w1_);
      *(LAS u32x2*)da = u32x2{pk2(o[0], o[1]), pk2(o[2], o[3])};
    }
    WG_BAR();
    if (w >= 4) {
      u32x4 af[12];
#pragma unroll
      for (int ks = 0; ks < 12; ++ks) af[ks] = *(LAS const u32x4*)(a_o + ks * 16);
      __builtin_amdgcn_sched_barrier(0);
      f32x4 d0 = {0.f, 0.f, 0.f, 0.f}, d1 = d0;
#pragma unroll
      for (int ks = 0; ks < 12; ks += 2) {
        d0 = __builtin_amdgcn_mfma_f32_16x16x32_bf16(Bf[ks], __builtin_bit_cast(bf16x8, af[ks]), d0, 0, 0, 0);
        d1 = __builtin_amdgcn_mfma_f32_16x16x32_bf16(Bf[ks + 1], __builtin_bit_cast(bf16x8, af[ks + 1]), d1, 0, 0, 0);
      }
      st_sc(wsr, a_p2, enc4(d0 + d1, tg));
      if (w != 6) STAMP(3);
    }
  }
  if (w < 5) {
    const unsigned o = WS_P2 + (b2i * 16 + (lane >> 5) * 8) * D * 4 + (w * 32 + (lane & 31)) * 16;
    const int pr = (w * 32 + (lane & 31)) * 2 + (lane >> 5);
    f32x2 xr = up2(lds[L_X + pr]);
    LAS unsigned* dh = lds + L_HH + pr;
    asm volatile("" : "+v"(xr), "+v"(dh));
    u32x4 g[8];
    if (C2) while ((int)(now() - tcom) < C2) { }
#pragma nounroll
    for (unsigned spin = 0; spin < SPIN_CAP; ++spin) {
      asm volatile("" ::: "memory");
      unsigned bad = 0;
#pragma unroll
      for (int i = 0; i < 8; ++i) g[i] = ld_sc(wsr, o, i * D * 4);
#pragma unroll
      for (int i = 0; i < 8; ++i) bad |= bad4(g[i], tg);
      if (__all(bad == 0)) break;
      __builtin_amdgcn_s_sleep(1);
    }
    STAMP(5);
    const f32x4 y = ((dec4(g[0]) + dec4(g[1])) + (dec4(g[2]) + dec4(g[3]))) + ((dec4(g[4]) + dec4(g[5])) + (dec4(g[6]) + dec4(g[7])));
    const f32x2 hs = xr + rbf2(f32x2{sw32(y[0], y[2]), sw32(y[1], y[3])});
    const unsigned hp2 = pk2(hs[0], hs[1]);
    *dh = hp2;
    const f32x2 hv = up2(hp2);
    const float piv = __uint_as_float(__builtin_amdgcn_readfirstlane(__float_as_uint(hv[0])));
    const f32x2 d = hv - piv, dd = d * d;
    const float sp = sw32(d[0] + d[1], dd[0] + dd[1]), sq = sum16(sw16(sp, sp));
    const float s1 = __uint_as_float(__builtin_amdgcn_readlane(__float_as_uint(sq), 0)), s2 = __uint_as_float(__builtin_amdgcn_readlane(__float_as_uint(sq), 32));
    *(LAS f32x2*)(ldf + L_ST + 2 * w) = f32x2{fmaf(s1, 1.f / 128, piv), fmaf(-s1, s1 * (1.f / 128), s2)};
  }
  WG_BAR();
  if (w == 0) {
    const f32x4 sa = *(LAS const f32x4*)(ldf + L_ST), sb = *(LAS const f32x4*)(ldf + L_ST + 4);
    const f32x2 sc = *(LAS const f32x2*)(ldf + L_ST + 8);
    const unsigned hw2 = *own;
    __builtin_amdgcn_sched_barrier(0);
    const f32x2 hv = up2(hw2);
    const float mean = (((sa[0] + sa[2]) + (sb[0] + sb[2])) + sc[0]) * 0.2f;
    const float e0 = sa[0] - mean, e1 = sa[2] - mean, e2 = sb[0] - mean, e3 = sb[2] - mean, e4 = sc[0] - mean;
    const float var = fmaf(fmaf(e0, e0, e1 * e1) + fmaf(e2, e2, e3 * e3) + e4 * e4, 0.2f, (((sa[1] + sa[3]) + (sb[1] + sb[3])) + sc[1]) * (1.f / D));
    const float inv = inv_sd(var), mb = rbf(mean);
    const float inv2 = inv_sd(var * inv * inv), mb2 = rbf((mean - mb) * inv);
    const f32x2 tv = rbf2(rbf2(rbf2(rbf2(hv - mb) * inv) - mb2) * inv2);
    STAMP(6);
    const unsigned tb = pk2(tv[0], tv[1]), i0 = (tb & 0x7fffu) - TAB0, i1 = ((tb >> 16) & 0x7fffu) - TAB0;
    const rsrc_t outr = mk(out, 4 * D * 2);
    LAS const unsigned short* tab = (LAS const unsigned short*)(lds + L_TAB);
    f32x2 y = {bflo(tab[(i0 & 4095u) + ((tb >> 3) & 4096u)]), bflo(tab[(i1 & 4095u) + ((tb >> 19) & 4096u)])};
    if (!__all((i0 | i1) < 4096u) || FORCE_FB) {
      const float bias = bflo(lds[L_X + 320]);
#pragma nounroll
      for (int i = 0; i < 10; ++i) {
        const float t = __shfl(i & 1 ? tv[1] : tv[0], i >> 1);
        float a = 0.f;
#pragma nounroll
        for (int k = 0; k < 12; ++k) a = mix2(t, up2(lds[L_MIX + lane + 64 * k]), up2(lds[L_MIX + 768 + lane + 64 * k]), lds[L_MIX + 1536 + lane + 64 * k], a);
        a = rbf(rbf(sum64(a)) + bias);
        if (lane == (i >> 1)) { if (i & 1) y[1] = a; else y[0] = a; }
      }
    }
    const f32x2 o = hv + y;
    __builtin_amdgcn_raw_buffer_store_b32(pk2(o[0], o[1]), outr, a_out, 0, 16);
  }
  if (bid == 0 && tid == 64) __builtin_amdgcn_raw_buffer_store_b64(u32x2{(unsigned)E1, (unsigned)(E1 >> 32)}, wsr, 0, 0, 16);
}

extern "C" void kernel_launch(void* const* d_in, const int* in_sizes, int n_in, void* d_out, int out_size, void* d_ws, size_t ws_size,
                              hipStream_t stream) {
  typedef const unsigned short* P;
  fused_decode<<<256, 512, 0, stream>>>((P)d_in[0], (P)d_in[1], (P)d_in[2], (P)d_in[3], (P)d_in[4], (P)d_in[6], (P)d_in[7], (P)d_in[8], (P)d_in[9],
                                        (unsigned short*)d_out, (unsigned char*)d_ws);
}
```

```cpp
#include <hip/hip_runtime.h>
#include <stdint.h>

typedef unsigned u32x4 __attribute__((ext_vector_type(4)));
typedef unsigned u32x2 __attribute__((ext_vector_type(2)));
typedef float f32x4 __attribute__((ext_vector_type(4)));
typedef float f32x2 __attribute__((ext_vector_type(2)));
typedef short s16x4 __attribute__((ext_vector_type(4)));
typedef __bf16 bf16x2 __attribute__((ext_vector_type(2)));
typedef __bf16 bf16x8 __attribute__((ext_vector_type(8)));
typedef __amdgpu_buffer_rsrc_t rsrc_t;
#define LAS __attribute__((address_space(3)))

constexpr int D = 640, H = 32, SK = 4096, DH = 192, HD = H * DH, F = 1536;
constexpr unsigned SEQB = SK * DH * 2, KVB = 4u * H * SEQB, WB = D * HD * 2;
constexpr float L2E = 1.4426950408889634f, SCALE = 0.07216878364870322f;

constexpr int NT = 64;
constexpr int NPRE = 7;
constexpr int CK1 = 16, CK2 = 28, CK3 = 46;
constexpr int SW = 34;
constexpr int CKG = 30;
constexpr float EPS2 = 0.007f, EPS3 = -0.02f;
constexpr unsigned WAITT = 400;
constexpr int STAG = 85;
constexpr unsigned QDELAY = 600;
constexpr int C1 = 195, C2 = 392;
static_assert(NPRE < CK1 && CK1 < CK2 && CK2 < SW && NPRE < CKG && CKG < SW && SW + 2 <= CK3 && CK3 < NT - 2 && (CK3 - SW) % 2 == 0 && (NT - SW) % 2 == 0,
              "every stamp that a helper wave waits for is reached");

constexpr unsigned P1REC = 896;
constexpr unsigned WS_TS = 4096, WS_TB = WS_TS + 3 * 64 * 128, WS_QP = WS_TB + 256 * 128, WS_P1 = WS_QP + H * 4 * 96 * 8, WS_P2 = WS_P1 + 256 * P1REC,
                   WS_END = WS_P2 + 4 * 16 * D * 4;
constexpr unsigned OOB = 0x7ffffff0u;
constexpr unsigned SPIN_CAP = 1u << 17;
constexpr unsigned TAB0 = 100u << 7;

constexpr int OSTR = 208;
constexpr int S_BAR = 0, S_T1 = 1, S_T2 = 2, S_BAR3 = 3, S_END = 4, S_T3 = 5, S_TAB = 6, S_Q = 7, S_TAG = 8;
constexpr int L_CNT = 0, L_Q = 12, L_REC = L_Q + 96, L_O = L_REC + 4 * 196, L_RED = L_O + 4 * OSTR, L_ST = L_RED + 96, L_X = L_ST + 16, L_HH = L_X + 324,
              L_XN = L_HH + 320, L_QR = L_XN + 16 * 132, L_MIX = L_QR + 4 * 256, L_WO = L_MIX + 3 * F / 2, L_TAB = L_WO + 2 * DH * 36, L_END = L_TAB + 4096;
static_assert(L_END * 4 > 80 * 1024 && L_REC % 4 == 0 && L_O % 4 == 0 && L_RED % 4 == 0 && L_ST % 4 == 0 && L_X % 4 == 0 && L_XN % 4 == 0 && L_QR % 4 == 0 &&
              L_MIX % 4 == 0 && L_WO % 4 == 0 && L_TAB % 2 == 0, "aligned regions, one workgroup per CU");

__device__ __forceinline__ float bflo(unsigned w) { return __uint_as_float(w << 16); }
__device__ __forceinline__ float bfhi(unsigned w) { return __uint_as_float(w & 0xffff0000u); }
__device__ __forceinline__ unsigned pk2(float lo, float hi) { unsigned r; asm("v_cvt_pk_bf16_f32 %0, %1, %2" : "=v"(r) : "v"(lo), "v"(hi)); return r; }
__device__ __forceinline__ float rbf(float f) { return (float)(__bf16)f; }
__device__ __forceinline__ f32x2 up2(unsigned w) { return f32x2{bflo(w), bfhi(w)}; }
__device__ __forceinline__ f32x2 rbf2(f32x2 v) { return up2(pk2(v[0], v[1])); }
__device__ __forceinline__ float dot2(unsigned a, unsigned b, float c) {
  return __builtin_amdgcn_fdot2_f32_bf16(__builtin_bit_cast(bf16x2, a), __builtin_bit_cast(bf16x2, b), c, false);
}
template <int CTRL> __device__ __forceinline__ float dpp(float x) {
  return __builtin_bit_cast(float, __builtin_amdgcn_mov_dpp(__builtin_bit_cast(int, x), CTRL, 0xf, 0xf, true));
}
constexpr int XOR1 = 0xB1, XOR2 = 0x4E, HMIR = 0x141, ROR8 = 0x128;
__device__ __forceinline__ float sw32(float a, float b) {
  const auto r = __builtin_amdgcn_permlane32_swap(__float_as_uint(a), __float_as_uint(b), false, false);
  return __uint_as_float(r[0]) + __uint_as_float(r[1]);
}
__device__ __forceinline__ float sw16(float a, float b) {
  const auto r = __builtin_amdgcn_permlane16_swap(__float_as_uint(a), __float_as_uint(b), false, false);
  return __uint_as_float(r[0]) + __uint_as_float(r[1]);
}
__device__ __forceinline__ float sum16(float v) { v += dpp<XOR1>(v); v += dpp<XOR2>(v); v += dpp<HMIR>(v); v += dpp<ROR8>(v); return v; }
__device__ __forceinline__ float sum64(float v) { v = sum16(v); v = sw16(v, v); return sw32(v, v); }
__device__ __forceinline__ float scatter8(const float* a, bool b3) {
  float y[4], z[2];
#pragma unroll
  for (int e = 0; e < 4; ++e) y[e] = sw32(a[e], a[e + 4]);
#pragma unroll
  for (int e = 0; e < 2; ++e) z[e] = sw16(y[e], y[e + 2]);
  return (b3 ? z[1] : z[0]) + dpp<ROR8>(b3 ? z[0] : z[1]);
}
__device__ __forceinline__ bf16x8 tr8(LAS const unsigned* p, int step) {
  const s16x4 a = __builtin_amdgcn_ds_read_tr16_b64_v4i16((LAS s16x4*)p), b = __builtin_amdgcn_ds_read_tr16_b64_v4i16((LAS s16x4*)(p + step));
  return __builtin_bit_cast(bf16x8, __builtin_shufflevector(a, b, 0, 1, 2, 3, 4, 5, 6, 7));
}
__device__ __forceinline__ rsrc_t mk(const void* p, unsigned n) { return __builtin_amdgcn_make_buffer_rsrc((void*)p, 0, n, 0x00020000); }
__device__ __forceinline__ u32x4 ld_nt(rsrc_t r, unsigned v, unsigned s) { return __builtin_amdgcn_raw_buffer_load_b128(r, v, s, 2); }
__device__ __forceinline__ u32x4 ld_sc(rsrc_t r, unsigned v, unsigned s = 0) { return __builtin_amdgcn_raw_buffer_load_b128(r, v, s, 16); }
__device__ __forceinline__ void st_sc(rsrc_t r, unsigned v, u32x4 x) { __builtin_amdgcn_raw_buffer_store_b128(x, r, v, 0, 16); }
__device__ __forceinline__ unsigned now() { return (unsigned)__builtin_amdgcn_s_memrealtime(); }
__device__ __forceinline__ unsigned lds_peek(LAS unsigned* p) {
  return __builtin_amdgcn_readfirstlane(__hip_atomic_load(p, __ATOMIC_RELAXED, __HIP_MEMORY_SCOPE_WORKGROUP));
}
#define PIN() do { asm volatile("" ::: "memory"); __builtin_amdgcn_sched_barrier(0); } while (0)
#define WG_BAR() do { asm volatile("s_waitcnt lgkmcnt(0)" ::: "memory"); __builtin_amdgcn_s_barrier(); PIN(); } while (0)
__device__ __forceinline__ void helper_bar(LAS unsigned* cnt, unsigned target, int lane) {
  asm volatile("s_waitcnt lgkmcnt(0)" ::: "memory");
  if (lane == 0) __hip_atomic_fetch_add(cnt, 1u, __ATOMIC_RELAXED, __HIP_MEMORY_SCOPE_WORKGROUP);
  while (lds_peek(cnt) < target) { }
  asm volatile("" ::: "memory");
}

struct Tag { unsigned b0, b1, b2, b3, t32; };
__device__ __forceinline__ Tag make_tag(unsigned t) { return Tag{t & 0xffu, (t + (t >> 8)) & 0xffu, (t + (t >> 16)) & 0xffu, (t + (t >> 24)) & 0xffu, t}; }
__device__ __forceinline__ u32x4 enc4(f32x4 v, const Tag& t) {
  return u32x4{(__float_as_uint(v[0]) & 0xffffff00u) | t.b0, (__float_as_uint(v[1]) & 0xffffff00u) | t.b1, (__float_as_uint(v[2]) & 0xffffff00u) | t.b2,
               (__float_as_uint(v[3]) & 0xffffff00u) | t.b3};
}
__device__ __forceinline__ u32x4 untag(u32x4 g, const Tag& t) { return g ^ u32x4{t.b0, t.b1, t.b2, t.b3}; }
__device__ __forceinline__ unsigned any4(u32x4 x) { return (x[0] | x[1]) | (x[2] | x[3]); }
__device__ __forceinline__ f32x4 asf4(u32x4 x) { return __builtin_bit_cast(f32x4, x); }

__device__ __forceinline__ float mix2(float t, f32x2 wa, f32x2 wb, unsigned wc, float acc) {
  constexpr float K0 = -2.f * L2E * 0.7978845608028654f, K1 = K0 * 0.044715f;
  const f32x2 u = rbf2(rbf2(wa * t) + wb);
  const f32x2 z = u * __builtin_elementwise_fma(u * u, (f32x2)K1, (f32x2)K0);
  const f32x2 d = f32x2{__builtin_amdgcn_exp2f(z[0]), __builtin_amdgcn_exp2f(z[1])} + 1.f;
  const f32x2 g = u * f32x2{__builtin_amdgcn_rcpf(d[0]), __builtin_amdgcn_rcpf(d[1])};
  return dot2(pk2(g[0], g[1]), wc, acc);
}
__device__ __forceinline__ float inv_sd(float var) { return __builtin_amdgcn_rcpf(rbf(__builtin_amdgcn_sqrtf(rbf(rbf(var) + 1e-5f)))); }

__global__ void __launch_bounds__(512, 2)
fused_decode(const unsigned short* x, const unsigned short* kc, const unsigned short* vc, const unsigned short* wq, const unsigned short* wo,
             const unsigned short* w1, const unsigned short* b1, const unsigned short* w2, const unsigned short* b2, unsigned short* out,
             unsigned char* ws) {
  __shared__ __attribute__((aligned(16))) unsigned lds_raw[L_END];
  LAS unsigned* lds = (LAS unsigned*)lds_raw;
  LAS float* ldf = (LAS float*)lds_raw;
  const int tid = threadIdx.x, lane = tid & 63, w = __builtin_amdgcn_readfirstlane(tid >> 6);
  const int bid = blockIdx.x, h = ((bid >> 1) & 3) * 8 + ((bid >> 3) & 7), j = (bid >> 6) * 2 + ((bid ^ h) & 1), b = j >> 1, s = j & 1;
  const bool has_q = j >= 2 && s == 1, has_o = j < 2 || s == 0;
  const int hp = h >> 1, co = (h & 1) * 5 + (j < 2 ? j : b + 1);
  const int b2i = bid >> 6, cg = bid & 63;
  const unsigned tstart = now();
  asm volatile("" :: "s"(x), "s"(kc), "s"(vc), "s"(wq), "s"(wo), "s"(w1), "s"(b1), "s"(w2), "s"(b2), "s"(out), "s"(ws));
  const rsrc_t wsr = mk(ws, WS_END);
  bf16x8 Bf[12];
  unsigned a_p1, a_p2, tagw;
  LAS const float* a_rec;
  LAS const unsigned* a_o;

  if (__builtin_expect(w < 4, 1)) {
    __builtin_amdgcn_s_setprio(2);
    const int r = lane >> 3, c = lane & 7;
    const bool hi = r >> 2;
    const unsigned kb = hi ? 86 : 0, c1 = hi ? 85 : 86, d2 = hi ? 85 : 86;
    const unsigned seq = (unsigned)(b * H + h) * SEQB + s * (SEQB / 2);
    auto tv = [&](int i) -> unsigned {
      const unsigned mm = ((16 * i + 4 * w) & 255) + (r & 3);
      return (mm + kb + (mm >= c1 ? 85 : 0) + (mm >= 171 ? d2 : 0)) * 384 + c * 16; };
    auto ts = [&](int i) -> unsigned { return seq + (unsigned)(i >> 4) * (512 * 384); };
    const rsrc_t kr = mk(kc, KVB), vr = mk(vc, KVB);
    u32x4 K[NPRE][3], V[NPRE][3];
#pragma unroll
    for (int p = 0; p < NPRE; ++p) {
      if (p == 1) WG_BAR();
      if (p) asm volatile("s_waitcnt vmcnt(3)" ::: "memory");
#pragma unroll
      for (int i = 0; i < 3; ++i) K[p][i] = ld_nt(kr, tv(p) + 128 * i, ts(p));
      PIN();
      if (p) asm volatile("s_waitcnt vmcnt(3)" ::: "memory");
#pragma unroll
      for (int i = 0; i < 3; ++i) V[p][i] = ld_nt(vr, tv(p) + 128 * i, ts(p));
      PIN();
    }
    while (lds_peek(lds + L_CNT + S_Q) == 0) __builtin_amdgcn_s_sleep(1);
    PIN();
    u32x4 qf[3];
#pragma unroll
    for (int i = 0; i < 3; ++i) qf[i] = *(LAS const u32x4*)(lds + L_Q + (c + 8 * i) * 4);
    float m = __uint_as_float(0xff7f0000u), l = 0.f;
    f32x2 acc[12];
#pragma unroll
    for (int k = 0; k < 12; ++k) acc[k] = f32x2{0.f, 0.f};
    unsigned pace = 0;
    bool stamp3 = false;
    auto step_fn = [&](u32x4 (&Kc)[3], u32x4 (&Vc)[3], bool refill, unsigned nbytes, unsigned av, unsigned as, bool last = false) {
      float a0 = 0.f, a1 = 0.f, a2 = 0.f;
#pragma unroll
      for (int k = 0; k < 4; ++k) { a0 = dot2(Kc[0][k], qf[0][k], a0); a1 = dot2(Kc[1][k], qf[1][k], a1); a2 = dot2(Kc[2][k], qf[2][k], a2); }
      float a = (a0 + a1) + a2;
      asm volatile("" : "+v"(a));
      PIN();
      if (refill) {
        if (pace) {
          asm volatile("s_waitcnt vmcnt(4)" ::: "memory");
          unsigned tn;
          while ((int)(((tn = now()) << 6) - pace) < 0) { }
          if (stamp3) { if (lane == 0) lds[L_CNT + S_T3] = tn | 1u; stamp3 = false; }
        }
        const rsrc_t k2 = mk(kc, nbytes);
#pragma unroll
        for (int i = 0; i < 3; ++i) Kc[i] = ld_nt(k2, av + 128 * i, as);
      }
      PIN();
      f32x2 vf[12];
#pragma unroll
      for (int k = 0; k < 12; ++k) vf[k] = up2(Vc[k >> 2][k & 3]);
      if (refill) {
        asm volatile("" : "+v"(vf[0]), "+v"(vf[1]), "+v"(vf[2]), "+v"(vf[3]), "+v"(vf[4]), "+v"(vf[5]));
        asm volatile("" : "+v"(vf[6]), "+v"(vf[7]), "+v"(vf[8]), "+v"(vf[9]), "+v"(vf[10]), "+v"(vf[11]));
        PIN();
        const rsrc_t v2 = mk(vc, nbytes);
#pragma unroll
        for (int i = 0; i < 3; ++i) Vc[i] = ld_nt(v2, av + 128 * i, as);
        PIN();
      }
      a += dpp<XOR1>(a); a += dpp<XOR2>(a); a += dpp<HMIR>(a);
      const float sc = rbf(rbf(a) * SCALE);
      float mn = fmaxf(m, sc);
      if (last) {
        mn = fmaxf(mn, dpp<ROR8>(mn));
        const auto q16 = __builtin_amdgcn_permlane16_swap(__float_as_uint(mn), __float_as_uint(mn), false, false);
        mn = fmaxf(__uint_as_float(q16[0]), __uint_as_float(q16[1]));
        const auto q32 = __builtin_amdgcn_permlane32_swap(__float_as_uint(mn), __float_as_uint(mn), false, false);
        mn = fmaxf(__uint_as_float(q32[0]), __uint_as_float(q32[1]));
      }
      const float alpha = __builtin_amdgcn_exp2f((m - mn) * L2E), p = __builtin_amdgcn_exp2f((sc - mn) * L2E);
      l = fmaf(l, alpha, p); m = mn;
#pragma unroll
      for (int k = 0; k < 12; ++k) acc[k] = __builtin_elementwise_fma(vf[k], (f32x2)p, acc[k] * alpha);
      PIN();
    };
#pragma unroll
    for (int p = 0; p < NPRE; ++p) step_fn(K[p], V[p], p == 0, KVB, tv(NPRE), ts(NPRE));
    int t = NPRE + 1;
    do {
      if (w == 1 && t == CKG && lane == 0) lds[L_CNT + S_TAB] = 1;
      if (w == 0 && (t == CK1 || t == CK2)) { const unsigned tn = now() | 1u; if (lane == 0) lds[L_CNT + (t == CK1 ? S_T1 : S_T2)] = tn; }
      step_fn(K[0], V[0], true, KVB, tv(t), ts(t));
    } while (++t < SW);
    unsigned tend;
    while ((tend = lds_peek(lds + L_CNT + S_END)) == 0) __builtin_amdgcn_s_sleep(1);
    tagw = lds_peek(lds + L_CNT + S_TAG);
    const int stag = (STAG * ((((bid * 37) & 63) + 64 * w) - 128) * 64) >> 8;
    unsigned tgt = now() << 6;
    int tau = (int)((tend << 6) + stag - tgt) / (NT - 1 - SW);
#pragma unroll
    for (int i = 0; i < 3; ++i) K[1][i] = ld_nt(kr, tv(t) + 128 * i, ts(t));
    PIN();
#pragma unroll
    for (int i = 0; i < 3; ++i) V[1][i] = ld_nt(vr, tv(t) + 128 * i, ts(t));
    PIN();
    do {
      const unsigned t3 = lds_peek(lds + L_CNT + S_END);
      if (t3 != tend) { tend = t3; tau = (int)((tend << 6) + stag - tgt) / (NT - 1 - t); }
      tgt += tau;
      pace = tgt | 1u;
      step_fn(K[0], V[0], true, KVB, tv(t + 1), ts(t + 1));
      tgt += tau;
      pace = t + 2 < NT ? tgt | 1u : 0u;
      if (w == 0 && t + 2 == CK3) stamp3 = true;
      step_fn(K[1], V[1], true, t + 2 < NT ? KVB : 0u, tv(t + 2), ts(t + 2));
      t += 2;
    } while (t < NT);
    LAS float* drec = ldf + L_REC + w * 196 + c * 8 + r;
    asm volatile("" : "+v"(drec));
    step_fn(K[0], V[0], false, 0, 0, 0, true);
#pragma unroll
    for (int k = 0; k < 12; k += 4) asm volatile("" : "+v"(acc[k]), "+v"(acc[k + 1]), "+v"(acc[k + 2]), "+v"(acc[k + 3]));
    l += dpp<ROR8>(l); l = sw16(l, l); l = sw32(l, l);
    *(LAS f32x2*)(ldf + L_REC + w * 196 + 192) = f32x2{m, l};
#pragma unroll
    for (int i = 0; i < 3; ++i) {
      const float e[8] = {acc[4 * i][0], acc[4 * i][1], acc[4 * i + 1][0], acc[4 * i + 1][1], acc[4 * i + 2][0], acc[4 * i + 2][1], acc[4 * i + 3][0], acc[4 * i + 3][1]};
      drec[i * 64] = scatter8(e, lane & 8);
    }
  } else {
    const int hid = tid - 256, hw = w - 4;
    unsigned ep = __builtin_amdgcn_raw_buffer_load_b32(wsr, 0, 0, 16);
    if (hid < 12) lds[L_CNT + hid] = 0;
    const int cq = (j - 2) >> 1, rg = hid >> 3, c8 = hid & 7;
    u32x4 W[20], X[5];
    const rsrc_t xr = mk(x, has_q ? 4 * D * 2 : 0), wqr = mk(wq, has_q ? WB : 0);
#pragma unroll
    for (int i = 0; i < 5; ++i) { const int ch = 20 * hw + (lane & 15) + 16 * i; X[i] = ld_nt(xr, (lane >> 4) * (D * 2) + (ch >= 80 ? ch - 80 : ch) * 16, 0); }
    PIN();
#pragma unroll
    for (int i = 0; i < 20; ++i) {
      W[i] = ld_nt(wqr, (unsigned)(lane >> 3) * (HD * 2) + c8 * 16, (unsigned)(160 * hw + 8 * i) * (HD * 2) + (h * DH + cq * 64) * 2);
      if ((i & 3) == 3) PIN();
    }
    const unsigned mo = hid < 192 ? hid * 16 : OOB;
    const u32x4 Ma = ld_nt(mk(w1, F * 2), mo, 0), Mb = ld_nt(mk(b1, F * 2), mo, 0), Mc = ld_nt(mk(w2, F * 2), mo, 0);
    const unsigned b2v = __builtin_amdgcn_raw_buffer_load_b16(mk(b2, 2), 0, 0, 2);
    PIN();
    WG_BAR();
    asm volatile("" : "+v"(ep));
    tagw = __builtin_amdgcn_readfirstlane(ep) + 1;
    const Tag tg = make_tag(tagw);
    if (hid == 0) lds[L_CNT + S_TAG] = tagw;
    if (has_q) {
      float sa = 0.f, sb = 0.f;
#pragma unroll
      for (int i = 0; i < 20; i += 2) { sa = dot2(X[i >> 2][i & 3], 0x3f803f80u, sa); sb = dot2(X[i >> 2][(i & 3) + 1], 0x3f803f80u, sb); }
      const float mean = sum16(sa + sb) * (1.f / D);
      f32x2 s2 = {0.f, 0.f}, s3 = s2;
#pragma unroll
      for (int i = 0; i < 20; i += 2) {
        const f32x2 d = up2(X[i >> 2][i & 3]) - mean, e = up2(X[i >> 2][(i & 3) + 1]) - mean;
        s2 = __builtin_elementwise_fma(d, d, s2); s3 = __builtin_elementwise_fma(e, e, s3);
      }
      s2 += s3;
      const float inv = inv_sd(sum16(s2[0] + s2[1]) * (1.f / D)), mb = rbf(mean);
      LAS unsigned* xn = lds + L_XN + hw * 4 * 132;
#pragma unroll
      for (int i = 0; i < 2; ++i) {
        u32x4 o;
#pragma unroll
        for (int k = 0; k < 4; ++k) { const f32x2 y = rbf2(up2(X[i][k]) - mb) * inv; o[k] = pk2(y[0], y[1]); }
        *(LAS u32x4*)(xn + (lane >> 4) * 132 + ((lane & 15) + 16 * i) * 4) = o;
      }
      asm volatile("" ::: "memory");
      u32x4 af[5];
#pragma unroll
      for (int ks = 0; ks < 5; ++ks) af[ks] = *(LAS const u32x4*)(xn + (lane & 3) * 132 + ks * 16 + (lane >> 4) * 4);
      LAS unsigned* st = lds + L_WO + hw * 64 * 36;
      f32x4 dq[4];
#pragma unroll
      for (int nt = 0; nt < 4; ++nt) dq[nt] = f32x4{0.f, 0.f, 0.f, 0.f};
#pragma unroll
      for (int ks = 0; ks < 5; ++ks) {
        LAS unsigned* sk = st + (ks & 1) * 32 * 36;
#pragma unroll
        for (int i = 0; i < 4; ++i) *(LAS u32x4*)(sk + (8 * i + (lane >> 3)) * 36 + c8 * 4) = W[4 * ks + i];
        asm volatile("" ::: "memory");
        LAS const unsigned* rd = sk + (8 * (lane >> 4) + ((lane >> 2) & 3)) * 36 + (lane & 3) * 2;
#pragma unroll
        for (int nt = 0; nt < 4; ++nt) dq[nt] = __builtin_amdgcn_mfma_f32_16x16x32_bf16(__builtin_bit_cast(bf16x8, af[ks]), tr8(rd + nt * 8, 4 * 36), dq[nt], 0, 0, 0);
        asm volatile("" ::: "memory");
      }
      const bool g1 = lane & 16, g2 = lane & 32;
#pragma unroll
      for (int nt = 0; nt < 4; ++nt) {
        const float a01 = g1 ? dq[nt][1] : dq[nt][0], a23 = g1 ? dq[nt][3] : dq[nt][2];
        ldf[L_QR + hw * 256 + (lane >> 4) * 64 + nt * 16 + (lane & 15)] = g2 ? a23 : a01;
      }
      helper_bar(lds + L_CNT + S_BAR, 4, lane);
      if (hw == 0) {
        f32x4 a = *(LAS const f32x4*)(ldf + L_QR + 4 * lane);
#pragma unroll
        for (int ww = 1; ww < 4; ++ww) a += *(LAS const f32x4*)(ldf + L_QR + ww * 256 + 4 * lane);
        st_sc(wsr, WS_QP + ((h * 4 + (lane >> 4)) * 96 + cq * 32 + (lane & 15) * 2) * 8, u32x4{pk2(a[0], a[1]), tg.t32, pk2(a[2], a[3]), tg.t32});
      }
    }
    if (hw == 3) {
      while (now() - tstart < QDELAY) __builtin_amdgcn_s_sleep(2);
      const unsigned o = lane < 48 ? WS_QP + (h * 4 + b) * 96 * 8 + lane * 16 : OOB, vm = lane < 48 ? ~0u : 0u;
      u32x4 g;
#pragma nounroll
      for (unsigned spin = 0; spin < SPIN_CAP; ++spin) {
        asm volatile("" ::: "memory");
        g = ld_sc(wsr, o);
        if (__all((((g[1] ^ tg.t32) | (g[3] ^ tg.t32)) & vm) == 0)) break;
        __builtin_amdgcn_s_sleep(8);
      }
      if (lane < 48) *(LAS u32x2*)(lds + L_Q + 2 * lane) = u32x2{g[0], g[2]};
      asm volatile("s_waitcnt lgkmcnt(0)" ::: "memory");
      if (lane == 0) lds[L_CNT + S_Q] = 1;
    } else {
      *(LAS u32x4*)(lds + L_MIX + hid * 4) = Ma; *(LAS u32x4*)(lds + L_MIX + 768 + hid * 4) = Mb; *(LAS u32x4*)(lds + L_MIX + 1536 + hid * 4) = Mc;
      const unsigned bits0 = (TAB0 + ((bid * 32) & 4095)) | (bid >> 7) << 15;
#pragma nounroll
      for (int i0 = 0; i0 < 32; i0 += 8) {
        float ac[8];
#pragma unroll
        for (int i = 0; i < 8; ++i) {
          const float t = __uint_as_float((bits0 + i0 + i) << 16);
          ac[i] = 0.f;
#pragma unroll
          for (int k = 0; k < 4; ++k) ac[i] = mix2(t, up2(Ma[k]), up2(Mb[k]), Mc[k], ac[i]);
        }
        float y = scatter8(ac, lane & 8);
        y += dpp<XOR1>(y); y += dpp<XOR2>(y); y += dpp<HMIR>(y);
        if ((lane & 7) == 0) ldf[L_RED + hw * 32 + i0 + (lane >> 3)] = y;
      }
      helper_bar(lds + L_CNT + S_BAR3, 3, lane);
      if (hw == 2 && lane < 8) {
        f32x4 y = *(LAS const f32x4*)(ldf + L_RED + 4 * lane);
#pragma unroll
        for (int ww = 1; ww < 3; ++ww) y += *(LAS const f32x4*)(ldf + L_RED + ww * 32 + 4 * lane);
        const f32x2 y0 = rbf2(f32x2{y[0], y[1]}) + bflo(b2v), y1 = rbf2(f32x2{y[2], y[3]}) + bflo(b2v);
        st_sc(wsr, WS_TB + bid * 128 + lane * 16, u32x4{pk2(y0[0], y0[1]), tg.t32, pk2(y1[0], y1[1]), tg.t32});
      }
    }
    const rsrc_t wor = mk(wo, has_o ? WB : 0);
    u32x4 Wo[12];
#pragma unroll
    for (int i = 0; i < 12; ++i) Wo[i] = ld_nt(wor, (unsigned)rg * (D * 2) + c8 * 16, (unsigned)(hp * 2 * DH + 32 * i) * (D * 2) + co * 128);
    if (hid < 80) *(LAS u32x4*)(lds + L_X + hid * 4) = __builtin_nontemporal_load((const u32x4*)(x + b2i * D + hid * 8));
    if (hid == 80) lds[L_X + 320] = b2v;
#pragma unroll
    for (int i = 0; i < 12; ++i) *(LAS u32x4*)(lds + L_WO + (rg + 32 * i) * 36 + c8 * 4) = Wo[i];
    helper_bar(lds + L_CNT + S_BAR, has_q ? 8 : 4, lane);
    LAS const unsigned* wl = lds + L_WO + (8 * (lane >> 4) + ((lane >> 2) & 3)) * 36 + hw * 8 + (lane & 3) * 2;
#pragma unroll
    for (int ks = 0; ks < 12; ++ks) Bf[ks] = tr8(wl + ks * 32 * 36, 4 * 36);
    a_p1 = lane <= 48 ? WS_P1 + (h * 8 + j) * P1REC + lane * 16 : OOB;
    a_p2 = (lane & 12) ? OOB : WS_P2 + (((lane & 3) * 16 + hp) * D + co * 64 + hw * 16 + (lane >> 4) * 4) * 4;
    a_rec = ldf + L_REC + 4 * (lane < 48 ? lane : 48);
    a_o = lds + L_O + (lane & 3) * OSTR + (lane >> 4) * 4;
    asm volatile("" : "+v"(a_p1), "+v"(a_p2), "+v"(a_rec), "+v"(a_o));
    if (hw < 2) {
      while (lds_peek(lds + L_CNT + S_TAB) == 0) __builtin_amdgcn_s_sleep(16);
#pragma nounroll
      for (int half = 0; half < 2; ++half) {
        u32x4 g[8];
#pragma nounroll
        for (unsigned spin = 0; spin < SPIN_CAP; ++spin) {
          asm volatile("" ::: "memory");
          unsigned bad = 0;
#pragma unroll
          for (int i = 0; i < 8; ++i) g[i] = ld_sc(wsr, WS_TB + hid * 16, (half * 8 + i) * 2048);
#pragma unroll
          for (int i = 0; i < 8; ++i) bad |= (g[i][1] ^ tg.t32) | (g[i][3] ^ tg.t32);
          if (__all(bad == 0)) break;
          __builtin_amdgcn_s_sleep(32);
        }
#pragma unroll
        for (int i = 0; i < 8; ++i) *(LAS u32x2*)(lds + L_TAB + ((half * 8 + i) * 128 + hid) * 2) = u32x2{g[i][0], g[i][2]};
      }
    }
    if (hw == 2) {
      unsigned tm0 = 0;
#pragma nounroll
      for (int ck = 0; ck < 3; ++ck) {
        unsigned t2;
        while ((t2 = lds_peek(lds + L_CNT + (ck == 0 ? S_T1 : ck == 1 ? S_T2 : S_T3))) == 0) __builtin_amdgcn_s_sleep(8);
        if (((bid >> 4) & 3) == (bid >> 6) && lane == 0)
          __builtin_amdgcn_raw_buffer_store_b64(u32x2{t2, tg.t32}, wsr, WS_TS + (ck * 64 + (bid & 15) * 4 + (bid >> 6)) * 128, 0, 16);
        while (now() - t2 < WAITT) __builtin_amdgcn_s_sleep(8);
        const u32x2 g = __builtin_amdgcn_raw_buffer_load_b64(wsr, WS_TS + (ck * 64 + lane) * 128, 0, 16);
        const unsigned tm = t2 + (int)(sum64(g[1] == tg.t32 ? (float)min(max((int)(g[0] - t2), -2000), 2000) : (float)(WAITT + 30)) * (1.f / 64));
        const float left = ck == 1 ? (NT - 1.f - CK2) / (CK2 - CK1) * (1.f - EPS2) : (NT - 1.f - CK3) / (CK3 - CK2) * (1.f - EPS3);
        if (ck && lane == 0) lds[L_CNT + S_END] = (tm + (int)((float)(int)(tm - tm0) * left)) | 1u;
        tm0 = tm;
      }
    }
  }
  const Tag tg = make_tag(tagw);
  WG_BAR();
  if (w == 4) {
    float mm[4];
    f32x4 rv[4];
#pragma unroll
    for (int i = 0; i < 4; ++i) { mm[i] = ldf[L_REC + i * 196 + 192]; rv[i] = *(LAS const f32x4*)(a_rec + i * 196); }
    __builtin_amdgcn_sched_barrier(0);
    const float M = fmaxf(fmaxf(mm[0], mm[1]), fmaxf(mm[2], mm[3]));
    f32x4 a = {0.f, 0.f, 0.f, 0.f};
#pragma unroll
    for (int i = 0; i < 4; ++i) a += __builtin_amdgcn_exp2f((mm[i] - M) * L2E) * rv[i];
    if (lane >= 48) a = f32x4{M, a[1], 0.f, 0.f};
    st_sc(wsr, a_p1, enc4(a, tg));
  }
  LAS const unsigned* own = lds + L_HH + cg * 5 + (lane < 5 ? lane : 0);
  unsigned a_out = lane < 5 ? (b2i * D + cg * 10 + 2 * lane) * 2 : OOB;
  asm volatile("" : "+v"(own), "+v"(a_out));
  const unsigned tcom = lds_peek(lds + L_CNT + S_END);
  if (has_o) {
    {
      unsigned oa = lane <= 48 ? WS_P1 + (hp * 16 + (w >> 2) * 8 + 2 * (w & 3)) * P1REC + lane * 16 : OOB;
      unsigned vm = lane <= 48 ? 0xffu : 0u;
      LAS unsigned* da = lane < 48 ? lds + L_O + (w & 3) * OSTR + (w >> 2) * 96 + 2 * lane : lds + L_QR + 2 * lane;
      asm volatile("" : "+v"(da), "+v"(oa), "+v"(vm));
      u32x4 ga, gb;
      while ((int)(now() - tcom) < C1) { }
#pragma nounroll
      for (unsigned spin = 0; spin < SPIN_CAP; ++spin) {
        asm volatile("" ::: "memory");
        ga = untag(ld_sc(wsr, oa), tg); gb = untag(ld_sc(wsr, oa + P1REC), tg);
        if (__all(((any4(ga) | any4(gb)) & vm) == 0)) break;
        __builtin_amdgcn_s_sleep(1);
      }
      const float m0 = __uint_as_float(__builtin_amdgcn_readlane(ga[0], 48)), m1 = __uint_as_float(__builtin_amdgcn_readlane(gb[0], 48)), M = fmaxf(m0, m1);
      const float l0 = __uint_as_float(__builtin_amdgcn_readlane(ga[1], 48)), l1 = __uint_as_float(__builtin_amdgcn_readlane(gb[1], 48));
      float f0 = __builtin_amdgcn_exp2f((m0 - M) * L2E), f1 = __builtin_amdgcn_exp2f((m1 - M) * L2E);
      const float inv = __builtin_amdgcn_rcpf(fmaf(f0, l0, f1 * l1));
      f0 *= inv; f1 *= inv;
      const f32x4 o = __builtin_elementwise_fma(asf4(ga), (f32x4)f0, asf4(gb) * f1);
      *(LAS u32x2*)da = u32x2{pk2(o[0], o[1]), pk2(o[2], o[3])};
    }
    WG_BAR();
    if (w >= 4) {
      u32x4 af[12];
#pragma unroll
      for (int ks = 0; ks < 12; ++ks) af[ks] = *(LAS const u32x4*)(a_o + ks * 16);
      __builtin_amdgcn_sched_barrier(0);
      f32x4 d0 = {0.f, 0.f, 0.f, 0.f}, d1 = d0;
#pragma unroll
      for (int ks = 0; ks < 12; ks += 2) {
        d0 = __builtin_amdgcn_mfma_f32_16x16x32_bf16(Bf[ks], __builtin_bit_cast(bf16x8, af[ks]), d0, 0, 0, 0);
        d1 = __builtin_amdgcn_mfma_f32_16x16x32_bf16(Bf[ks + 1], __builtin_bit_cast(bf16x8, af[ks + 1]), d1, 0, 0, 0);
      }
      st_sc(wsr, a_p2, enc4(d0 + d1, tg));
    }
  }
  if (w < 5) {
    unsigned o = WS_P2 + (b2i * 16 + (lane >> 5) * 8) * D * 4 + (w * 32 + (lane & 31)) * 16, o4 = o + 4 * D * 4;
    const int pr = (w * 32 + (lane & 31)) * 2 + (lane >> 5);
    f32x2 xr = up2(lds[L_X + pr]);
    LAS unsigned* dh = lds + L_HH + pr;
    asm volatile("" : "+v"(xr), "+v"(dh), "+v"(o), "+v"(o4));
    f32x4 y;
    while ((int)(now() - tcom) < C2) { }
#pragma nounroll
    for (unsigned spin = 0; spin < SPIN_CAP; ++spin) {
      asm volatile("" ::: "memory");
      u32x4 g[8];
#pragma unroll
      for (int i = 0; i < 8; ++i) g[i] = ld_sc(wsr, i < 4 ? o : o4, (i & 3) * D * 4);
      unsigned bad = 0;
      y = f32x4{0.f, 0.f, 0.f, 0.f};
#pragma unroll
      for (int i = 0; i < 8; ++i) { const u32x4 u = untag(g[i], tg); bad |= any4(u); y += asf4(u); asm volatile("" : "+v"(y), "+v"(bad)); }
      if (__all((bad & 0xffu) == 0)) break;
      __builtin_amdgcn_s_sleep(1);
    }
    const f32x2 hs = xr + rbf2(f32x2{sw32(y[0], y[2]), sw32(y[1], y[3])});
    const unsigned hp2 = pk2(hs[0], hs[1]);
    *dh = hp2;
    const f32x2 hv = up2(hp2);
    const float piv = __uint_as_float(__builtin_amdgcn_readfirstlane(__float_as_uint(hv[0])));
    const f32x2 d = hv - piv, dd = d * d;
    const float sp = sw32(d[0] + d[1], dd[0] + dd[1]), sq = sum16(sw16(sp, sp));
    const float s1 = __uint_as_float(__builtin_amdgcn_readlane(__float_as_uint(sq), 0)), s2 = __uint_as_float(__builtin_amdgcn_readlane(__float_as_uint(sq), 32));
    *(LAS f32x2*)(ldf + L_ST + 2 * w) = f32x2{fmaf(s1, 1.f / 128, piv), fmaf(-s1, s1 * (1.f / 128), s2)};
  }
  WG_BAR();
  if (w == 0) {
    const f32x4 sa = *(LAS const f32x4*)(ldf + L_ST), sb = *(LAS const f32x4*)(ldf + L_ST + 4);
    const f32x2 sc = *(LAS const f32x2*)(ldf + L_ST + 8);
    const unsigned hw2 = *own;
    __builtin_amdgcn_sched_barrier(0);
    const f32x2 hv = up2(hw2);
    const float mean = (((sa[0] + sa[2]) + (sb[0] + sb[2])) + sc[0]) * 0.2f;
    const float e0 = sa[0] - mean, e1 = sa[2] - mean, e2 = sb[0] - mean, e3 = sb[2] - mean, e4 = sc[0] - mean;
    const float var = fmaf(fmaf(e0, e0, e1 * e1) + fmaf(e2, e2, e3 * e3) + e4 * e4, 0.2f, (((sa[1] + sa[3]) + (sb[1] + sb[3])) + sc[1]) * (1.f / D));
    const float inv = inv_sd(var), mb = rbf(mean);
    const float inv2 = inv_sd(var * inv * inv), mb2 = rbf((mean - mb) * inv);
    const f32x2 t0 = rbf2(rbf2(rbf2(hv - mb) * inv) - mb2) * inv2;
    const unsigned tb = pk2(t0[0], t0[1]), i0 = (tb & 0x7fffu) - TAB0, i1 = ((tb >> 16) & 0x7fffu) - TAB0;
    LAS const unsigned short* tab = (LAS const unsigned short*)(lds + L_TAB);
    f32x2 y = {bflo(tab[(i0 & 4095u) + ((tb >> 3) & 4096u)]), bflo(tab[(i1 & 4095u) + ((tb >> 19) & 4096u)])};
    if (!__all((i0 | i1) < 4096u)) {
      const float bias = bflo(lds[L_X + 320]);
#pragma nounroll
      for (int i = 0; i < 10; ++i) {
        const float t = __shfl(i & 1 ? bfhi(tb) : bflo(tb), i >> 1);
        float a = 0.f;
#pragma nounroll
        for (int k = 0; k < 12; ++k) a = mix2(t, up2(lds[L_MIX + lane + 64 * k]), up2(lds[L_MIX + 768 + lane + 64 * k]), lds[L_MIX + 1536 + lane + 64 * k], a);
        a = rbf(rbf(sum64(a)) + bias);
        if (lane == (i >> 1)) { if (i & 1) y[1] = a; else y[0] = a; }
      }
    }
    const f32x2 o = hv + y;
    __builtin_amdgcn_raw_buffer_store_b32(pk2(o[0], o[1]), mk(out, 4 * D * 2), a_out, 0, 16);
  }
  if (bid == 0 && tid == 64) __builtin_amdgcn_raw_buffer_store_b32(tagw, wsr, 0, 0, 16);
}

extern "C" void kernel_launch(void* const* d_in, const int* in_sizes, int n_in, void* d_out, int out_size, void* d_ws, size_t ws_size,
                              hipStream_t stream) {
  typedef const unsigned short* P;
  fused_decode<<<256, 512, 0, stream>>>((P)d_in[0], (P)d_in[1], (P)d_in[2], (P)d_in[3], (P)d_in[4], (P)d_in[6], (P)d_in[7], (P)d_in[8], (P)d_in[9],
                                        (unsigned short*)d_out, (unsigned char*)d_ws);
}
```

```cpp
#include <hip/hip_runtime.h>
#include <stdint.h>

typedef unsigned u32x4 __attribute__((ext_vector_type(4)));
typedef unsigned u32x2 __attribute__((ext_vector_type(2)));
typedef float f32x4 __attribute__((ext_vector_type(4)));
typedef float f32x2 __attribute__((ext_vector_type(2)));
typedef __bf16 bf16x2 __attribute__((ext_vector_type(2)));
typedef __bf16 bf16x8 __attribute__((ext_vector_type(8)));
typedef __amdgpu_buffer_rsrc_t rsrc_t;
#define LAS __attribute__((address_space(3)))

#define NPRE 7
#define QDELAY 600
#define PAIR 2
#define WAITT 400
#define CK1 15
#define CK2 27
#define SW 33
#define EPS -0.002f
#define C1 195
#define C2 388
#define CKG 29
#define FORCE_FB 0
#define STAG 85
#define STK 37
#define CK3 45
#define QSLP 8
#define EPS3 -0.023f
#define XMAP 2
#define EARLYK 1
#define CAP 5
#define PSH 0
#define PRE0 1
#define PRE1 0
#define ORD 0
#define ROTX 0
#define STR2(x) #x
#define STR(x) STR2(x)

constexpr int D = 640, H = 32, SK = 4096, DH = 192, HD = H * DH, F = 1536;
constexpr unsigned SEQB = SK * DH * 2, TILEB = 8 * DH * 2, KVB = 4u * H * SEQB, WB = D * HD * 2;
constexpr unsigned P1REC = 896;
constexpr unsigned WS_TS = 4096, WS_TB = WS_TS + 4 * 64 * 128, WS_QP = WS_TB + 256 * 128, WS_P1 = WS_QP + H * 4 * 96 * 8, WS_P2 = WS_P1 + 256 * P1REC, WS_END = WS_P2 + 4 * 16 * D * 4;
constexpr unsigned OOB = 0x7ffffff0u;
constexpr unsigned SPIN_CAP = 1u << 17;
constexpr unsigned TAB0 = 100u << 7;
constexpr float L2E = 1.4426950408889634f, SCALE = 0.07216878364870322f;
constexpr int OSTR = 208;
constexpr int L_CNT = 0, L_Q = 12, L_REC = L_Q + 96, L_O = L_REC + 4 * 196, L_RED = L_O + 4 * OSTR, L_ST = L_RED + 8 * 16, L_X = L_ST + 16, L_HH = L_X + 324,
              L_XN = L_HH + 320, L_QR = L_XN + 16 * 132, L_MIX = L_QR + 4 * 256, L_WO = L_MIX + 3 * F / 2, L_Y = L_WO, L_TAB = L_WO + 2 * DH * 36, L_SC = L_TAB + 4096, L_END = L_SC + 2048;
constexpr int LDS_DW = 28672;
static_assert(L_END <= LDS_DW && L_REC % 4 == 0 && L_O % 4 == 0 && L_XN % 4 == 0 && L_QR % 4 == 0 && L_MIX % 4 == 0 && L_WO % 4 == 0, "lds map");

__device__ __forceinline__ float bflo(unsigned w) { return __uint_as_float(w << 16); }
__device__ __forceinline__ float bfhi(unsigned w) { return __uint_as_float(w & 0xffff0000u); }
__device__ __forceinline__ unsigned pk2(float lo, float hi) { unsigned r; asm("v_cvt_pk_bf16_f32 %0, %1, %2" : "=v"(r) : "v"(lo), "v"(hi)); return r; }
__device__ __forceinline__ float rbf(float f) { return (float)(__bf16)f; }
__device__ __forceinline__ f32x2 up2(unsigned w) { return f32x2{bflo(w), bfhi(w)}; }
__device__ __forceinline__ f32x2 rbf2(f32x2 v) { return up2(pk2(v[0], v[1])); }
__device__ __forceinline__ float dot2(unsigned a, unsigned b, float c) {
  return __builtin_amdgcn_fdot2_f32_bf16(__builtin_bit_cast(bf16x2, a), __builtin_bit_cast(bf16x2, b), c, false);
}
template <int CTRL> __device__ __forceinline__ float dpp(float x) {
  return __builtin_bit_cast(float, __builtin_amdgcn_mov_dpp(__builtin_bit_cast(int, x), CTRL, 0xf, 0xf, true));
}
constexpr int XOR1 = 0xB1, XOR2 = 0x4E, HMIR = 0x141, ROR8 = 0x128;
__device__ __forceinline__ float sw32(float a, float b) {
  const auto r = __builtin_amdgcn_permlane32_swap(__float_as_uint(a), __float_as_uint(b), false, false);
  return __uint_as_float(r[0]) + __uint_as_float(r[1]);
}
__device__ __forceinline__ float sw16(float a, float b) {
  const auto r = __builtin_amdgcn_permlane16_swap(__float_as_uint(a), __float_as_uint(b), false, false);
  return __uint_as_float(r[0]) + __uint_as_float(r[1]);
}
__device__ __forceinline__ float sum16(float v) { v += dpp<XOR1>(v); v += dpp<XOR2>(v); v += dpp<HMIR>(v); v += dpp<ROR8>(v); return v; }
__device__ __forceinline__ float sum64(float v) { v = sum16(v); v = sw16(v, v); return sw32(v, v); }
__device__ __forceinline__ float scatter8(const float* a, bool b3) {
  float y[4], z[2];
#pragma unroll
  for (int e = 0; e < 4; ++e) y[e] = sw32(a[e], a[e + 4]);
#pragma unroll
  for (int e = 0; e < 2; ++e) z[e] = sw16(y[e], y[e + 2]);
  return (b3 ? z[1] : z[0]) + dpp<ROR8>(b3 ? z[0] : z[1]);
}
typedef short s16x4 __attribute__((ext_vector_type(4)));
__device__ __forceinline__ bf16x8 tr8(LAS const unsigned* p, int step) {
  const s16x4 a = __builtin_amdgcn_ds_read_tr16_b64_v4i16((LAS s16x4*)p), b = __builtin_amdgcn_ds_read_tr16_b64_v4i16((LAS s16x4*)(p + step));
  return __builtin_bit_cast(bf16x8, __builtin_shufflevector(a, b, 0, 1, 2, 3, 4, 5, 6, 7));
}
__device__ __forceinline__ rsrc_t mk(const void* p, unsigned n) { return __builtin_amdgcn_make_buffer_rsrc((void*)p, 0, n, 0x00020000); }
__device__ __forceinline__ u32x4 ld_nt(rsrc_t r, unsigned v, unsigned s) { return __builtin_amdgcn_raw_buffer_load_b128(r, v, s, 2); }
__device__ __forceinline__ u32x4 ld_sc(rsrc_t r, unsigned v, unsigned s = 0) { return __builtin_amdgcn_raw_buffer_load_b128(r, v, s, 16); }
__device__ __forceinline__ void st_sc(rsrc_t r, unsigned v, u32x4 x) { __builtin_amdgcn_raw_buffer_store_b128(x, r, v, 0, 16); }
__device__ __forceinline__ unsigned now() { return (unsigned)__builtin_amdgcn_s_memrealtime(); }
__device__ __forceinline__ unsigned lds_peek(LAS unsigned* p) {
  return __builtin_amdgcn_readfirstlane(__hip_atomic_load(p, __ATOMIC_RELAXED, __HIP_MEMORY_SCOPE_WORKGROUP));
}
#define PIN() do { asm volatile("" ::: "memory"); __builtin_amdgcn_sched_barrier(0); } while (0)
#define WG_BAR() do { asm volatile("s_waitcnt lgkmcnt(0)" ::: "memory"); __builtin_amdgcn_s_barrier(); PIN(); } while (0)
#define STAMP(i) do { } while (0)
struct Tag { unsigned b0, b1, b2, b3, t32; };
__device__ __forceinline__ Tag make_tag(unsigned long long e) {
  const unsigned t = (unsigned)e;
  return Tag{t & 0xffu, (t + (t >> 8)) & 0xffu, (t + (t >> 16)) & 0xffu, (t + (t >> 24)) & 0xffu, t};
}
__device__ __forceinline__ u32x4 enc4(f32x4 v, const Tag& t) {
  return u32x4{(__float_as_uint(v[0]) & 0xffffff00u) | t.b0, (__float_as_uint(v[1]) & 0xffffff00u) | t.b1, (__float_as_uint(v[2]) & 0xffffff00u) | t.b2,
               (__float_as_uint(v[3]) & 0xffffff00u) | t.b3};
}
__device__ __forceinline__ u32x4 untag(u32x4 g, const Tag& t) { return g ^ u32x4{t.b0, t.b1, t.b2, t.b3}; }
__device__ __forceinline__ unsigned any4(u32x4 x) { return (x[0] | x[1]) | (x[2] | x[3]); }
__device__ __forceinline__ f32x4 asf4(u32x4 x) { return __builtin_bit_cast(f32x4, x); }
__device__ __forceinline__ void helper_bar(LAS unsigned* cnt, unsigned target, int lane) {
  asm volatile("s_waitcnt lgkmcnt(0)" ::: "memory");
  if (lane == 0) __hip_atomic_fetch_add(cnt, 1u, __ATOMIC_RELAXED, __HIP_MEMORY_SCOPE_WORKGROUP);
  while (lds_peek(cnt) < target) { }
  asm volatile("" ::: "memory");
}
__device__ __forceinline__ float mix2(float t, f32x2 wa, f32x2 wb, unsigned wc, float acc) {
  constexpr float K0 = -2.f * L2E * 0.7978845608028654f, K1 = K0 * 0.044715f;
  const f32x2 u = rbf2(rbf2(wa * t) + wb);
  const f32x2 z = u * __builtin_elementwise_fma(u * u, (f32x2)K1, (f32x2)K0);
  const f32x2 d = f32x2{__builtin_amdgcn_exp2f(z[0]), __builtin_amdgcn_exp2f(z[1])} + 1.f;
  const f32x2 g = u * f32x2{__builtin_amdgcn_rcpf(d[0]), __builtin_amdgcn_rcpf(d[1])};
  return dot2(pk2(g[0], g[1]), wc, acc);
}
__device__ __forceinline__ float inv_sd(float var) { return __builtin_amdgcn_rcpf(rbf(__builtin_amdgcn_sqrtf(rbf(rbf(var) + 1e-5f)))); }

__global__ void __launch_bounds__(512, 2)
fused_decode(const unsigned short* x, const unsigned short* kc, const unsigned short* vc, const unsigned short* wq, const unsigned short* wo,
             const unsigned short* w1, const unsigned short* b1, const unsigned short* w2, const unsigned short* b2, unsigned short* out,
             unsigned char* ws) {
  __shared__ __attribute__((aligned(16))) unsigned lds_raw[LDS_DW];
  LAS unsigned* lds = (LAS unsigned*)lds_raw;
  LAS float* ldf = (LAS float*)lds_raw;
  const int tid = threadIdx.x, lane = tid & 63, w = __builtin_amdgcn_readfirstlane(tid >> 6);
  const int bid = blockIdx.x, h = XMAP == 2 ? ((bid >> 1) & 3) * 8 + ((bid >> 3) & 7) : XMAP ? (bid & 7) * 4 + ((bid >> 3) & 3) : bid >> 3,
            j = XMAP == 2 ? (bid >> 6) * 2 + ((bid ^ h) & 1) : XMAP ? bid >> 5 : (bid + h) & 7, b = j >> 1, s = j & 1;
  const int PSS = h & 1;
  const bool has_q = j >= 2 && s == 1, has_o = j < 2 || s == 0;
  const int hp = h >> 1, co = (h & 1) * 5 + (j < 2 ? j : b + 1);
  const int b2i = bid >> 6, cg = bid & 63;
  STAMP(0);
  const unsigned tstart = now();
  asm volatile("" :: "s"(x), "s"(kc), "s"(vc), "s"(wq), "s"(wo), "s"(w1), "s"(b1), "s"(w2), "s"(b2), "s"(out), "s"(ws));
  const rsrc_t wsr0 = mk(ws, WS_END);
  bf16x8 Bf[12];
  unsigned a_p1, a_p2;
  LAS const float* a_rec;
  LAS const unsigned* a_o;
  unsigned long long E1;

  if (__builtin_expect(w < 4, 1)) {
    __builtin_amdgcn_s_setprio(2);
    const int r = lane >> 3, c = lane & 7;
    constexpr int NT = 64;
    const bool hi = r >> 2;
    const unsigned kb = hi ? 86 : 0, c1 = hi ? 85 : 86, d2 = hi ? 85 : 86;
    const unsigned seq = (unsigned)(b * H + h) * SEQB + s * (SEQB / 2);
    auto tv = [&](int u, int hf) -> unsigned {
      const unsigned mm = ((32 * u + (ORD ? 8 * w + 4 * hf : 16 * hf + 4 * w)) & 255) + (r & 3);
      return (mm + kb + (mm >= c1 ? 85 : 0) + (mm >= 171 ? d2 : 0)) * 384 + c * 16; };
    auto ts = [&](int u) -> unsigned { return seq + (unsigned)((u >> 3) & 3) * (512 * 384); };
    auto req = [&](u32x4 (&X)[3], int u, int hf) {
      const rsrc_t q = mk(u < 32 ? kc : vc, KVB);
      const unsigned av = tv(u, hf), as = ts(u);
#pragma unroll
      for (int i = 0; i < 3; ++i) X[i] = ld_nt(q, av + 128 * i, as);
      PIN(); };
    u32x4 T[NPRE][2][3];
#pragma unroll
    for (int p = 0; p < NPRE; ++p) {
      if (p == EARLYK) WG_BAR();
#pragma unroll
      for (int hf = 0; hf < 2; ++hf) {
        if (p) asm volatile("s_waitcnt vmcnt(3)" ::: "memory");
        req(T[p][hf], p, hf);
      }
    }
    while (lds_peek(lds + L_CNT + 7) == 0) __builtin_amdgcn_s_sleep(1);
    PIN();
    STAMP(1);
    u32x4 qf[3];
#pragma unroll
    for (int i = 0; i < 3; ++i) qf[i] = *(LAS const u32x4*)(lds + L_Q + (c + 8 * i) * 4);
    float m = __uint_as_float(0xff7f0000u);
    LAS float* sl = ldf + L_SC + w * 512 + r;
    auto kstep = [&](u32x4 (&X)[2][3], int u, bool refill, int un) {
      float a[2];
#pragma unroll
      for (int hf = 0; hf < 2; ++hf) {
        float a0 = 0.f, a1 = 0.f, a2 = 0.f;
#pragma unroll
        for (int k = 0; k < 4; ++k) { a0 = dot2(X[hf][0][k], qf[0][k], a0); a1 = dot2(X[hf][1][k], qf[1][k], a1); a2 = dot2(X[hf][2][k], qf[2][k], a2); }
        a[hf] = (a0 + a1) + a2;
        asm volatile("" : "+v"(a[hf]));
        PIN();
        if (refill) req(X[hf], un, hf);
      }
#pragma unroll
      for (int hf = 0; hf < 2; ++hf) {
        float y = a[hf];
        y += dpp<XOR1>(y); y += dpp<XOR2>(y); y += dpp<HMIR>(y);
        y = rbf(rbf(y) * SCALE);
        m = fmaxf(m, y);
        sl[u * 16 + hf * 8] = y;
      }
      PIN();
    };
#pragma unroll
    for (int p = 0; p < NPRE; ++p) kstep(T[p], p, p == 0, NPRE);
    int t = NPRE + 1;
    do {
      if (w == 1 && t == CKG && lane == 0) lds[L_CNT + 6] = 1;
      if (w == 0 && (t == CK1 || t == CK2)) { const unsigned tn = now() | 1u; if (lane == 0) lds[L_CNT + (t == CK1 ? 1 : 2)] = tn; }
      kstep(T[0], t - 1, true, t);
    } while (++t < SW);
    static_assert(SW == 33 && NPRE < CK1 && CK1 < CK2 && CK2 < SW && NPRE < CKG && CKG < SW && SW + 2 <= CK3 && CK3 < 62 && (CK3 - SW) % 2 == 0, "every stamp the helpers wait for is reached");
    m = fmaxf(m, dpp<ROR8>(m));
    { const auto q16 = __builtin_amdgcn_permlane16_swap(__float_as_uint(m), __float_as_uint(m), false, false);
      m = fmaxf(__uint_as_float(q16[0]), __uint_as_float(q16[1]));
      const auto q32 = __builtin_amdgcn_permlane32_swap(__float_as_uint(m), __float_as_uint(m), false, false);
      m = fmaxf(__uint_as_float(q32[0]), __uint_as_float(q32[1])); }
    float l = 0.f;
    f32x2 acc[12];
#pragma unroll
    for (int k = 0; k < 12; ++k) acc[k] = f32x2{0.f, 0.f};
    bool stamp3 = false;
    auto vstep = [&](u32x4 (&X)[2][3], int u, bool refill, unsigned pace) {
      float p[2];
#pragma unroll
      for (int hf = 0; hf < 2; ++hf) { p[hf] = __builtin_amdgcn_exp2f((sl[(u - 32) * 16 + hf * 8] - m) * L2E); l += p[hf]; }
      asm volatile("" : "+v"(p[0]), "+v"(p[1]));
      PIN();
#pragma unroll
      for (int hf = 0; hf < 2; ++hf) {
        f32x2 vf[12];
#pragma unroll
        for (int k = 0; k < 12; ++k) vf[k] = up2(X[hf][k >> 2][k & 3]);
        if (refill) {
          asm volatile("" : "+v"(vf[0]), "+v"(vf[1]), "+v"(vf[2]), "+v"(vf[3]), "+v"(vf[4]), "+v"(vf[5]));
          asm volatile("" : "+v"(vf[6]), "+v"(vf[7]), "+v"(vf[8]), "+v"(vf[9]), "+v"(vf[10]), "+v"(vf[11]));
          PIN();
          if (hf == 0) {
            if (CAP < 6) asm volatile("s_waitcnt vmcnt(" STR(CAP) ")" ::: "memory");
            unsigned tn;
            while ((int)(((tn = now()) << 6) - pace) < 0) { }
            if (stamp3) { if (lane == 0) lds[L_CNT + 8] = tn | 1u; stamp3 = false; }
          }
          req(X[hf], u + 2, hf);
        }
#pragma unroll
        for (int k = 0; k < 12; ++k) acc[k] = __builtin_elementwise_fma(vf[k], (f32x2)p[hf], acc[k]);
#pragma unroll
        for (int k = 0; k < 12; k += 4) asm volatile("" : "+v"(acc[k]), "+v"(acc[k + 1]), "+v"(acc[k + 2]), "+v"(acc[k + 3]));
        PIN();
      }
    };
    unsigned tend;
    while ((tend = lds_peek(lds + L_CNT + 4)) == 0) __builtin_amdgcn_s_sleep(1);
    E1 = (unsigned long long)lds_peek(lds + L_CNT + 11) << 32 | lds_peek(lds + L_CNT + 10);
    const int stag = (STAG * ((((bid * STK) & 63) + 64 * w) - 128) * 64) >> 8;
    unsigned tgt = now() << 6;
    int tau = (int)((tend << 6) + stag - tgt) / (NT - 1 - SW);
    req(T[1][0], t, 0); req(T[1][1], t, 1);
    do {
      const unsigned t3 = lds_peek(lds + L_CNT + 4);
      if (t3 != tend) { tend = t3; tau = (int)((tend << 6) + stag - tgt) / (NT - 1 - t); }
      tgt += tau;
      vstep(T[0], t - 1, true, tgt);
      tgt += tau;
      if (w == 0 && t + 2 == CK3) stamp3 = true;
      vstep(T[1], t, true, tgt);
      t += 2;
    } while (t < NT - 1);
    LAS float* drec = ldf + L_REC + w * 196 + c * 8 + r;
    asm volatile("" : "+v"(drec));
    vstep(T[0], NT - 2, false, 0);
    vstep(T[1], NT - 1, false, 0);
    l += dpp<ROR8>(l); l = sw16(l, l); l = sw32(l, l);
    *(LAS f32x2*)(ldf + L_REC + w * 196 + 192) = f32x2{m, l};
#pragma unroll
    for (int i = 0; i < 3; ++i) {
      const float e[8] = {acc[4 * i][0], acc[4 * i][1], acc[4 * i + 1][0], acc[4 * i + 1][1], acc[4 * i + 2][0], acc[4 * i + 2][1], acc[4 * i + 3][0], acc[4 * i + 3][1]};
      drec[i * 64] = scatter8(e, lane & 8);
    }
  } else {
    const int hid = tid - 256, hw = w - 4;
    u32x2 ep = __builtin_amdgcn_raw_buffer_load_b64(wsr0, 0, 0, 16);
    if (hid < 12) lds[L_CNT + hid] = 0;
    const int cq = (j - 2) >> 1, rg = hid >> 3, c8 = hid & 7;
    u32x4 W[20], X[5];
    const rsrc_t xr = mk(x, has_q ? 4 * D * 2 : 0), wqr = mk(wq, has_q ? WB : 0);
#pragma unroll
    for (int i = 0; i < 5; ++i) { const int ch = 20 * hw + (lane & 15) + 16 * i; X[i] = ld_nt(xr, (lane >> 4) * (D * 2) + (ch >= 80 ? ch - 80 : ch) * 16, 0); }
    PIN();
#pragma unroll
    for (int i = 0; i < 20; ++i) {
      W[i] = ld_nt(wqr, (unsigned)(lane >> 3) * (HD * 2) + c8 * 16, (unsigned)(160 * hw + 8 * i) * (HD * 2) + (h * DH + cq * 64) * 2);
      if ((i & 3) == 3) PIN();
    }
    const unsigned mo = hid < 192 ? hid * 16 : OOB;
    const u32x4 Ma = ld_nt(mk(w1, F * 2), mo, 0), Mb = ld_nt(mk(b1, F * 2), mo, 0), Mc = ld_nt(mk(w2, F * 2), mo, 0);
    const unsigned b2v = __builtin_amdgcn_raw_buffer_load_b16(mk(b2, 2), 0, 0, 2);
    PIN();
    WG_BAR();
    asm volatile("" : "+v"(ep));
    E1 = ((unsigned long long)__builtin_amdgcn_readfirstlane(ep[1]) << 32 | (unsigned)__builtin_amdgcn_readfirstlane(ep[0])) + 1;
    const Tag tg = make_tag(E1);
    if (hid == 0) *(LAS u32x2*)(lds + L_CNT + 10) = u32x2{(unsigned)E1, (unsigned)(E1 >> 32)};
    if (has_q) {
      float sa = 0.f, sb = 0.f;
#pragma unroll
      for (int i = 0; i < 20; i += 2) { sa = dot2(X[i >> 2][i & 3], 0x3f803f80u, sa); sb = dot2(X[i >> 2][(i & 3) + 1], 0x3f803f80u, sb); }
      const float mean = sum16(sa + sb) * (1.f / D);
      f32x2 s2 = {0.f, 0.f}, s3 = s2;
#pragma unroll
      for (int i = 0; i < 20; i += 2) {
        const f32x2 d = up2(X[i >> 2][i & 3]) - mean, e = up2(X[i >> 2][(i & 3) + 1]) - mean;
        s2 = __builtin_elementwise_fma(d, d, s2); s3 = __builtin_elementwise_fma(e, e, s3);
      }
      s2 += s3;
      const float inv = inv_sd(sum16(s2[0] + s2[1]) * (1.f / D)), mb = rbf(mean);
      LAS unsigned* xn = lds + L_XN + hw * 4 * 132;
#pragma unroll
      for (int i = 0; i < 2; ++i) {
        u32x4 o;
#pragma unroll
        for (int k = 0; k < 4; ++k) { const f32x2 y = rbf2(up2(X[i][k]) - mb) * inv; o[k] = pk2(y[0], y[1]); }
        *(LAS u32x4*)(xn + (lane >> 4) * 132 + ((lane & 15) + 16 * i) * 4) = o;
      }
      asm volatile("" ::: "memory");
      u32x4 af[5];
#pragma unroll
      for (int ks = 0; ks < 5; ++ks) af[ks] = *(LAS const u32x4*)(xn + (lane & 3) * 132 + ks * 16 + (lane >> 4) * 4);
      LAS unsigned* st = lds + L_WO + hw * 64 * 36;
      f32x4 dq[4];
#pragma unroll
      for (int nt = 0; nt < 4; ++nt) dq[nt] = f32x4{0.f, 0.f, 0.f, 0.f};
#pragma unroll
      for (int ks = 0; ks < 5; ++ks) {
        LAS unsigned* sk = st + (ks & 1) * 32 * 36;
#pragma unroll
        for (int i = 0; i < 4; ++i) *(LAS u32x4*)(sk + (8 * i + (lane >> 3)) * 36 + c8 * 4) = W[4 * ks + i];
        asm volatile("" ::: "memory");
        LAS const unsigned* rd = sk + (8 * (lane >> 4) + ((lane >> 2) & 3)) * 36 + (lane & 3) * 2;
#pragma unroll
        for (int nt = 0; nt < 4; ++nt) dq[nt] = __builtin_amdgcn_mfma_f32_16x16x32_bf16(__builtin_bit_cast(bf16x8, af[ks]), tr8(rd + nt * 8, 4 * 36), dq[nt], 0, 0, 0);
        asm volatile("" ::: "memory");
      }
      const bool g1 = lane & 16, g2 = lane & 32;
#pragma unroll
      for (int nt = 0; nt < 4; ++nt) {
        const float a01 = g1 ? dq[nt][1] : dq[nt][0], a23 = g1 ? dq[nt][3] : dq[nt][2];
        ldf[L_QR + hw * 256 + (lane >> 4) * 64 + nt * 16 + (lane & 15)] = g2 ? a23 : a01;
      }
      helper_bar(lds + L_CNT, 4, lane);
      if (hw == 0) {
        f32x4 a = *(LAS const f32x4*)(ldf + L_QR + 4 * lane);
#pragma unroll
        for (int ww = 1; ww < 4; ++ww) a += *(LAS const f32x4*)(ldf + L_QR + ww * 256 + 4 * lane);
        st_sc(wsr0, WS_QP + ((h * 4 + (lane >> 4)) * 96 + cq * 32 + (lane & 15) * 2) * 8, u32x4{pk2(a[0], a[1]), tg.t32, pk2(a[2], a[3]), tg.t32});
      }
    }
    STAMP(3);
    if (hw == 3) {
      if (QDELAY) while (now() - tstart < QDELAY) __builtin_amdgcn_s_sleep(2);
      const unsigned o = lane < 48 ? WS_QP + (h * 4 + b) * 96 * 8 + lane * 16 : OOB, vm = lane < 48 ? ~0u : 0u;
      u32x4 g;
#pragma nounroll
      for (unsigned spin = 0; spin < SPIN_CAP; ++spin) {
        asm volatile("" ::: "memory");
        g = ld_sc(wsr0, o);
        if (__all((((g[1] ^ tg.t32) | (g[3] ^ tg.t32)) & vm) == 0)) break;
        __builtin_amdgcn_s_sleep(QSLP);
      }
      if (lane < 48) *(LAS u32x2*)(lds + L_Q + 2 * lane) = u32x2{g[0], g[2]};
      asm volatile("s_waitcnt lgkmcnt(0)" ::: "memory");
      if (lane == 0) lds[L_CNT + 7] = 1;
      STAMP(1);
    } else {
      *(LAS u32x4*)(lds + L_MIX + hid * 4) = Ma; *(LAS u32x4*)(lds + L_MIX + 768 + hid * 4) = Mb; *(LAS u32x4*)(lds + L_MIX + 1536 + hid * 4) = Mc;
      const unsigned bits0 = (TAB0 + ((bid * 32) & 4095)) | (bid >> 7) << 15;
#pragma nounroll
      for (int i0 = 0; i0 < 32; i0 += 8) {
        float ac[8];
#pragma unroll
        for (int i = 0; i < 8; ++i) {
          const float t = __uint_as_float((bits0 + i0 + i) << 16);
          ac[i] = 0.f;
#pragma unroll
          for (int k = 0; k < 4; ++k) ac[i] = mix2(t, up2(Ma[k]), up2(Mb[k]), Mc[k], ac[i]);
        }
        float y = scatter8(ac, lane & 8);
        y += dpp<XOR1>(y); y += dpp<XOR2>(y); y += dpp<HMIR>(y);
        if ((lane & 7) == 0) ldf[L_RED + hw * 32 + i0 + (lane >> 3)] = y;
      }
      helper_bar(lds + L_CNT + 3, 3, lane);
      if (hw == 2 && lane < 8) {
        f32x4 y = *(LAS const f32x4*)(ldf + L_RED + 4 * lane);
#pragma unroll
        for (int ww = 1; ww < 3; ++ww) y += *(LAS const f32x4*)(ldf + L_RED + ww * 32 + 4 * lane);
        const f32x2 y0 = rbf2(f32x2{y[0], y[1]}) + bflo(b2v), y1 = rbf2(f32x2{y[2], y[3]}) + bflo(b2v);
        st_sc(wsr0, WS_TB + bid * 128 + lane * 16, u32x4{pk2(y0[0], y0[1]), tg.t32, pk2(y1[0], y1[1]), tg.t32});
      }
    }
    const rsrc_t wor = mk(wo, has_o ? WB : 0);
    u32x4 Wo[12];
#pragma unroll
    for (int i = 0; i < 12; ++i) Wo[i] = ld_nt(wor, (unsigned)rg * (D * 2) + c8 * 16, (unsigned)(hp * 2 * DH + 32 * i) * (D * 2) + co * 128);
    if (hid < 80) *(LAS u32x4*)(lds + L_X + hid * 4) = __builtin_nontemporal_load((const u32x4*)(x + b2i * D + hid * 8));
    if (hid == 80) lds[L_X + 320] = b2v;
#pragma unroll
    for (int i = 0; i < 12; ++i) *(LAS u32x4*)(lds + L_WO + (rg + 32 * i) * 36 + c8 * 4) = Wo[i];
    helper_bar(lds + L_CNT, has_q ? 8 : 4, lane);
    LAS const unsigned* wl = lds + L_WO + (8 * (lane >> 4) + ((lane >> 2) & 3)) * 36 + hw * 8 + (lane & 3) * 2;
#pragma unroll
    for (int ks = 0; ks < 12; ++ks) Bf[ks] = tr8(wl + ks * 32 * 36, 4 * 36);
    a_p1 = lane <= 48 ? WS_P1 + (h * 8 + j) * P1REC + lane * 16 : OOB;
    a_p2 = (lane & 12) ? OOB : WS_P2 + (((lane & 3) * 16 + hp) * D + co * 64 + hw * 16 + (lane >> 4) * 4) * 4;
    a_rec = ldf + L_REC + 4 * (lane < 48 ? lane : 48);
    a_o = lds + L_O + (lane & 3) * OSTR + (lane >> 4) * 4;
    asm volatile("" : "+v"(a_p1), "+v"(a_p2), "+v"(a_rec), "+v"(a_o));
    if (hw < 2) {
      while (lds_peek(lds + L_CNT + 6) == 0) __builtin_amdgcn_s_sleep(16);
#pragma nounroll
      for (int half = 0; half < 2; ++half) {
        u32x4 g[8];
#pragma nounroll
        for (unsigned spin = 0; spin < SPIN_CAP; ++spin) {
          asm volatile("" ::: "memory");
          unsigned bad = 0;
#pragma unroll
          for (int i = 0; i < 8; ++i) g[i] = ld_sc(wsr0, WS_TB + hid * 16, (half * 8 + i) * 2048);
#pragma unroll
          for (int i = 0; i < 8; ++i) bad |= (g[i][1] ^ tg.t32) | (g[i][3] ^ tg.t32);
          if (__all(bad == 0)) break;
          __builtin_amdgcn_s_sleep(32);
        }
#pragma unroll
        for (int i = 0; i < 8; ++i) *(LAS u32x2*)(lds + L_TAB + ((half * 8 + i) * 128 + hid) * 2) = u32x2{g[i][0], g[i][2]};
      }
    }
    if (hw == 2) {
      unsigned tm0 = 0;
#pragma nounroll
      for (int ck = 0; ck < 3; ++ck) {
        unsigned t2;
        while ((t2 = lds_peek(lds + L_CNT + (ck < 2 ? 1 + ck : 8))) == 0) __builtin_amdgcn_s_sleep(8);
        if ((XMAP == 2 ? ((bid >> 4) & 3) == (bid >> 6) : XMAP ? (h & 3) == (j & 3) : (bid & 3) == ((h >> 1) & 3)) && lane == 0)
          __builtin_amdgcn_raw_buffer_store_b64(u32x2{t2, tg.t32}, wsr0, WS_TS + (ck * 64 + (XMAP == 2 ? (bid & 15) * 4 + (bid >> 6) : XMAP ? (bid & 7) * 8 + j : bid >> 2)) * 128, 0, 16);
        while (now() - t2 < WAITT) __builtin_amdgcn_s_sleep(8);
        const u32x2 g = __builtin_amdgcn_raw_buffer_load_b64(wsr0, WS_TS + (ck * 64 + lane) * 128, 0, 16);
        const unsigned tm = t2 + (int)(sum64(g[1] == tg.t32 ? (float)min(max((int)(g[0] - t2), -2000), 2000) : (float)(WAITT + 30)) * (1.f / 64));
        const float left = ck == 1 ? (63.f - CK2) / (CK2 - CK1) * (1.f - EPS) : (63.f - CK3) / (CK3 - CK2) * (1.f - EPS3);
        if (ck && lane == 0) lds[L_CNT + 4] = (tm + (int)((float)(int)(tm - tm0) * left)) | 1u;
        tm0 = tm;
      }
    }
  }
  const Tag tg = make_tag(E1);
  const rsrc_t wsr = wsr0;
  WG_BAR();
  STAMP(2);
  if (w == 4) {
    float mm[4];
    f32x4 rv[4];
#pragma unroll
    for (int i = 0; i < 4; ++i) { mm[i] = ldf[L_REC + i * 196 + 192]; rv[i] = *(LAS const f32x4*)(a_rec + i * 196); }
    __builtin_amdgcn_sched_barrier(0);
    const float M = fmaxf(fmaxf(mm[0], mm[1]), fmaxf(mm[2], mm[3]));
    f32x4 a = {0.f, 0.f, 0.f, 0.f};
#pragma unroll
    for (int i = 0; i < 4; ++i) a += __builtin_amdgcn_exp2f((mm[i] - M) * L2E) * rv[i];
    if (lane >= 48) a = f32x4{M, a[1], 0.f, 0.f};
    st_sc(wsr, a_p1, enc4(a, tg));
    STAMP(1);
  }
  LAS const unsigned* own = lds + L_HH + cg * 5 + (lane < 5 ? lane : 0);
  unsigned a_out = lane < 5 ? (b2i * D + cg * 10 + 2 * lane) * 2 : OOB;
  asm volatile("" : "+v"(own), "+v"(a_out));
  const unsigned tcom = lds_peek(lds + L_CNT + 4);
  if (has_o) {
    {
      unsigned oa = lane <= 48 ? WS_P1 + (hp * 16 + (w >> 2) * 8 + 2 * (w & 3)) * P1REC + lane * 16 : OOB;
      unsigned vm = lane <= 48 ? 0xffu : 0u;
      LAS unsigned* da = lane < 48 ? lds + L_O + (w & 3) * OSTR + (w >> 2) * 96 + 2 * lane : lds + L_QR + 2 * lane;
      asm volatile("" : "+v"(da), "+v"(oa), "+v"(vm));
      u32x4 ga, gb;
      if (C1) while ((int)(now() - tcom) < C1) { }
#pragma nounroll
      for (unsigned spin = 0; spin < SPIN_CAP; ++spin) {
        asm volatile("" ::: "memory");
        ga = untag(ld_sc(wsr, oa), tg); gb = untag(ld_sc(wsr, oa + P1REC), tg);
        if (__all(((any4(ga) | any4(gb)) & vm) == 0)) break;
        __builtin_amdgcn_s_sleep(1);
      }
      STAMP(4);
      const float m0 = __uint_as_float(__builtin_amdgcn_readlane(ga[0], 48)), m1 = __uint_as_float(__builtin_amdgcn_readlane(gb[0], 48)), M = fmaxf(m0, m1);
      const float l0 = __uint_as_float(__builtin_amdgcn_readlane(ga[1], 48)), l1 = __uint_as_float(__builtin_amdgcn_readlane(gb[1], 48));
      float w0 = __builtin_amdgcn_exp2f((m0 - M) * L2E), w1_ = __builtin_amdgcn_exp2f((m1 - M) * L2E);
      const float inv = __builtin_amdgcn_rcpf(fmaf(w0, l0, w1_ * l1));
      w0 *= inv; w1_ *= inv;
      const f32x4 o = __builtin_elementwise_fma(asf4(ga), (f32x4)w0, asf4(gb) * w1_);
      *(LAS u32x2*)da = u32x2{pk2(o[0], o[1]), pk2(o[2], o[3])};
    }
    WG_BAR();
    if (w >= 4) {
      u32x4 af[12];
#pragma unroll
      for (int ks = 0; ks < 12; ++ks) af[ks] = *(LAS const u32x4*)(a_o + ks * 16);
      __builtin_amdgcn_sched_barrier(0);
      f32x4 d0 = {0.f, 0.f, 0.f, 0.f}, d1 = d0;
#pragma unroll
      for (int ks = 0; ks < 12; ks += 2) {
        d0 = __builtin_amdgcn_mfma_f32_16x16x32_bf16(Bf[ks], __builtin_bit_cast(bf16x8, af[ks]), d0, 0, 0, 0);
        d1 = __builtin_amdgcn_mfma_f32_16x16x32_bf16(Bf[ks + 1], __builtin_bit_cast(bf16x8, af[ks + 1]), d1, 0, 0, 0);
      }
      st_sc(wsr, a_p2, enc4(d0 + d1, tg));
      if (w != 6) STAMP(3);
    }
  }
  if (w < 5) {
    unsigned o = WS_P2 + (b2i * 16 + (lane >> 5) * 8) * D * 4 + (w * 32 + (lane & 31)) * 16, o4 = o + 4 * D * 4;
    const int pr = (w * 32 + (lane & 31)) * 2 + (lane >> 5);
    f32x2 xr = up2(lds[L_X + pr]);
    LAS unsigned* dh = lds + L_HH + pr;
    asm volatile("" : "+v"(xr), "+v"(dh), "+v"(o), "+v"(o4));
    f32x4 y;
    if (C2) while ((int)(now() - tcom) < C2) { }
#pragma nounroll
    for (unsigned spin = 0; spin < SPIN_CAP; ++spin) {
      asm volatile("" ::: "memory");
      u32x4 g[8];
#pragma unroll
      for (int i = 0; i < 8; ++i) g[i] = ld_sc(wsr, i < 4 ? o : o4, (i & 3) * D * 4);
      unsigned bad = 0;
      y = f32x4{0.f, 0.f, 0.f, 0.f};
#pragma unroll
      for (int i = 0; i < 8; ++i) { const u32x4 x = untag(g[i], tg); bad |= any4(x); y += asf4(x); asm volatile("" : "+v"(y), "+v"(bad)); }
      if (__all((bad & 0xffu) == 0)) break;
      __builtin_amdgcn_s_sleep(1);
    }
    STAMP(5);
    const f32x2 hs = xr + rbf2(f32x2{sw32(y[0], y[2]), sw32(y[1], y[3])});
    const unsigned hp2 = pk2(hs[0], hs[1]);
    *dh = hp2;
    const f32x2 hv = up2(hp2);
    const float piv = __uint_as_float(__builtin_amdgcn_readfirstlane(__float_as_uint(hv[0])));
    const f32x2 d = hv - piv, dd = d * d;
    const float sp = sw32(d[0] + d[1], dd[0] + dd[1]), sq = sum16(sw16(sp, sp));
    const float s1 = __uint_as_float(__builtin_amdgcn_readlane(__float_as_uint(sq), 0)), s2 = __uint_as_float(__builtin_amdgcn_readlane(__float_as_uint(sq), 32));
    *(LAS f32x2*)(ldf + L_ST + 2 * w) = f32x2{fmaf(s1, 1.f / 128, piv), fmaf(-s1, s1 * (1.f / 128), s2)};
  }
  WG_BAR();
  if (w == 0) {
    const f32x4 sa = *(LAS const f32x4*)(ldf + L_ST), sb = *(LAS const f32x4*)(ldf + L_ST + 4);
    const f32x2 sc = *(LAS const f32x2*)(ldf + L_ST + 8);
    const unsigned hw2 = *own;
    __builtin_amdgcn_sched_barrier(0);
    const f32x2 hv = up2(hw2);
    const float mean = (((sa[0] + sa[2]) + (sb[0] + sb[2])) + sc[0]) * 0.2f;
    const float e0 = sa[0] - mean, e1 = sa[2] - mean, e2 = sb[0] - mean, e3 = sb[2] - mean, e4 = sc[0] - mean;
    const float var = fmaf(fmaf(e0, e0, e1 * e1) + fmaf(e2, e2, e3 * e3) + e4 * e4, 0.2f, (((sa[1] + sa[3]) + (sb[1] + sb[3])) + sc[1]) * (1.f / D));
    const float inv = inv_sd(var), mb = rbf(mean);
    const float inv2 = inv_sd(var * inv * inv), mb2 = rbf((mean - mb) * inv);
    const f32x2 t0 = rbf2(rbf2(rbf2(hv - mb) * inv) - mb2) * inv2;
    STAMP(6);
    const unsigned tb = pk2(t0[0], t0[1]), i0 = (tb & 0x7fffu) - TAB0, i1 = ((tb >> 16) & 0x7fffu) - TAB0;
    const rsrc_t outr = mk(out, 4 * D * 2);
    LAS const unsigned short* tab = (LAS const unsigned short*)(lds + L_TAB);
    f32x2 y = {bflo(tab[(i0 & 4095u) + ((tb >> 3) & 4096u)]), bflo(tab[(i1 & 4095u) + ((tb >> 19) & 4096u)])};
    if (!__all((i0 | i1) < 4096u) || FORCE_FB) {
      const float bias = bflo(lds[L_X + 320]);
#pragma nounroll
      for (int i = 0; i < 10; ++i) {
        const float t = __shfl(i & 1 ? bfhi(tb) : bflo(tb), i >> 1);
        float a = 0.f;
#pragma nounroll
        for (int k = 0; k < 12; ++k) a = mix2(t, up2(lds[L_MIX + lane + 64 * k]), up2(lds[L_MIX + 768 + lane + 64 * k]), lds[L_MIX + 1536 + lane + 64 * k], a);
        a = rbf(rbf(sum64(a)) + bias);
        if (lane == (i >> 1)) { if (i & 1) y[1] = a; else y[0] = a; }
      }
    }
    const f32x2 o = hv + y;
    __builtin_amdgcn_raw_buffer_store_b32(pk2(o[0], o[1]), outr, a_out, 0, 16);
  }
  if (bid == 0 && tid == 64) __builtin_amdgcn_raw_buffer_store_b64(u32x2{(unsigned)E1, (unsigned)(E1 >> 32)}, wsr, 0, 0, 16);
}

extern "C" void kernel_launch(void* const* d_in, const int* in_sizes, int n_in, void* d_out, int out_size, void* d_ws, size_t ws_size,
                              hipStream_t stream) {
  typedef const unsigned short* P;
  fused_decode<<<256, 512, 0, stream>>>((P)d_in[0], (P)d_in[1], (P)d_in[2], (P)d_in[3], (P)d_in[4], (P)d_in[6], (P)d_in[7], (P)d_in[8], (P)d_in[9],
                                        (unsigned short*)d_out, (unsigned char*)d_ws);
}
```

```cpp
#include <hip/hip_runtime.h>
#include <stdint.h>

typedef unsigned u32x4 __attribute__((ext_vector_type(4)));
typedef unsigned u32x2 __attribute__((ext_vector_type(2)));
typedef float f32x4 __attribute__((ext_vector_type(4)));
typedef float f32x2 __attribute__((ext_vector_type(2)));
typedef short s16x4 __attribute__((ext_vector_type(4)));
typedef __bf16 bf16x2 __attribute__((ext_vector_type(2)));
typedef __bf16 bf16x8 __attribute__((ext_vector_type(8)));
typedef __amdgpu_buffer_rsrc_t rsrc_t;
#define LAS __attribute__((address_space(3)))

constexpr int D = 640, H = 32, SK = 4096, DH = 192, HD = H * DH, F = 1536;
constexpr unsigned SEQB = SK * DH * 2, KVB = 4u * H * SEQB, WB = D * HD * 2;
constexpr float L2E = 1.4426950408889634f, SCALE = 0.07216878364870322f;

constexpr int NT = 64, SW = NT / 2 + 1;
constexpr int NPRE = 8;
constexpr int CK1 = 15, CK2 = 27, CK3 = 45;
constexpr int CKG = 29;
constexpr float EPS2 = -0.002f, EPS3 = -0.023f;
constexpr unsigned WAITT = 400;
constexpr int STAG = 85;
constexpr unsigned QDELAY = 600;
constexpr int C1 = 185, C2 = 376;
static_assert(NPRE < CK1 && CK1 < CK2 && CK2 < SW && NPRE < CKG && CKG < SW && SW + 2 <= CK3 && CK3 < NT - 2 && (CK3 - SW) % 2 == 0,
              "every stamp that a helper wave waits for is reached");

constexpr unsigned P1REC = 896;
constexpr unsigned WS_TS = 4096, WS_TB = WS_TS + 3 * 64 * 128, WS_QP = WS_TB + 256 * 128, WS_P1 = WS_QP + H * 4 * 96 * 8, WS_P2 = WS_P1 + 256 * P1REC,
                   WS_END = WS_P2 + 4 * 16 * D * 4;
constexpr unsigned OOB = 0x7ffffff0u;
constexpr unsigned SPIN_CAP = 1u << 17;
constexpr unsigned TAB0 = 100u << 7;

constexpr int OSTR = 208;
constexpr int S_BAR = 0, S_T1 = 1, S_T2 = 2, S_BAR3 = 3, S_END = 4, S_T3 = 5, S_TAB = 6, S_Q = 7, S_TAG = 8;
constexpr int L_CNT = 0, L_Q = 12, L_REC = L_Q + 96, L_O = L_REC + 4 * 196, L_RED = L_O + 4 * OSTR, L_ST = L_RED + 96, L_X = L_ST + 16, L_HH = L_X + 324,
              L_XN = L_HH + 320, L_QR = L_XN + 16 * 132, L_MIX = L_QR + 4 * 256, L_WO = L_MIX + 3 * F / 2, L_TAB = L_WO + 2 * DH * 36, L_SC = L_TAB + 4096,
              L_END = L_SC + 2048;
static_assert(L_END * 4 > 80 * 1024 && L_REC % 4 == 0 && L_O % 4 == 0 && L_RED % 4 == 0 && L_ST % 4 == 0 && L_X % 4 == 0 && L_XN % 4 == 0 && L_QR % 4 == 0 &&
              L_MIX % 4 == 0 && L_WO % 4 == 0 && L_TAB % 2 == 0, "aligned regions, one workgroup per CU");

__device__ __forceinline__ float bflo(unsigned w) { return __uint_as_float(w << 16); }
__device__ __forceinline__ float bfhi(unsigned w) { return __uint_as_float(w & 0xffff0000u); }
__device__ __forceinline__ unsigned pk2(float lo, float hi) { unsigned r; asm("v_cvt_pk_bf16_f32 %0, %1, %2" : "=v"(r) : "v"(lo), "v"(hi)); return r; }
__device__ __forceinline__ float rbf(float f) { return (float)(__bf16)f; }
__device__ __forceinline__ f32x2 up2(unsigned w) { return f32x2{bflo(w), bfhi(w)}; }
__device__ __forceinline__ f32x2 rbf2(f32x2 v) { return up2(pk2(v[0], v[1])); }
__device__ __forceinline__ float dot2(unsigned a, unsigned b, float c) {
  return __builtin_amdgcn_fdot2_f32_bf16(__builtin_bit_cast(bf16x2, a), __builtin_bit_cast(bf16x2, b), c, false);
}
template <int CTRL> __device__ __forceinline__ float dpp(float x) {
  return __builtin_bit_cast(float, __builtin_amdgcn_mov_dpp(__builtin_bit_cast(int, x), CTRL, 0xf, 0xf, true));
}
constexpr int XOR1 = 0xB1, XOR2 = 0x4E, HMIR = 0x141, ROR8 = 0x128;
__device__ __forceinline__ float sw32(float a, float b) {
  const auto r = __builtin_amdgcn_permlane32_swap(__float_as_uint(a), __float_as_uint(b), false, false);
  return __uint_as_float(r[0]) + __uint_as_float(r[1]);
}
__device__ __forceinline__ float sw16(float a, float b) {
  const auto r = __builtin_amdgcn_permlane16_swap(__float_as_uint(a), __float_as_uint(b), false, false);
  return __uint_as_float(r[0]) + __uint_as_float(r[1]);
}
__device__ __forceinline__ float sum16(float v) { v += dpp<XOR1>(v); v += dpp<XOR2>(v); v += dpp<HMIR>(v); v += dpp<ROR8>(v); return v; }
__device__ __forceinline__ float sum64(float v) { v = sum16(v); v = sw16(v, v); return sw32(v, v); }
__device__ __forceinline__ float scatter8(const float* a, bool b3) {
  float y[4], z[2];
#pragma unroll
  for (int e = 0; e < 4; ++e) y[e] = sw32(a[e], a[e + 4]);
#pragma unroll
  for (int e = 0; e < 2; ++e) z[e] = sw16(y[e], y[e + 2]);
  return (b3 ? z[1] : z[0]) + dpp<ROR8>(b3 ? z[0] : z[1]);
}
__device__ __forceinline__ bf16x8 tr8(LAS const unsigned* p, int step) {
  const s16x4 a = __builtin_amdgcn_ds_read_tr16_b64_v4i16((LAS s16x4*)p), b = __builtin_amdgcn_ds_read_tr16_b64_v4i16((LAS s16x4*)(p + step));
  return __builtin_bit_cast(bf16x8, __builtin_shufflevector(a, b, 0, 1, 2, 3, 4, 5, 6, 7));
}
__device__ __forceinline__ rsrc_t mk(const void* p, unsigned n) { return __builtin_amdgcn_make_buffer_rsrc((void*)p, 0, n, 0x00020000); }
__device__ __forceinline__ u32x4 ld_nt(rsrc_t r, unsigned v, unsigned s) { return __builtin_amdgcn_raw_buffer_load_b128(r, v, s, 2); }
__device__ __forceinline__ u32x4 ld_sc(rsrc_t r, unsigned v, unsigned s = 0) { return __builtin_amdgcn_raw_buffer_load_b128(r, v, s, 16); }
__device__ __forceinline__ void st_sc(rsrc_t r, unsigned v, u32x4 x) { __builtin_amdgcn_raw_buffer_store_b128(x, r, v, 0, 16); }
__device__ __forceinline__ unsigned now() { return (unsigned)__builtin_amdgcn_s_memrealtime(); }
__device__ __forceinline__ unsigned lds_peek(LAS unsigned* p) {
  return __builtin_amdgcn_readfirstlane(__hip_atomic_load(p, __ATOMIC_RELAXED, __HIP_MEMORY_SCOPE_WORKGROUP));
}
#define PIN() do { asm volatile("" ::: "memory"); __builtin_amdgcn_sched_barrier(0); } while (0)
#define WG_BAR() do { asm volatile("s_waitcnt lgkmcnt(0)" ::: "memory"); __builtin_amdgcn_s_barrier(); PIN(); } while (0)
__device__ __forceinline__ void helper_bar(LAS unsigned* cnt, unsigned target, int lane) {
  asm volatile("s_waitcnt lgkmcnt(0)" ::: "memory");
  if (lane == 0) __hip_atomic_fetch_add(cnt, 1u, __ATOMIC_RELAXED, __HIP_MEMORY_SCOPE_WORKGROUP);
  while (lds_peek(cnt) < target) { }
  asm volatile("" ::: "memory");
}

struct Tag { unsigned b0, b1, b2, b3, t32; };
__device__ __forceinline__ Tag make_tag(unsigned t) { return Tag{t & 0xffu, (t + (t >> 8)) & 0xffu, (t + (t >> 16)) & 0xffu, (t + (t >> 24)) & 0xffu, t}; }
__device__ __forceinline__ u32x4 enc4(f32x4 v, const Tag& t) {
  return u32x4{(__float_as_uint(v[0]) & 0xffffff00u) | t.b0, (__float_as_uint(v[1]) & 0xffffff00u) | t.b1, (__float_as_uint(v[2]) & 0xffffff00u) | t.b2,
               (__float_as_uint(v[3]) & 0xffffff00u) | t.b3};
}
__device__ __forceinline__ u32x4 untag(u32x4 g, const Tag& t) { return g ^ u32x4{t.b0, t.b1, t.b2, t.b3}; }
__device__ __forceinline__ unsigned any4(u32x4 x) { return (x[0] | x[1]) | (x[2] | x[3]); }
__device__ __forceinline__ f32x4 asf4(u32x4 x) { return __builtin_bit_cast(f32x4, x); }

__device__ __forceinline__ float mix2(float t, f32x2 wa, f32x2 wb, unsigned wc, float acc) {
  constexpr float K0 = -2.f * L2E * 0.7978845608028654f, K1 = K0 * 0.044715f;
  const f32x2 u = rbf2(rbf2(wa * t) + wb);
  const f32x2 z = u * __builtin_elementwise_fma(u * u, (f32x2)K1, (f32x2)K0);
  const f32x2 d = f32x2{__builtin_amdgcn_exp2f(z[0]), __builtin_amdgcn_exp2f(z[1])} + 1.f;
  const f32x2 g = u * f32x2{__builtin_amdgcn_rcpf(d[0]), __builtin_amdgcn_rcpf(d[1])};
  return dot2(pk2(g[0], g[1]), wc, acc);
}
__device__ __forceinline__ float inv_sd(float var) { return __builtin_amdgcn_rcpf(rbf(__builtin_amdgcn_sqrtf(rbf(rbf(var) + 1e-5f)))); }

__global__ void __launch_bounds__(512, 2)
fused_decode(const unsigned short* x, const unsigned short* kc, const unsigned short* vc, const unsigned short* wq, const unsigned short* wo,
             const unsigned short* w1, const unsigned short* b1, const unsigned short* w2, const unsigned short* b2, unsigned short* out,
             unsigned char* ws) {
  __shared__ __attribute__((aligned(16))) unsigned lds_raw[L_END];
  LAS unsigned* lds = (LAS unsigned*)lds_raw;
  LAS float* ldf = (LAS float*)lds_raw;
  const int tid = threadIdx.x, lane = tid & 63, w = __builtin_amdgcn_readfirstlane(tid >> 6);
  const int bid = blockIdx.x, h = ((bid >> 1) & 3) * 8 + ((bid >> 3) & 7), j = (bid >> 6) * 2 + ((bid ^ h) & 1), b = j >> 1, s = j & 1;
  const bool has_q = j >= 2 && s == 1, has_o = !has_q;
  const int hp = h >> 1, co = (h & 1) * 5 + (j < 2 ? j : b + 1);
  const int b2i = bid >> 6, cg = bid & 63;
  const unsigned tstart = now();
  asm volatile("" :: "s"(x), "s"(kc), "s"(vc), "s"(wq), "s"(wo), "s"(w1), "s"(b1), "s"(w2), "s"(b2), "s"(out), "s"(ws));
  const rsrc_t wsr = mk(ws, WS_END);
  bf16x8 Bf[12];
  unsigned a_p1, a_p2, tagw;
  LAS const float* a_rec;
  LAS const unsigned* a_o;

  if (__builtin_expect(w < 4, 1)) {
    __builtin_amdgcn_s_setprio(2);
    const int r = lane >> 3, c = lane & 7;
    const bool hi = r >> 2;
    const unsigned kb = hi ? 86 : 0, c1 = hi ? 85 : 86, d2 = hi ? 85 : 86;
    const unsigned seq = (unsigned)(b * H + h) * SEQB + s * (SEQB / 2);
    auto req = [&](u32x4 (&X)[3], int u, int hf) {
      const rsrc_t q = mk(u < NT / 2 ? kc : vc, KVB);
      const unsigned mm = ((32 * u + 16 * hf + 4 * w) & 255) + (r & 3);
      const unsigned av = (mm + kb + (mm >= c1 ? 85 : 0) + (mm >= 171 ? d2 : 0)) * 384 + c * 16, as = seq + (unsigned)((u >> 3) & 3) * (512 * 384);
#pragma unroll
      for (int i = 0; i < 3; ++i) X[i] = ld_nt(q, av + 128 * i, as);
      PIN(); };
    u32x4 T[NPRE][2][3];
#pragma unroll
    for (int p = 0; p < NPRE; ++p) {
      if (p == 1) WG_BAR();
#pragma unroll
      for (int hf = 0; hf < 2; ++hf) {
        if (p) asm volatile("s_waitcnt vmcnt(3)" ::: "memory");
        req(T[p][hf], p, hf);
      }
    }
    while (lds_peek(lds + L_CNT + S_Q) == 0) __builtin_amdgcn_s_sleep(1);
    PIN();
    u32x4 qf[3];
#pragma unroll
    for (int i = 0; i < 3; ++i) qf[i] = *(LAS const u32x4*)(lds + L_Q + (c + 8 * i) * 4);
    float m = __uint_as_float(0xff7f0000u);
    LAS float* sl = ldf + L_SC + w * 512 + r;
    auto kstep = [&](u32x4 (&X)[2][3], int u, bool refill, int un) {
      float a[2];
#pragma unroll
      for (int hf = 0; hf < 2; ++hf) {
        float a0 = 0.f, a1 = 0.f, a2 = 0.f;
#pragma unroll
        for (int k = 0; k < 4; ++k) { a0 = dot2(X[hf][0][k], qf[0][k], a0); a1 = dot2(X[hf][1][k], qf[1][k], a1); a2 = dot2(X[hf][2][k], qf[2][k], a2); }
        a[hf] = (a0 + a1) + a2;
        asm volatile("" : "+v"(a[hf]));
        PIN();
        if (refill) req(X[hf], un, hf);
      }
#pragma unroll
      for (int hf = 0; hf < 2; ++hf) {
        float y = a[hf];
        y += dpp<XOR1>(y); y += dpp<XOR2>(y); y += dpp<HMIR>(y);
        y = rbf(rbf(y) * SCALE);
        m = fmaxf(m, y);
        sl[u * 16 + hf * 8] = y;
      }
      PIN();
    };
#pragma unroll
    for (int p = 0; p < NPRE; ++p) kstep(T[p], p, p == 0, NPRE);
    int t = NPRE + 1;
    do {
      if (w == 1 && t == CKG && lane == 0) lds[L_CNT + S_TAB] = 1;
      if (w == 0 && (t == CK1 || t == CK2)) { const unsigned tn = now() | 1u; if (lane == 0) lds[L_CNT + (t == CK1 ? S_T1 : S_T2)] = tn; }
      kstep(T[0], t - 1, true, t);
    } while (++t < SW);
    m = fmaxf(m, dpp<ROR8>(m));
    { const auto q16 = __builtin_amdgcn_permlane16_swap(__float_as_uint(m), __float_as_uint(m), false, false);
      m = fmaxf(__uint_as_float(q16[0]), __uint_as_float(q16[1]));
      const auto q32 = __builtin_amdgcn_permlane32_swap(__float_as_uint(m), __float_as_uint(m), false, false);
      m = fmaxf(__uint_as_float(q32[0]), __uint_as_float(q32[1])); }
    float l = 0.f;
    f32x2 acc[12];
#pragma unroll
    for (int k = 0; k < 12; ++k) acc[k] = f32x2{0.f, 0.f};
    bool stamp3 = false;
    auto vstep = [&](u32x4 (&X)[2][3], int u, bool refill, unsigned pace) {
      float p[2];
#pragma unroll
      for (int hf = 0; hf < 2; ++hf) { p[hf] = __builtin_amdgcn_exp2f((sl[(u - NT / 2) * 16 + hf * 8] - m) * L2E); l += p[hf]; }
      asm volatile("" : "+v"(p[0]), "+v"(p[1]));
      PIN();
#pragma unroll
      for (int hf = 0; hf < 2; ++hf) {
        f32x2 vf[12];
#pragma unroll
        for (int k = 0; k < 12; ++k) vf[k] = up2(X[hf][k >> 2][k & 3]);
        if (refill) {
          asm volatile("" : "+v"(vf[0]), "+v"(vf[1]), "+v"(vf[2]), "+v"(vf[3]), "+v"(vf[4]), "+v"(vf[5]));
          asm volatile("" : "+v"(vf[6]), "+v"(vf[7]), "+v"(vf[8]), "+v"(vf[9]), "+v"(vf[10]), "+v"(vf[11]));
          PIN();
          if (hf == 0) {
            asm volatile("s_waitcnt vmcnt(5)" ::: "memory");
            unsigned tn;
            while ((int)(((tn = now()) << 6) - pace) < 0) { }
            if (stamp3) { if (lane == 0) lds[L_CNT + S_T3] = tn | 1u; stamp3 = false; }
          }
          req(X[hf], u + 2, hf);
        }
#pragma unroll
        for (int k = 0; k < 12; ++k) acc[k] = __builtin_elementwise_fma(vf[k], (f32x2)p[hf], acc[k]);
#pragma unroll
        for (int k = 0; k < 12; k += 4) asm volatile("" : "+v"(acc[k]), "+v"(acc[k + 1]), "+v"(acc[k + 2]), "+v"(acc[k + 3]));
        PIN();
      }
    };
    unsigned tend;
    while ((tend = lds_peek(lds + L_CNT + S_END)) == 0) __builtin_amdgcn_s_sleep(1);
    tagw = lds_peek(lds + L_CNT + S_TAG);
    const int stag = (STAG * ((((bid * 37) & 63) + 64 * w) - 128) * 64) >> 8;
    unsigned tgt = now() << 6;
    int tau = (int)((tend << 6) + stag - tgt) / (NT - 1 - SW);
    req(T[1][0], t, 0); req(T[1][1], t, 1);
    do {
      const unsigned t3 = lds_peek(lds + L_CNT + S_END);
      if (t3 != tend) { tend = t3; tau = (int)((tend << 6) + stag - tgt) / (NT - 1 - t); }
      tgt += tau;
      vstep(T[0], t - 1, true, tgt);
      tgt += tau;
      if (w == 0 && t + 2 == CK3) stamp3 = true;
      vstep(T[1], t, true, tgt);
      t += 2;
    } while (t < NT - 1);
    LAS float* drec = ldf + L_REC + w * 196 + c * 8 + r;
    asm volatile("" : "+v"(drec));
    vstep(T[0], NT - 2, false, 0);
    vstep(T[1], NT - 1, false, 0);
    l += dpp<ROR8>(l); l = sw16(l, l); l = sw32(l, l);
    *(LAS f32x2*)(ldf + L_REC + w * 196 + 192) = f32x2{m, l};
#pragma unroll
    for (int i = 0; i < 3; ++i) {
      const float e[8] = {acc[4 * i][0], acc[4 * i][1], acc[4 * i + 1][0], acc[4 * i + 1][1], acc[4 * i + 2][0], acc[4 * i + 2][1], acc[4 * i + 3][0], acc[4 * i + 3][1]};
      drec[i * 64] = scatter8(e, lane & 8);
    }
  } else {
    const int hid = tid - 256, hw = w - 4;
    unsigned ep = __builtin_amdgcn_raw_buffer_load_b32(wsr, 0, 0, 16);
    if (hid < 12) lds[L_CNT + hid] = 0;
    const int cq = (j - 2) >> 1, rg = hid >> 3, c8 = hid & 7;
    u32x4 W[20], X[5];
    const rsrc_t xr = mk(x, has_q ? 4 * D * 2 : 0), wqr = mk(wq, has_q ? WB : 0), wbr = has_o ? mk(wo, WB) : wqr;
#pragma unroll
    for (int i = 0; i < 5; ++i) { const int ch = 20 * hw + (lane & 15) + 16 * i; X[i] = ld_nt(xr, (lane >> 4) * (D * 2) + (ch >= 80 ? ch - 80 : ch) * 16, 0); }
    PIN();
    const unsigned wv = has_o ? (unsigned)rg * (D * 2) + c8 * 16 : (unsigned)(lane >> 3) * (HD * 2) + c8 * 16;
    const unsigned ws0 = has_o ? (unsigned)(hp * 2 * DH) * (D * 2) + co * 128 : (unsigned)(160 * hw) * (HD * 2) + (h * DH + cq * 64) * 2;
    const unsigned wst = has_o ? 32 * D * 2 : 8 * HD * 2;
#pragma unroll
    for (int i = 0; i < 20; ++i) {
      W[i] = ld_nt(i < 12 ? wbr : wqr, wv, ws0 + i * wst);
      if ((i & 3) == 3) PIN();
    }
    const unsigned mo = hid < 192 ? hid * 16 : OOB;
    const u32x4 Ma = ld_nt(mk(w1, F * 2), mo, 0), Mb = ld_nt(mk(b1, F * 2), mo, 0), Mc = ld_nt(mk(w2, F * 2), mo, 0);
    const unsigned b2v = __builtin_amdgcn_raw_buffer_load_b16(mk(b2, 2), 0, 0, 2);
    PIN();
    WG_BAR();
    asm volatile("" : "+v"(ep));
    tagw = __builtin_amdgcn_readfirstlane(ep) + 1;
    const Tag tg = make_tag(tagw);
    if (hid == 0) lds[L_CNT + S_TAG] = tagw;
    if (has_o) {
#pragma unroll
      for (int i = 0; i < 12; ++i) *(LAS u32x4*)(lds + L_WO + (rg + 32 * i) * 36 + c8 * 4) = W[i];
    } else {
      float sa = 0.f, sb = 0.f;
#pragma unroll
      for (int i = 0; i < 20; i += 2) { sa = dot2(X[i >> 2][i & 3], 0x3f803f80u, sa); sb = dot2(X[i >> 2][(i & 3) + 1], 0x3f803f80u, sb); }
      const float mean = sum16(sa + sb) * (1.f / D);
      f32x2 s2 = {0.f, 0.f}, s3 = s2;
#pragma unroll
      for (int i = 0; i < 20; i += 2) {
        const f32x2 d = up2(X[i >> 2][i & 3]) - mean, e = up2(X[i >> 2][(i & 3) + 1]) - mean;
        s2 = __builtin_elementwise_fma(d, d, s2); s3 = __builtin_elementwise_fma(e, e, s3);
      }
      s2 += s3;
      const float inv = inv_sd(sum16(s2[0] + s2[1]) * (1.f / D)), mb = rbf(mean);
      LAS unsigned* xn = lds + L_XN + hw * 4 * 132;
#pragma unroll
      for (int i = 0; i < 2; ++i) {
        u32x4 o;
#pragma unroll
        for (int k = 0; k < 4; ++k) { const f32x2 y = rbf2(up2(X[i][k]) - mb) * inv; o[k] = pk2(y[0], y[1]); }
        *(LAS u32x4*)(xn + (lane >> 4) * 132 + ((lane & 15) + 16 * i) * 4) = o;
      }
      asm volatile("" ::: "memory");
      u32x4 af[5];
#pragma unroll
      for (int ks = 0; ks < 5; ++ks) af[ks] = *(LAS const u32x4*)(xn + (lane & 3) * 132 + ks * 16 + (lane >> 4) * 4);
      LAS unsigned* st = lds + L_WO + hw * 64 * 36;
      f32x4 dq[4];
#pragma unroll
      for (int nt = 0; nt < 4; ++nt) dq[nt] = f32x4{0.f, 0.f, 0.f, 0.f};
#pragma unroll
      for (int ks = 0; ks < 5; ++ks) {
        LAS unsigned* sk = st + (ks & 1) * 32 * 36;
#pragma unroll
        for (int i = 0; i < 4; ++i) *(LAS u32x4*)(sk + (8 * i + (lane >> 3)) * 36 + c8 * 4) = W[4 * ks + i];
        asm volatile("" ::: "memory");
        LAS const unsigned* rd = sk + (8 * (lane >> 4) + ((lane >> 2) & 3)) * 36 + (lane & 3) * 2;
#pragma unroll
        for (int nt = 0; nt < 4; ++nt) dq[nt] = __builtin_amdgcn_mfma_f32_16x16x32_bf16(__builtin_bit_cast(bf16x8, af[ks]), tr8(rd + nt * 8, 4 * 36), dq[nt], 0, 0, 0);
        asm volatile("" ::: "memory");
      }
      const bool g1 = lane & 16, g2 = lane & 32;
#pragma unroll
      for (int nt = 0; nt < 4; ++nt) {
        const float a01 = g1 ? dq[nt][1] : dq[nt][0], a23 = g1 ? dq[nt][3] : dq[nt][2];
        ldf[L_QR + hw * 256 + (lane >> 4) * 64 + nt * 16 + (lane & 15)] = g2 ? a23 : a01;
      }
      helper_bar(lds + L_CNT + S_BAR, 4, lane);
      if (hw == 0) {
        f32x4 a = *(LAS const f32x4*)(ldf + L_QR + 4 * lane);
#pragma unroll
        for (int ww = 1; ww < 4; ++ww) a += *(LAS const f32x4*)(ldf + L_QR + ww * 256 + 4 * lane);
        st_sc(wsr, WS_QP + ((h * 4 + (lane >> 4)) * 96 + cq * 32 + (lane & 15) * 2) * 8, u32x4{pk2(a[0], a[1]), tg.t32, pk2(a[2], a[3]), tg.t32});
      }
    }
    if (hw == 3) {
      while (now() - tstart < QDELAY) __builtin_amdgcn_s_sleep(2);
      const unsigned o = lane < 48 ? WS_QP + (h * 4 + b) * 96 * 8 + lane * 16 : OOB, vm = lane < 48 ? ~0u : 0u;
      u32x4 g;
#pragma nounroll
      for (unsigned spin = 0; spin < SPIN_CAP; ++spin) {
        asm volatile("" ::: "memory");
        g = ld_sc(wsr, o);
        if (__all((((g[1] ^ tg.t32) | (g[3] ^ tg.t32)) & vm) == 0)) break;
        __builtin_amdgcn_s_sleep(8);
      }
      if (lane < 48) *(LAS u32x2*)(lds + L_Q + 2 * lane) = u32x2{g[0], g[2]};
      asm volatile("s_waitcnt lgkmcnt(0)" ::: "memory");
      if (lane == 0) lds[L_CNT + S_Q] = 1;
    } else {
      *(LAS u32x4*)(lds + L_MIX + hid * 4) = Ma; *(LAS u32x4*)(lds + L_MIX + 768 + hid * 4) = Mb; *(LAS u32x4*)(lds + L_MIX + 1536 + hid * 4) = Mc;
      const unsigned bits0 = (TAB0 + ((bid * 32) & 4095)) | (bid >> 7) << 15;
#pragma nounroll
      for (int i0 = 0; i0 < 32; i0 += 8) {
        float ac[8];
#pragma unroll
        for (int i = 0; i < 8; ++i) {
          const float t = __uint_as_float((bits0 + i0 + i) << 16);
          ac[i] = 0.f;
#pragma unroll
          for (int k = 0; k < 4; ++k) ac[i] = mix2(t, up2(Ma[k]), up2(Mb[k]), Mc[k], ac[i]);
        }
        float y = scatter8(ac, lane & 8);
        y += dpp<XOR1>(y); y += dpp<XOR2>(y); y += dpp<HMIR>(y);
        if ((lane & 7) == 0) ldf[L_RED + hw * 32 + i0 + (lane >> 3)] = y;
      }
      helper_bar(lds + L_CNT + S_BAR3, 3, lane);
      if (hw == 2 && lane < 8) {
        f32x4 y = *(LAS const f32x4*)(ldf + L_RED + 4 * lane);
#pragma unroll
        for (int ww = 1; ww < 3; ++ww) y += *(LAS const f32x4*)(ldf + L_RED + ww * 32 + 4 * lane);
        const f32x2 y0 = rbf2(f32x2{y[0], y[1]}) + bflo(b2v), y1 = rbf2(f32x2{y[2], y[3]}) + bflo(b2v);
        st_sc(wsr, WS_TB + bid * 128 + lane * 16, u32x4{pk2(y0[0], y0[1]), tg.t32, pk2(y1[0], y1[1]), tg.t32});
      }
    }
    if (hid < 80) *(LAS u32x4*)(lds + L_X + hid * 4) = __builtin_nontemporal_load((const u32x4*)(x + b2i * D + hid * 8));
    if (hid == 80) lds[L_X + 320] = b2v;
    helper_bar(lds + L_CNT + S_BAR, has_q ? 8 : 4, lane);
    LAS const unsigned* wl = lds + L_WO + (8 * (lane >> 4) + ((lane >> 2) & 3)) * 36 + hw * 8 + (lane & 3) * 2;
#pragma unroll
    for (int ks = 0; ks < 12; ++ks) Bf[ks] = tr8(wl + ks * 32 * 36, 4 * 36);
    a_p1 = lane <= 48 ? WS_P1 + (h * 8 + j) * P1REC + lane * 16 : OOB;
    a_p2 = (lane & 12) ? OOB : WS_P2 + (((lane & 3) * 16 + hp) * D + co * 64 + hw * 16 + (lane >> 4) * 4) * 4;
    a_rec = ldf + L_REC + 4 * (lane < 48 ? lane : 48);
    a_o = lds + L_O + (lane & 3) * OSTR + (lane >> 4) * 4;
    asm volatile("" : "+v"(a_p1), "+v"(a_p2), "+v"(a_rec), "+v"(a_o));
    if (hw < 2) {
      while (lds_peek(lds + L_CNT + S_TAB) == 0) __builtin_amdgcn_s_sleep(16);
#pragma nounroll
      for (int half = 0; half < 2; ++half) {
        u32x4 g[8];
#pragma nounroll
        for (unsigned spin = 0; spin < SPIN_CAP; ++spin) {
          asm volatile("" ::: "memory");
          unsigned bad = 0;
#pragma unroll
          for (int i = 0; i < 8; ++i) g[i] = ld_sc(wsr, WS_TB + hid * 16, (half * 8 + i) * 2048);
#pragma unroll
          for (int i = 0; i < 8; ++i) bad |= (g[i][1] ^ tg.t32) | (g[i][3] ^ tg.t32);
          if (__all(bad == 0)) break;
          __builtin_amdgcn_s_sleep(32);
        }
#pragma unroll
        for (int i = 0; i < 8; ++i) *(LAS u32x2*)(lds + L_TAB + ((half * 8 + i) * 128 + hid) * 2) = u32x2{g[i][0], g[i][2]};
      }
    }
    if (hw == 2) {
      unsigned tm0 = 0;
#pragma nounroll
      for (int ck = 0; ck < 3; ++ck) {
        unsigned t2;
        while ((t2 = lds_peek(lds + L_CNT + (ck == 0 ? S_T1 : ck == 1 ? S_T2 : S_T3))) == 0) __builtin_amdgcn_s_sleep(8);
        if (((bid >> 4) & 3) == (bid >> 6) && lane == 0)
          __builtin_amdgcn_raw_buffer_store_b64(u32x2{t2, tg.t32}, wsr, WS_TS + (ck * 64 + (bid & 15) * 4 + (bid >> 6)) * 128, 0, 16);
        while (now() - t2 < WAITT) __builtin_amdgcn_s_sleep(8);
        const u32x2 g = __builtin_amdgcn_raw_buffer_load_b64(wsr, WS_TS + (ck * 64 + lane) * 128, 0, 16);
        const unsigned tm = t2 + (int)(sum64(g[1] == tg.t32 ? (float)min(max((int)(g[0] - t2), -2000), 2000) : (float)(WAITT + 30)) * (1.f / 64));
        const float left = ck == 1 ? (NT - 1.f - CK2) / (CK2 - CK1) * (1.f - EPS2) : (NT - 1.f - CK3) / (CK3 - CK2) * (1.f - EPS3);
        if (ck && lane == 0) lds[L_CNT + S_END] = (tm + (int)((float)(int)(tm - tm0) * left)) | 1u;
        tm0 = tm;
      }
    }
  }
  const Tag tg = make_tag(tagw);
  WG_BAR();
  if (w == 4) {
    float mm[4];
    f32x4 rv[4];
#pragma unroll
    for (int i = 0; i < 4; ++i) { mm[i] = ldf[L_REC + i * 196 + 192]; rv[i] = *(LAS const f32x4*)(a_rec + i * 196); }
    __builtin_amdgcn_sched_barrier(0);
    const float M = fmaxf(fmaxf(mm[0], mm[1]), fmaxf(mm[2], mm[3]));
    f32x4 a = {0.f, 0.f, 0.f, 0.f};
#pragma unroll
    for (int i = 0; i < 4; ++i) a += __builtin_amdgcn_exp2f((mm[i] - M) * L2E) * rv[i];
    if (lane >= 48) a = f32x4{M, a[1], 0.f, 0.f};
    st_sc(wsr, a_p1, enc4(a, tg));
  }
  LAS const unsigned* own = lds + L_HH + cg * 5 + (lane < 5 ? lane : 0);
  unsigned a_out = lane < 5 ? (b2i * D + cg * 10 + 2 * lane) * 2 : OOB;
  asm volatile("" : "+v"(own), "+v"(a_out));
  const unsigned tcom = lds_peek(lds + L_CNT + S_END);
  if (has_o) {
    {
      unsigned oa = lane <= 48 ? WS_P1 + (hp * 16 + (w >> 2) * 8 + 2 * (w & 3)) * P1REC + lane * 16 : OOB;
      unsigned vm = lane <= 48 ? 0xffu : 0u;
      LAS unsigned* da = lane < 48 ? lds + L_O + (w & 3) * OSTR + (w >> 2) * 96 + 2 * lane : lds + L_QR + 2 * lane;
      asm volatile("" : "+v"(da), "+v"(oa), "+v"(vm));
      u32x4 ga, gb;
      while ((int)(now() - tcom) < C1) { }
#pragma nounroll
      for (unsigned spin = 0; spin < SPIN_CAP; ++spin) {
        asm volatile("" ::: "memory");
        ga = untag(ld_sc(wsr, oa), tg); gb = untag(ld_sc(wsr, oa + P1REC), tg);
        if (__all(((any4(ga) | any4(gb)) & vm) == 0)) break;
        __builtin_amdgcn_s_sleep(1);
      }
      const float m0 = __uint_as_float(__builtin_amdgcn_readlane(ga[0], 48)), m1 = __uint_as_float(__builtin_amdgcn_readlane(gb[0], 48)), M = fmaxf(m0, m1);
      const float l0 = __uint_as_float(__builtin_amdgcn_readlane(ga[1], 48)), l1 = __uint_as_float(__builtin_amdgcn_readlane(gb[1], 48));
      float f0 = __builtin_amdgcn_exp2f((m0 - M) * L2E), f1 = __builtin_amdgcn_exp2f((m1 - M) * L2E);
      const float inv = __builtin_amdgcn_rcpf(fmaf(f0, l0, f1 * l1));
      f0 *= inv; f1 *= inv;
      const f32x4 o = __builtin_elementwise_fma(asf4(ga), (f32x4)f0, asf4(gb) * f1);
      *(LAS u32x2*)da = u32x2{pk2(o[0], o[1]), pk2(o[2], o[3])};
    }
    WG_BAR();
    if (w >= 4) {
      u32x4 af[12];
#pragma unroll
      for (int ks = 0; ks < 12; ++ks) af[ks] = *(LAS const u32x4*)(a_o + ks * 16);
      __builtin_amdgcn_sched_barrier(0);
      f32x4 d0 = {0.f, 0.f, 0.f, 0.f}, d1 = d0;
#pragma unroll
      for (int ks = 0; ks < 12; ks += 2) {
        d0 = __builtin_amdgcn_mfma_f32_16x16x32_bf16(Bf[ks], __builtin_bit_cast(bf16x8, af[ks]), d0, 0, 0, 0);
        d1 = __builtin_amdgcn_mfma_f32_16x16x32_bf16(Bf[ks + 1], __builtin_bit_cast(bf16x8, af[ks + 1]), d1, 0, 0, 0);
      }
      st_sc(wsr, a_p2, enc4(d0 + d1, tg));
    }
  }
  if (w < 5) {
    unsigned o = WS_P2 + (b2i * 16 + (lane >> 5) * 8) * D * 4 + (w * 32 + (lane & 31)) * 16, o4 = o + 4 * D * 4;
    const int pr = (w * 32 + (lane & 31)) * 2 + (lane >> 5);
    f32x2 xr = up2(lds[L_X + pr]);
    LAS unsigned* dh = lds + L_HH + pr;
    asm volatile("" : "+v"(xr), "+v"(dh), "+v"(o), "+v"(o4));
    f32x4 y;
    while ((int)(now() - tcom) < C2) { }
#pragma nounroll
    for (unsigned spin = 0; spin < SPIN_CAP; ++spin) {
      asm volatile("" ::: "memory");
      u32x4 g[8];
#pragma unroll
      for (int i = 0; i < 8; ++i) g[i] = ld_sc(wsr, i < 4 ? o : o4, (i & 3) * D * 4);
      unsigned bad = 0;
      y = f32x4{0.f, 0.f, 0.f, 0.f};
#pragma unroll
      for (int i = 0; i < 8; ++i) { const u32x4 u = untag(g[i], tg); bad |= any4(u); y += asf4(u); asm volatile("" : "+v"(y), "+v"(bad)); }
      if (__all((bad & 0xffu) == 0)) break;
      __builtin_amdgcn_s_sleep(1);
    }
    const f32x2 hs = xr + rbf2(f32x2{sw32(y[0], y[2]), sw32(y[1], y[3])});
    const unsigned hp2 = pk2(hs[0], hs[1]);
    *dh = hp2;
    const f32x2 hv = up2(hp2);
    const float piv = __uint_as_float(__builtin_amdgcn_readfirstlane(__float_as_uint(hv[0])));
    const f32x2 d = hv - piv, dd = d * d;
    const float sp = sw32(d[0] + d[1], dd[0] + dd[1]), sq = sum16(sw16(sp, sp));
    const float s1 = __uint_as_float(__builtin_amdgcn_readlane(__float_as_uint(sq), 0)), s2 = __uint_as_float(__builtin_amdgcn_readlane(__float_as_uint(sq), 32));
    *(LAS f32x2*)(ldf + L_ST + 2 * w) = f32x2{fmaf(s1, 1.f / 128, piv), fmaf(-s1, s1 * (1.f / 128), s2)};
  }
  WG_BAR();
  if (w == 0) {
    const f32x4 sa = *(LAS const f32x4*)(ldf + L_ST), sb = *(LAS const f32x4*)(ldf + L_ST + 4);
    const f32x2 sc = *(LAS const f32x2*)(ldf + L_ST + 8);
    const unsigned hw2 = *own;
    __builtin_amdgcn_sched_barrier(0);
    const f32x2 hv = up2(hw2);
    const float mean = (((sa[0] + sa[2]) + (sb[0] + sb[2])) + sc[0]) * 0.2f;
    const float e0 = sa[0] - mean, e1 = sa[2] - mean, e2 = sb[0] - mean, e3 = sb[2] - mean, e4 = sc[0] - mean;
    const float var = fmaf(fmaf(e0, e0, e1 * e1) + fmaf(e2, e2, e3 * e3) + e4 * e4, 0.2f, (((sa[1] + sa[3]) + (sb[1] + sb[3])) + sc[1]) * (1.f / D));
    const float inv = inv_sd(var), mb = rbf(mean);
    const float inv2 = inv_sd(var * inv * inv), mb2 = rbf((mean - mb) * inv);
    const f32x2 t0 = rbf2(rbf2(rbf2(hv - mb) * inv) - mb2) * inv2;
    const unsigned tb = pk2(t0[0], t0[1]), i0 = (tb & 0x7fffu) - TAB0, i1 = ((tb >> 16) & 0x7fffu) - TAB0;
    LAS const unsigned short* tab = (LAS const unsigned short*)(lds + L_TAB);
    f32x2 y = {bflo(tab[(i0 & 4095u) + ((tb >> 3) & 4096u)]), bflo(tab[(i1 & 4095u) + ((tb >> 19) & 4096u)])};
    if (!__all((i0 | i1) < 4096u)) {
      const float bias = bflo(lds[L_X + 320]);
#pragma nounroll
      for (int i = 0; i < 10; ++i) {
        const float t = __shfl(i & 1 ? bfhi(tb) : bflo(tb), i >> 1);
        float a = 0.f;
#pragma nounroll
        for (int k = 0; k < 12; ++k) a = mix2(t, up2(lds[L_MIX + lane + 64 * k]), up2(lds[L_MIX + 768 + lane + 64 * k]), lds[L_MIX + 1536 + lane + 64 * k], a);
        a = rbf(rbf(sum64(a)) + bias);
        if (lane == (i >> 1)) { if (i & 1) y[1] = a; else y[0] = a; }
      }
    }
    const f32x2 o = hv + y;
    __builtin_amdgcn_raw_buffer_store_b32(pk2(o[0], o[1]), mk(out, 4 * D * 2), a_out, 0, 16);
  }
  if (bid == 0 && tid == 64) __builtin_amdgcn_raw_buffer_store_b32(tagw, wsr, 0, 0, 16);
}

extern "C" void kernel_launch(void* const* d_in, const int* in_sizes, int n_in, void* d_out, int out_size, void* d_ws, size_t ws_size,
                              hipStream_t stream) {
  typedef const unsigned short* P;
  fused_decode<<<256, 512, 0, stream>>>((P)d_in[0], (P)d_in[1], (P)d_in[2], (P)d_in[3], (P)d_in[4], (P)d_in[6], (P)d_in[7], (P)d_in[8], (P)d_in[9],
                                        (unsigned short*)d_out, (unsigned char*)d_ws);
}
```

```cpp
#include <hip/hip_runtime.h>
#include <stdint.h>

typedef unsigned u32x4 __attribute__((ext_vector_type(4)));
typedef unsigned u32x2 __attribute__((ext_vector_type(2)));
typedef float f32x4 __attribute__((ext_vector_type(4)));
typedef float f32x2 __attribute__((ext_vector_type(2)));
typedef short s16x4 __attribute__((ext_vector_type(4)));
typedef __bf16 bf16x2 __attribute__((ext_vector_type(2)));
typedef __bf16 bf16x8 __attribute__((ext_vector_type(8)));
typedef __amdgpu_buffer_rsrc_t rsrc_t;
#define LAS __attribute__((address_space(3)))

constexpr int D = 640, H = 32, SK = 4096, DH = 192, HD = H * DH, F = 1536;
constexpr unsigned SEQB = SK * DH * 2, KVB = 4u * H * SEQB, WB = D * HD * 2;
constexpr float L2E = 1.4426950408889634f, SCALE = 0.07216878364870322f;

constexpr int NT = 64, SW = NT / 2 + 1;
constexpr int NPRE = 8;
constexpr int CK1 = 14, CK2 = 27, CK3 = 45;
constexpr int BST = 21, BOFF = 30, BSTEP = 35;
constexpr int CKG = 29;
constexpr float EPS2 = -0.002f, EPS3 = -0.02f;
constexpr unsigned WAITT = 400;
constexpr int STAG = 85;
constexpr unsigned QDELAY = 550, Q2DELAY = 1250;
constexpr int C1 = 182, C2 = 374;
static_assert(NPRE < CK1 && CK1 < BST && BST < SW && CK1 < CK2 && CK2 < SW && NPRE < CKG && CKG < SW && SW + 2 <= CK3 && CK3 < NT - 2 && (CK3 - SW) % 2 == 0,
              "every stamp that a helper wave waits for is reached");

constexpr unsigned P1REC = 896;
constexpr unsigned WS_TS = 4096, WS_TB = WS_TS + 3 * 64 * 128, WS_QP = WS_TB + 256 * 128, WS_P1 = WS_QP + H * 4 * 96 * 8, WS_P2 = WS_P1 + 512 * P1REC,
                   WS_END = WS_P2 + 4 * 16 * D * 4;
constexpr unsigned OOB = 0x7ffffff0u;
constexpr unsigned SPIN_CAP = 1u << 17;
constexpr unsigned TAB0 = 100u << 7;

constexpr int OSTR = 208;
constexpr int S_BAR = 0, S_T1 = 1, S_T2 = 2, S_BAR3 = 3, S_END = 4, S_T3 = 5, S_TAB = 6, S_Q = 7, S_Q2 = 8, S_TAG = 9, S_BST = 10;
constexpr int L_CNT = 0, L_Q = 12, L_REC = L_Q + 192, L_O = L_REC + 8 * 196, L_RED = L_O + 4 * OSTR, L_ST = L_RED + 96, L_X = L_ST + 16, L_HH = L_X + 324,
              L_XN = L_HH + 320, L_QR = L_XN + 16 * 132, L_MIX = L_QR + 4 * 256, L_WO = L_MIX + 3 * F / 2, L_TAB = L_WO + 2 * DH * 36, L_SC = L_TAB + 4096,
              L_END = L_SC + 2048;
static_assert(L_END * 4 > 80 * 1024 && L_REC % 4 == 0 && L_O % 4 == 0 && L_RED % 4 == 0 && L_ST % 4 == 0 && L_X % 4 == 0 && L_XN % 4 == 0 && L_QR % 4 == 0 &&
              L_MIX % 4 == 0 && L_WO % 4 == 0 && L_TAB % 2 == 0, "aligned regions, one workgroup per CU");

__device__ __forceinline__ float bflo(unsigned w) { return __uint_as_float(w << 16); }
__device__ __forceinline__ float bfhi(unsigned w) { return __uint_as_float(w & 0xffff0000u); }
__device__ __forceinline__ unsigned pk2(float lo, float hi) { unsigned r; asm("v_cvt_pk_bf16_f32 %0, %1, %2" : "=v"(r) : "v"(lo), "v"(hi)); return r; }
__device__ __forceinline__ float rbf(float f) { return (float)(__bf16)f; }
__device__ __forceinline__ f32x2 up2(unsigned w) { return f32x2{bflo(w), bfhi(w)}; }
__device__ __forceinline__ f32x2 rbf2(f32x2 v) { return up2(pk2(v[0], v[1])); }
__device__ __forceinline__ float dot2(unsigned a, unsigned b, float c) {
  return __builtin_amdgcn_fdot2_f32_bf16(__builtin_bit_cast(bf16x2, a), __builtin_bit_cast(bf16x2, b), c, false);
}
template <int CTRL> __device__ __forceinline__ float dpp(float x) {
  return __builtin_bit_cast(float, __builtin_amdgcn_mov_dpp(__builtin_bit_cast(int, x), CTRL, 0xf, 0xf, true));
}
constexpr int XOR1 = 0xB1, XOR2 = 0x4E, HMIR = 0x141, ROR8 = 0x128;
__device__ __forceinline__ float sw32(float a, float b) {
  const auto r = __builtin_amdgcn_permlane32_swap(__float_as_uint(a), __float_as_uint(b), false, false);
  return __uint_as_float(r[0]) + __uint_as_float(r[1]);
}
__device__ __forceinline__ float sw16(float a, float b) {
  const auto r = __builtin_amdgcn_permlane16_swap(__float_as_uint(a), __float_as_uint(b), false, false);
  return __uint_as_float(r[0]) + __uint_as_float(r[1]);
}
__device__ __forceinline__ float wmax(float m) {
  m = fmaxf(m, dpp<ROR8>(m));
  const auto q16 = __builtin_amdgcn_permlane16_swap(__float_as_uint(m), __float_as_uint(m), false, false);
  m = fmaxf(__uint_as_float(q16[0]), __uint_as_float(q16[1]));
  const auto q32 = __builtin_amdgcn_permlane32_swap(__float_as_uint(m), __float_as_uint(m), false, false);
  return fmaxf(__uint_as_float(q32[0]), __uint_as_float(q32[1]));
}
__device__ __forceinline__ float sum16(float v) { v += dpp<XOR1>(v); v += dpp<XOR2>(v); v += dpp<HMIR>(v); v += dpp<ROR8>(v); return v; }
__device__ __forceinline__ float sum64(float v) { v = sum16(v); v = sw16(v, v); return sw32(v, v); }
__device__ __forceinline__ float scatter8(const float* a, bool b3) {
  float y[4], z[2];
#pragma unroll
  for (int e = 0; e < 4; ++e) y[e] = sw32(a[e], a[e + 4]);
#pragma unroll
  for (int e = 0; e < 2; ++e) z[e] = sw16(y[e], y[e + 2]);
  return (b3 ? z[1] : z[0]) + dpp<ROR8>(b3 ? z[0] : z[1]);
}
__device__ __forceinline__ bf16x8 tr8(LAS const unsigned* p, int step) {
  const s16x4 a = __builtin_amdgcn_ds_read_tr16_b64_v4i16((LAS s16x4*)p), b = __builtin_amdgcn_ds_read_tr16_b64_v4i16((LAS s16x4*)(p + step));
  return __builtin_bit_cast(bf16x8, __builtin_shufflevector(a, b, 0, 1, 2, 3, 4, 5, 6, 7));
}
__device__ __forceinline__ rsrc_t mk(const void* p, unsigned n) { return __builtin_amdgcn_make_buffer_rsrc((void*)p, 0, n, 0x00020000); }
__device__ __forceinline__ u32x4 ld_nt(rsrc_t r, unsigned v, unsigned s) { return __builtin_amdgcn_raw_buffer_load_b128(r, v, s, 2); }
__device__ __forceinline__ u32x4 ld_sc(rsrc_t r, unsigned v, unsigned s = 0) { return __builtin_amdgcn_raw_buffer_load_b128(r, v, s, 16); }
__device__ __forceinline__ void st_sc(rsrc_t r, unsigned v, u32x4 x) { __builtin_amdgcn_raw_buffer_store_b128(x, r, v, 0, 16); }
__device__ __forceinline__ unsigned now() { return (unsigned)__builtin_amdgcn_s_memrealtime(); }
__device__ __forceinline__ unsigned lds_peek(LAS unsigned* p) {
  return __builtin_amdgcn_readfirstlane(__hip_atomic_load(p, __ATOMIC_RELAXED, __HIP_MEMORY_SCOPE_WORKGROUP));
}
#define PIN() do { asm volatile("" ::: "memory"); __builtin_amdgcn_sched_barrier(0); } while (0)
#define WG_BAR() do { asm volatile("s_waitcnt lgkmcnt(0)" ::: "memory"); __builtin_amdgcn_s_barrier(); PIN(); } while (0)
__device__ __forceinline__ void helper_bar(LAS unsigned* cnt, unsigned target, int lane) {
  asm volatile("s_waitcnt lgkmcnt(0)" ::: "memory");
  if (lane == 0) __hip_atomic_fetch_add(cnt, 1u, __ATOMIC_RELAXED, __HIP_MEMORY_SCOPE_WORKGROUP);
  while (lds_peek(cnt) < target) { }
  asm volatile("" ::: "memory");
}

struct Tag { unsigned b0, b1, b2, b3, t32; };
__device__ __forceinline__ Tag make_tag(unsigned t) { return Tag{t & 0xffu, (t + (t >> 8)) & 0xffu, (t + (t >> 16)) & 0xffu, (t + (t >> 24)) & 0xffu, t}; }
__device__ __forceinline__ u32x4 enc4(f32x4 v, const Tag& t) {
  return u32x4{(__float_as_uint(v[0]) & 0xffffff00u) | t.b0, (__float_as_uint(v[1]) & 0xffffff00u) | t.b1, (__float_as_uint(v[2]) & 0xffffff00u) | t.b2,
               (__float_as_uint(v[3]) & 0xffffff00u) | t.b3};
}
__device__ __forceinline__ u32x4 untag(u32x4 g, const Tag& t) { return g ^ u32x4{t.b0, t.b1, t.b2, t.b3}; }
__device__ __forceinline__ unsigned any4(u32x4 x) { return (x[0] | x[1]) | (x[2] | x[3]); }
__device__ __forceinline__ f32x4 asf4(u32x4 x) { return __builtin_bit_cast(f32x4, x); }

__device__ __forceinline__ float mix2(float t, f32x2 wa, f32x2 wb, unsigned wc, float acc) {
  constexpr float K0 = -2.f * L2E * 0.7978845608028654f, K1 = K0 * 0.044715f;
  const f32x2 u = rbf2(rbf2(wa * t) + wb);
  const f32x2 z = u * __builtin_elementwise_fma(u * u, (f32x2)K1, (f32x2)K0);
  const f32x2 d = f32x2{__builtin_amdgcn_exp2f(z[0]), __builtin_amdgcn_exp2f(z[1])} + 1.f;
  const f32x2 g = u * f32x2{__builtin_amdgcn_rcpf(d[0]), __builtin_amdgcn_rcpf(d[1])};
  return dot2(pk2(g[0], g[1]), wc, acc);
}
__device__ __forceinline__ float inv_sd(float var) { return __builtin_amdgcn_rcpf(rbf(__builtin_amdgcn_sqrtf(rbf(rbf(var) + 1e-5f)))); }

__global__ void __launch_bounds__(512, 2)
fused_decode(const unsigned short* x, const unsigned short* kc, const unsigned short* vc, const unsigned short* wq, const unsigned short* wo,
             const unsigned short* w1, const unsigned short* b1, const unsigned short* w2, const unsigned short* b2, unsigned short* out,
             unsigned char* ws) {
  __shared__ __attribute__((aligned(16))) unsigned lds_raw[L_END];
  LAS unsigned* lds = (LAS unsigned*)lds_raw;
  LAS float* ldf = (LAS float*)lds_raw;
  const int tid = threadIdx.x, lane = tid & 63, w = __builtin_amdgcn_readfirstlane(tid >> 6);
  const int bid = blockIdx.x;
  const int pa = (bid & 7) * 32 + (bid >> 3), pb = 512 + ((pa + 1) & 255), sqa = pa / 3, sqb = pb / 6;
  const int h = ((bid >> 1) & 3) * 8 + ((bid >> 3) & 7), j = (bid >> 6) * 2 + ((bid ^ h) & 1);
  const bool has_q = j >= 2 && (j & 1), has_o = !has_q;
  const int hp = h >> 1, co = (h & 1) * 5 + (j < 2 ? j : (j >> 1) + 1);
  const int b2i = bid >> 6, cg = bid & 63;
  const int sw = w & 3;
  const int r = lane >> 3, c = lane & 7;
  const bool hi = r >> 2;
  const unsigned kb = hi ? 86 : 0, c1 = hi ? 85 : 86, d2 = hi ? 85 : 86;
  const int ya = ((pa - 3 * sqa) * 512 + 1) / 3, nA = ((pa - 3 * sqa + 1) * 512 + 1) / 3 - ya, yb = ((pb - 6 * sqb) * 512 + 3) / 6, nB = 256 - nA;
  const int cA = (nA + 3 - sw) >> 2, cB = 64 - cA;
  const int ra = (pa * 170) >> 8, rb = (pa * 85) >> 8;
  auto kidx = [&](int u, int hf) -> int { const int k = 2 * (u & 31) + hf; return u < 32 ? k : (k < cB ? k + cA : k - cB); };
  const int oA = sw + ra, oB = ((sw - nA) & 3) + rb - 4 * cA;
  const unsigned bA = (unsigned)sqa * SEQB, bB = (unsigned)sqb * SEQB;
  auto adr = [&](int u, int hf, unsigned& av, unsigned& as) {
    const int k = kidx(u, hf);
    const bool pA = k < cA;
    const int i0 = 4 * k + (pA ? oA : oB), n = pA ? nA : nB, y = (pA ? ya : yb) + i0 - (i0 >= n ? n : 0);
    const unsigned mm = ((4 * y) & 255) + (r & 3);
    av = (mm + kb + (mm >= c1 ? 85 : 0) + (mm >= 171 ? d2 : 0)) * 384 + c * 16;
    as = (pA ? bA : bB) + (unsigned)(y >> 6) * (512 * 384);
    asm volatile("" : "+v"(av), "+s"(as)); };
  unsigned av0[2], as0[2];
  adr(0, 0, av0[0], as0[0]); adr(0, 1, av0[1], as0[1]);
  __builtin_amdgcn_sched_barrier(0);
  const rsrc_t wsr = mk(ws, WS_END);
  bf16x8 Bf[12];
  unsigned a_p1, a_p2, tagw;
  LAS const float* a_rec;
  LAS const unsigned* a_o;

  if (__builtin_expect(w < 4, 1)) {
    __builtin_amdgcn_s_setprio(2);
    auto issue = [&](u32x4 (&X)[3], int u, unsigned av, unsigned as) {
      const rsrc_t q = mk(u < 32 ? kc : vc, KVB);
#pragma unroll
      for (int i = 0; i < 3; ++i) X[i] = ld_nt(q, av + 128 * i, as);
      PIN(); };
    auto req = [&](u32x4 (&X)[3], int u, int hf) { unsigned av, as; adr(u, hf, av, as); issue(X, u, av, as); };
    u32x4 T[NPRE][2][3];
#pragma unroll
    for (int p = 0; p < NPRE; ++p) {
      if (p == 1) WG_BAR();
#pragma unroll
      for (int hf = 0; hf < 2; ++hf) {
        if (p) asm volatile("s_waitcnt vmcnt(3)" ::: "memory");
        if (p) req(T[p][hf], p, hf); else issue(T[0][hf], 0, av0[hf], as0[hf]);
      }
    }
    while (lds_peek(lds + L_CNT + S_Q) == 0) __builtin_amdgcn_s_sleep(1);
    PIN();
    u32x4 qf[3];
#pragma unroll
    for (int i = 0; i < 3; ++i) qf[i] = *(LAS const u32x4*)(lds + L_Q + (c + 8 * i) * 4);
    float mA = __uint_as_float(0xff7f0000u), mB = mA;
    LAS float* sl = ldf + L_SC + w * 512 + r;
    auto kstep = [&](u32x4 (&X)[2][3], int u, bool refill, int un) {
      float a[2];
      unsigned av[2], as[2];
      if (refill) { adr(un, 0, av[0], as[0]); adr(un, 1, av[1], as[1]); PIN(); }
#pragma unroll
      for (int hf = 0; hf < 2; ++hf) {
        if (u >= NPRE && 2 * u + hf == cA) {
          while (lds_peek(lds + L_CNT + S_Q2) == 0) __builtin_amdgcn_s_sleep(1);
#pragma unroll
          for (int i = 0; i < 3; ++i) qf[i] = *(LAS const u32x4*)(lds + L_Q + 96 + (c + 8 * i) * 4);
        }
        float a0 = 0.f, a1 = 0.f, a2 = 0.f;
#pragma unroll
        for (int k = 0; k < 4; ++k) { a0 = dot2(X[hf][0][k], qf[0][k], a0); a1 = dot2(X[hf][1][k], qf[1][k], a1); a2 = dot2(X[hf][2][k], qf[2][k], a2); }
        a[hf] = (a0 + a1) + a2;
        asm volatile("" : "+v"(a[hf]));
        PIN();
        if (refill) issue(X[hf], un, av[hf], as[hf]);
      }
#pragma unroll
      for (int hf = 0; hf < 2; ++hf) {
        float y = a[hf];
        y += dpp<XOR1>(y); y += dpp<XOR2>(y); y += dpp<HMIR>(y);
        y = rbf(rbf(y) * SCALE);
        const bool pA = 2 * u + hf < cA;
        const float mo = fmaxf(pA ? mA : mB, y);
        mA = pA ? mo : mA; mB = pA ? mB : mo;
        sl[u * 16 + hf * 8] = y;
      }
      PIN();
    };
#pragma unroll
    for (int p = 0; p < NPRE; ++p) kstep(T[p], p, p == 0, NPRE);
    int t = NPRE + 1;
    auto marks = [&](int tt) {
      if (w == 1 && tt == CKG && lane == 0) lds[L_CNT + S_TAB] = 1;
      if (w == 0 && (tt == CK1 || tt == CK2)) { const unsigned tn = now() | 1u; if (lane == 0) lds[L_CNT + (tt == CK1 ? S_T1 : S_T2)] = tn; }
    };
    do {
      if (t == BST) {
        int n = min((int)lds_peek(lds + L_CNT + S_BST), (SW - 1 - BST) / 2);
        if (n > 0) {
          marks(t);
          req(T[1][0], t, 0); req(T[1][1], t, 1);
          while (--n) {
            marks(t + 1);
            kstep(T[0], t - 1, true, t + 1);
            marks(t + 2);
            kstep(T[1], t, true, t + 2);
            t += 2;
          }
          marks(t + 1);
          kstep(T[0], t - 1, true, t + 1);
          kstep(T[1], t, false, 0);
          t += 2;
        }
      }
      marks(t);
      kstep(T[0], t - 1, true, t);
    } while (++t < SW);
    mA = wmax(mA); mB = wmax(mB);
    float l = 0.f;
    f32x2 acc[12];
#pragma unroll
    for (int k = 0; k < 12; ++k) acc[k] = f32x2{0.f, 0.f};
    bool stamp3 = false;
    LAS float* drec = ldf + L_REC + w * 196 + c * 8 + r;
    auto flush = [&](int piece, float m) {
      l += dpp<ROR8>(l); l = sw16(l, l); l = sw32(l, l);
      *(LAS f32x2*)(ldf + L_REC + (piece * 4 + w) * 196 + 192) = f32x2{m, l};
#pragma unroll
      for (int i = 0; i < 3; ++i) {
        const float e[8] = {acc[4 * i][0], acc[4 * i][1], acc[4 * i + 1][0], acc[4 * i + 1][1], acc[4 * i + 2][0], acc[4 * i + 2][1], acc[4 * i + 3][0], acc[4 * i + 3][1]};
        drec[piece * 784 + i * 64] = scatter8(e, lane & 8);
      }
    };
    auto vstep = [&](u32x4 (&X)[2][3], int u, bool refill, unsigned pace, int fl) {
      float p[2];
      unsigned av[2], as[2];
      if (refill) { adr(u + 2, 0, av[0], as[0]); adr(u + 2, 1, av[1], as[1]); }
#pragma unroll
      for (int hf = 0; hf < 2; ++hf) { const int k = kidx(u, hf); p[hf] = __builtin_amdgcn_exp2f((sl[k * 8] - (k < cA ? mA : mB)) * L2E); }
      asm volatile("" : "+v"(p[0]), "+v"(p[1]));
      PIN();
#pragma unroll
      for (int hf = 0; hf < 2; ++hf) {
        f32x2 vf[12];
#pragma unroll
        for (int k = 0; k < 12; ++k) vf[k] = up2(X[hf][k >> 2][k & 3]);
        if (refill) {
          asm volatile("" : "+v"(vf[0]), "+v"(vf[1]), "+v"(vf[2]), "+v"(vf[3]), "+v"(vf[4]), "+v"(vf[5]));
          asm volatile("" : "+v"(vf[6]), "+v"(vf[7]), "+v"(vf[8]), "+v"(vf[9]), "+v"(vf[10]), "+v"(vf[11]));
          PIN();
          if (hf == 0) {
            asm volatile("s_waitcnt vmcnt(5)" ::: "memory");
            unsigned tn;
            while ((int)(((tn = now()) << 6) - pace) < 0) { }
            if (stamp3) { if (lane == 0) lds[L_CNT + S_T3] = tn | 1u; stamp3 = false; }
          }
          issue(X[hf], u + 2, av[hf], as[hf]);
        }
        if (hf == fl && 2 * (u - 32) + hf == cB) {
          flush(1, mB);
          l = 0.f;
#pragma unroll
          for (int k = 0; k < 12; ++k) acc[k] = f32x2{0.f, 0.f};
        }
        l += p[hf];
#pragma unroll
        for (int k = 0; k < 12; ++k) acc[k] = __builtin_elementwise_fma(vf[k], (f32x2)p[hf], acc[k]);
#pragma unroll
        for (int k = 0; k < 12; k += 4) asm volatile("" : "+v"(acc[k]), "+v"(acc[k + 1]), "+v"(acc[k + 2]), "+v"(acc[k + 3]));
        PIN();
      }
    };
    unsigned tend;
    while ((tend = lds_peek(lds + L_CNT + S_END)) == 0) __builtin_amdgcn_s_sleep(1);
    tagw = lds_peek(lds + L_CNT + S_TAG);
    const int stag = (STAG * ((((bid * 37) & 63) + 64 * w) - 128) * 64) >> 8;
    unsigned tgt = now() << 6;
    int tau = (int)((tend << 6) + stag - tgt) / (NT - 1 - SW);
    req(T[1][0], t, 0); req(T[1][1], t, 1);
    do {
      const unsigned t3 = lds_peek(lds + L_CNT + S_END);
      if (t3 != tend) { tend = t3; tau = (int)((tend << 6) + stag - tgt) / (NT - 1 - t); }
      tgt += tau;
      vstep(T[0], t - 1, true, tgt, 1);
      tgt += tau;
      if (w == 0 && t + 2 == CK3) stamp3 = true;
      vstep(T[1], t, true, tgt, 0);
      t += 2;
    } while (t < NT - 1);
    asm volatile("" : "+v"(drec));
    vstep(T[0], NT - 2, false, 0, 2);
    vstep(T[1], NT - 1, false, 0, 2);
    flush(0, mA);
  } else {
    const int hid = tid - 256, hw = w - 4;
    const unsigned tstart = now();
    unsigned ep = __builtin_amdgcn_raw_buffer_load_b32(wsr, 0, 0, 16);
    if (hid < 12) lds[L_CNT + hid] = 0;
    const int cq = (j - 2) >> 1, rg = hid >> 3, c8 = hid & 7;
    u32x4 W[20], X[5];
    const rsrc_t xr = mk(x, has_q ? 4 * D * 2 : 0), wqr = mk(wq, has_q ? WB : 0), wbr = has_o ? mk(wo, WB) : wqr;
#pragma unroll
    for (int i = 0; i < 5; ++i) { const int ch = 20 * hw + (lane & 15) + 16 * i; X[i] = ld_nt(xr, (lane >> 4) * (D * 2) + (ch >= 80 ? ch - 80 : ch) * 16, 0); }
    PIN();
    const unsigned wv = has_o ? (unsigned)rg * (D * 2) + c8 * 16 : (unsigned)(lane >> 3) * (HD * 2) + c8 * 16;
    const unsigned ws0 = has_o ? (unsigned)(hp * 2 * DH) * (D * 2) + co * 128 : (unsigned)(160 * hw) * (HD * 2) + (h * DH + cq * 64) * 2;
    const unsigned wst = has_o ? 32 * D * 2 : 8 * HD * 2;
#pragma unroll
    for (int i = 0; i < 20; ++i) {
      W[i] = ld_nt(i < 12 ? wbr : wqr, wv, ws0 + i * wst);
      if ((i & 3) == 3) PIN();
    }
    const unsigned mo = hid < 192 ? hid * 16 : OOB;
    const u32x4 Ma = ld_nt(mk(w1, F * 2), mo, 0), Mb = ld_nt(mk(b1, F * 2), mo, 0), Mc = ld_nt(mk(w2, F * 2), mo, 0);
    const unsigned b2v = __builtin_amdgcn_raw_buffer_load_b16(mk(b2, 2), 0, 0, 2);
    PIN();
    WG_BAR();
    asm volatile("" : "+v"(ep));
    tagw = __builtin_amdgcn_readfirstlane(ep) + 1;
    const Tag tg = make_tag(tagw);
    if (hid == 0) lds[L_CNT + S_TAG] = tagw;
    if (has_o) {
#pragma unroll
      for (int i = 0; i < 12; ++i) *(LAS u32x4*)(lds + L_WO + (rg + 32 * i) * 36 + c8 * 4) = W[i];
    } else {
      float sa = 0.f, sb = 0.f;
#pragma unroll
      for (int i = 0; i < 20; i += 2) { sa = dot2(X[i >> 2][i & 3], 0x3f803f80u, sa); sb = dot2(X[i >> 2][(i & 3) + 1], 0x3f803f80u, sb); }
      const float mean = sum16(sa + sb) * (1.f / D);
      f32x2 s2 = {0.f, 0.f}, s3 = s2;
#pragma unroll
      for (int i = 0; i < 20; i += 2) {
        const f32x2 d = up2(X[i >> 2][i & 3]) - mean, e = up2(X[i >> 2][(i & 3) + 1]) - mean;
        s2 = __builtin_elementwise_fma(d, d, s2); s3 = __builtin_elementwise_fma(e, e, s3);
      }
      s2 += s3;
      const float inv = inv_sd(sum16(s2[0] + s2[1]) * (1.f / D)), mb = rbf(mean);
      LAS unsigned* xn = lds + L_XN + hw * 4 * 132;
#pragma unroll
      for (int i = 0; i < 2; ++i) {
        u32x4 o;
#pragma unroll
        for (int k = 0; k < 4; ++k) { const f32x2 y = rbf2(up2(X[i][k]) - mb) * inv; o[k] = pk2(y[0], y[1]); }
        *(LAS u32x4*)(xn + (lane >> 4) * 132 + ((lane & 15) + 16 * i) * 4) = o;
      }
      asm volatile("" ::: "memory");
      u32x4 af[5];
#pragma unroll
      for (int ks = 0; ks < 5; ++ks) af[ks] = *(LAS const u32x4*)(xn + (lane & 3) * 132 + ks * 16 + (lane >> 4) * 4);
      LAS unsigned* st = lds + L_WO + hw * 64 * 36;
      f32x4 dq[4];
#pragma unroll
      for (int nt = 0; nt < 4; ++nt) dq[nt] = f32x4{0.f, 0.f, 0.f, 0.f};
#pragma unroll
      for (int ks = 0; ks < 5; ++ks) {
        LAS unsigned* sk = st + (ks & 1) * 32 * 36;
#pragma unroll
        for (int i = 0; i < 4; ++i) *(LAS u32x4*)(sk + (8 * i + (lane >> 3)) * 36 + c8 * 4) = W[4 * ks + i];
        asm volatile("" ::: "memory");
        LAS const unsigned* rd = sk + (8 * (lane >> 4) + ((lane >> 2) & 3)) * 36 + (lane & 3) * 2;
#pragma unroll
        for (int nt = 0; nt < 4; ++nt) dq[nt] = __builtin_amdgcn_mfma_f32_16x16x32_bf16(__builtin_bit_cast(bf16x8, af[ks]), tr8(rd + nt * 8, 4 * 36), dq[nt], 0, 0, 0);
        asm volatile("" ::: "memory");
      }
      const bool g1 = lane & 16, g2 = lane & 32;
#pragma unroll
      for (int nt = 0; nt < 4; ++nt) {
        const float a01 = g1 ? dq[nt][1] : dq[nt][0], a23 = g1 ? dq[nt][3] : dq[nt][2];
        ldf[L_QR + hw * 256 + (lane >> 4) * 64 + nt * 16 + (lane & 15)] = g2 ? a23 : a01;
      }
      helper_bar(lds + L_CNT + S_BAR, 4, lane);
      if (hw == 0) {
        f32x4 a = *(LAS const f32x4*)(ldf + L_QR + 4 * lane);
#pragma unroll
        for (int ww = 1; ww < 4; ++ww) a += *(LAS const f32x4*)(ldf + L_QR + ww * 256 + 4 * lane);
        st_sc(wsr, WS_QP + ((h * 4 + (lane >> 4)) * 96 + cq * 32 + (lane & 15) * 2) * 8, u32x4{pk2(a[0], a[1]), tg.t32, pk2(a[2], a[3]), tg.t32});
      }
    }
    auto get_q = [&](int pc, int sq, unsigned delay) {
      while (now() - tstart < delay) __builtin_amdgcn_s_sleep(2);
      const unsigned o = lane < 48 ? WS_QP + ((sq & 31) * 4 + (sq >> 5)) * 96 * 8 + lane * 16 : OOB, vm = lane < 48 ? ~0u : 0u;
      u32x4 g;
#pragma nounroll
      for (unsigned spin = 0; spin < SPIN_CAP; ++spin) {
        asm volatile("" ::: "memory");
        g = ld_sc(wsr, o);
        if (__all((((g[1] ^ tg.t32) | (g[3] ^ tg.t32)) & vm) == 0)) break;
        __builtin_amdgcn_s_sleep(8);
      }
      if (lane < 48) *(LAS u32x2*)(lds + L_Q + pc * 96 + 2 * lane) = u32x2{g[0], g[2]};
      asm volatile("s_waitcnt lgkmcnt(0)" ::: "memory");
      if (lane == 0) lds[L_CNT + S_Q + pc] = 1;
    };
    if (hw == 3) {
      get_q(0, sqa, QDELAY);
    } else {
      *(LAS u32x4*)(lds + L_MIX + hid * 4) = Ma; *(LAS u32x4*)(lds + L_MIX + 768 + hid * 4) = Mb; *(LAS u32x4*)(lds + L_MIX + 1536 + hid * 4) = Mc;
      const unsigned bits0 = (TAB0 + ((bid * 32) & 4095)) | (bid >> 7) << 15;
#pragma nounroll
      for (int i0 = 0; i0 < 32; i0 += 8) {
        float ac[8];
#pragma unroll
        for (int i = 0; i < 8; ++i) {
          const float t = __uint_as_float((bits0 + i0 + i) << 16);
          ac[i] = 0.f;
#pragma unroll
          for (int k = 0; k < 4; ++k) ac[i] = mix2(t, up2(Ma[k]), up2(Mb[k]), Mc[k], ac[i]);
        }
        float y = scatter8(ac, lane & 8);
        y += dpp<XOR1>(y); y += dpp<XOR2>(y); y += dpp<HMIR>(y);
        if ((lane & 7) == 0) ldf[L_RED + hw * 32 + i0 + (lane >> 3)] = y;
      }
      helper_bar(lds + L_CNT + S_BAR3, 3, lane);
      if (hw == 2 && lane < 8) {
        f32x4 y = *(LAS const f32x4*)(ldf + L_RED + 4 * lane);
#pragma unroll
        for (int ww = 1; ww < 3; ++ww) y += *(LAS const f32x4*)(ldf + L_RED + ww * 32 + 4 * lane);
        const f32x2 y0 = rbf2(f32x2{y[0], y[1]}) + bflo(b2v), y1 = rbf2(f32x2{y[2], y[3]}) + bflo(b2v);
        st_sc(wsr, WS_TB + bid * 128 + lane * 16, u32x4{pk2(y0[0], y0[1]), tg.t32, pk2(y1[0], y1[1]), tg.t32});
      }
    }
    if (hid < 80) *(LAS u32x4*)(lds + L_X + hid * 4) = __builtin_nontemporal_load((const u32x4*)(x + b2i * D + hid * 8));
    if (hid == 80) lds[L_X + 320] = b2v;
    helper_bar(lds + L_CNT + S_BAR, has_q ? 8 : 4, lane);
    LAS const unsigned* wl = lds + L_WO + (8 * (lane >> 4) + ((lane >> 2) & 3)) * 36 + hw * 8 + (lane & 3) * 2;
#pragma unroll
    for (int ks = 0; ks < 12; ++ks) Bf[ks] = tr8(wl + ks * 32 * 36, 4 * 36);
    a_p1 = lane <= 48 ? WS_P1 + (hw ? pb - 256 : pa) * P1REC + lane * 16 : OOB;
    a_p2 = (lane & 12) ? OOB : WS_P2 + (((lane & 3) * 16 + hp) * D + co * 64 + hw * 16 + (lane >> 4) * 4) * 4;
    a_rec = ldf + L_REC + (hw & 1) * 784 + 4 * (lane < 48 ? lane : 48);
    a_o = lds + L_O + (lane & 3) * OSTR + (lane >> 4) * 4;
    asm volatile("" : "+v"(a_p1), "+v"(a_p2), "+v"(a_rec), "+v"(a_o));
    if (hw == 3) get_q(1, sqb, Q2DELAY);
    if (hw < 2) {
      while (lds_peek(lds + L_CNT + S_TAB) == 0) __builtin_amdgcn_s_sleep(16);
#pragma nounroll
      for (int half = 0; half < 2; ++half) {
        u32x4 g[8];
#pragma nounroll
        for (unsigned spin = 0; spin < SPIN_CAP; ++spin) {
          asm volatile("" ::: "memory");
          unsigned bad = 0;
#pragma unroll
          for (int i = 0; i < 8; ++i) g[i] = ld_sc(wsr, WS_TB + hid * 16, (half * 8 + i) * 2048);
#pragma unroll
          for (int i = 0; i < 8; ++i) bad |= (g[i][1] ^ tg.t32) | (g[i][3] ^ tg.t32);
          if (__all(bad == 0)) break;
          __builtin_amdgcn_s_sleep(32);
        }
#pragma unroll
        for (int i = 0; i < 8; ++i) *(LAS u32x2*)(lds + L_TAB + ((half * 8 + i) * 128 + hid) * 2) = u32x2{g[i][0], g[i][2]};
      }
    }
    if (hw == 2) {
      unsigned tm0 = 0;
#pragma nounroll
      for (int ck = 0; ck < 3; ++ck) {
        unsigned t2;
        while ((t2 = lds_peek(lds + L_CNT + (ck == 0 ? S_T1 : ck == 1 ? S_T2 : S_T3))) == 0) __builtin_amdgcn_s_sleep(8);
        if (((bid >> 4) & 3) == (bid >> 6) && lane == 0)
          __builtin_amdgcn_raw_buffer_store_b64(u32x2{t2, tg.t32}, wsr, WS_TS + (ck * 64 + (bid & 15) * 4 + (bid >> 6)) * 128, 0, 16);
        while (now() - t2 < WAITT) __builtin_amdgcn_s_sleep(8);
        const u32x2 g = __builtin_amdgcn_raw_buffer_load_b64(wsr, WS_TS + (ck * 64 + lane) * 128, 0, 16);
        const unsigned tm = t2 + (int)(sum64(g[1] == tg.t32 ? (float)min(max((int)(g[0] - t2), -2000), 2000) : (float)(WAITT + 30)) * (1.f / 64));
        const float left = ck == 1 ? (NT - 1.f - CK2) / (CK2 - CK1) * (1.f - EPS2) : (NT - 1.f - CK3) / (CK3 - CK2) * (1.f - EPS3);
        if (ck == 0 && lane == 0) lds[L_CNT + S_BST] = max((int)(t2 - tm) - BOFF, 0) / BSTEP;
        if (ck && lane == 0) lds[L_CNT + S_END] = (tm + (int)((float)(int)(tm - tm0) * left)) | 1u;
        tm0 = tm;
      }
    }
  }
  const Tag tg = make_tag(tagw);
  WG_BAR();
  if (w == 4 || w == 5) {
    float mm[4];
    f32x4 rv[4];
#pragma unroll
    for (int i = 0; i < 4; ++i) { mm[i] = ldf[L_REC + ((w & 1) * 4 + i) * 196 + 192]; rv[i] = *(LAS const f32x4*)(a_rec + i * 196); }
    __builtin_amdgcn_sched_barrier(0);
    const float M = fmaxf(fmaxf(mm[0], mm[1]), fmaxf(mm[2], mm[3]));
    f32x4 a = {0.f, 0.f, 0.f, 0.f};
#pragma unroll
    for (int i = 0; i < 4; ++i) a += __builtin_amdgcn_exp2f((mm[i] - M) * L2E) * rv[i];
    if (lane >= 48) a = f32x4{M, a[1], 0.f, 0.f};
    st_sc(wsr, a_p1, enc4(a, tg));
  }
  LAS const unsigned* own = lds + L_HH + cg * 5 + (lane < 5 ? lane : 0);
  unsigned a_out = lane < 5 ? (b2i * D + cg * 10 + 2 * lane) * 2 : OOB;
  asm volatile("" : "+v"(own), "+v"(a_out));
  const unsigned tcom = lds_peek(lds + L_CNT + S_END);
  if (has_o) {
    {
      const int sq = (w & 3) * H + 2 * hp + (w >> 2), s0 = sq <= 85 ? 3 * sq : 6 * sq - 256, nr = sq < 85 ? 3 : (sq == 85 ? 5 : 6);
      unsigned oa = lane <= 48 ? WS_P1 + s0 * P1REC + lane * 16 : OOB;
      unsigned vm = lane <= 48 ? 0xffu : 0u;
      LAS unsigned* da = lane < 48 ? lds + L_O + (w & 3) * OSTR + (w >> 2) * 96 + 2 * lane : lds + L_QR + 2 * lane;
      asm volatile("" : "+v"(da), "+v"(oa), "+v"(vm));
      const rsrc_t w3 = mk(ws, nr > 3 ? WS_END : 0u), w5 = mk(ws, nr > 5 ? WS_END : 0u);
      const unsigned v3 = nr > 3 ? ~0u : 0u, v5 = nr > 5 ? ~0u : 0u;
      u32x4 g[6];
      while ((int)(now() - tcom) < C1) { }
#pragma nounroll
      for (unsigned spin = 0; spin < SPIN_CAP; ++spin) {
        asm volatile("" ::: "memory");
#pragma unroll
        for (int i = 0; i < 6; ++i) g[i] = ld_sc(i < 3 ? wsr : (i < 5 ? w3 : w5), oa, i * P1REC);
#pragma unroll
        for (int i = 0; i < 6; ++i) g[i] = untag(g[i], tg) & (i < 3 ? ~0u : (i < 5 ? v3 : v5));
        if (__all(((any4(g[0]) | any4(g[1]) | any4(g[2]) | any4(g[3]) | any4(g[4]) | any4(g[5])) & vm) == 0)) break;
        __builtin_amdgcn_s_sleep(1);
      }
      const float mz = __uint_as_float(g[0][0]);
      const float M = fmaxf(fmaxf(fmaxf(mz, __uint_as_float(g[1][0])), __uint_as_float(g[2][0])),
                            fmaxf(fmaxf(nr > 3 ? __uint_as_float(g[3][0]) : mz, nr > 3 ? __uint_as_float(g[4][0]) : mz), nr > 5 ? __uint_as_float(g[5][0]) : mz));
      float f[6], L = 0.f;
#pragma unroll
      for (int i = 0; i < 6; ++i) { f[i] = __builtin_amdgcn_exp2f(fminf(__uint_as_float(g[i][0]) - M, 0.f) * L2E); L = fmaf(f[i], __uint_as_float(g[i][1]), L); }
      const float inv = __builtin_amdgcn_rcpf(L);
      f32x4 o = {0.f, 0.f, 0.f, 0.f};
#pragma unroll
      for (int i = 0; i < 6; ++i) o = __builtin_elementwise_fma(asf4(g[i]), (f32x4)__uint_as_float(__builtin_amdgcn_readlane(__float_as_uint(f[i] * inv), 48)), o);
      *(LAS u32x2*)da = u32x2{pk2(o[0], o[1]), pk2(o[2], o[3])};
    }
    WG_BAR();
    if (w >= 4) {
      u32x4 af[12];
#pragma unroll
      for (int ks = 0; ks < 12; ++ks) af[ks] = *(LAS const u32x4*)(a_o + ks * 16);
      __builtin_amdgcn_sched_barrier(0);
      f32x4 d0 = {0.f, 0.f, 0.f, 0.f}, d1 = d0;
#pragma unroll
      for (int ks = 0; ks < 12; ks += 2) {
        d0 = __builtin_amdgcn_mfma_f32_16x16x32_bf16(Bf[ks], __builtin_bit_cast(bf16x8, af[ks]), d0, 0, 0, 0);
        d1 = __builtin_amdgcn_mfma_f32_16x16x32_bf16(Bf[ks + 1], __builtin_bit_cast(bf16x8, af[ks + 1]), d1, 0, 0, 0);
      }
      st_sc(wsr, a_p2, enc4(d0 + d1, tg));
    }
  }
  if (w < 5) {
    unsigned o = WS_P2 + (b2i * 16 + (lane >> 5) * 8) * D * 4 + (w * 32 + (lane & 31)) * 16, o4 = o + 4 * D * 4;
    const int pr = (w * 32 + (lane & 31)) * 2 + (lane >> 5);
    f32x2 xr = up2(lds[L_X + pr]);
    LAS unsigned* dh = lds + L_HH + pr;
    asm volatile("" : "+v"(xr), "+v"(dh), "+v"(o), "+v"(o4));
    f32x4 y;
    while ((int)(now() - tcom) < C2) { }
#pragma nounroll
    for (unsigned spin = 0; spin < SPIN_CAP; ++spin) {
      asm volatile("" ::: "memory");
      u32x4 g[8];
#pragma unroll
      for (int i = 0; i < 8; ++i) g[i] = ld_sc(wsr, i < 4 ? o : o4, (i & 3) * D * 4);
      unsigned bad = 0;
      y = f32x4{0.f, 0.f, 0.f, 0.f};
#pragma unroll
      for (int i = 0; i < 8; ++i) { const u32x4 u = untag(g[i], tg); bad |= any4(u); y += asf4(u); asm volatile("" : "+v"(y), "+v"(bad)); }
      if (__all((bad & 0xffu) == 0)) break;
      __builtin_amdgcn_s_sleep(1);
    }
    const f32x2 hs = xr + rbf2(f32x2{sw32(y[0], y[2]), sw32(y[1], y[3])});
    const unsigned hp2 = pk2(hs[0], hs[1]);
    *dh = hp2;
    const f32x2 hv = up2(hp2);
    const float piv = __uint_as_float(__builtin_amdgcn_readfirstlane(__float_as_uint(hv[0])));
    const f32x2 d = hv - piv, dd = d * d;
    const float sp = sw32(d[0] + d[1], dd[0] + dd[1]), sq = sum16(sw16(sp, sp));
    const float s1 = __uint_as_float(__builtin_amdgcn_readlane(__float_as_uint(sq), 0)), s2 = __uint_as_float(__builtin_amdgcn_readlane(__float_as_uint(sq), 32));
    *(LAS f32x2*)(ldf + L_ST + 2 * w) = f32x2{fmaf(s1, 1.f / 128, piv), fmaf(-s1, s1 * (1.f / 128), s2)};
  }
  WG_BAR();
  if (w == 0) {
    const f32x4 sa = *(LAS const f32x4*)(ldf + L_ST), sb = *(LAS const f32x4*)(ldf + L_ST + 4);
    const f32x2 sc = *(LAS const f32x2*)(ldf + L_ST + 8);
    const unsigned hw2 = *own;
    __builtin_amdgcn_sched_barrier(0);
    const f32x2 hv = up2(hw2);
    const float mean = (((sa[0] + sa[2]) + (sb[0] + sb[2])) + sc[0]) * 0.2f;
    const float e0 = sa[0] - mean, e1 = sa[2] - mean, e2 = sb[0] - mean, e3 = sb[2] - mean, e4 = sc[0] - mean;
    const float var = fmaf(fmaf(e0, e0, e1 * e1) + fmaf(e2, e2, e3 * e3) + e4 * e4, 0.2f, (((sa[1] + sa[3]) + (sb[1] + sb[3])) + sc[1]) * (1.f / D));
    const float inv = inv_sd(var), mb = rbf(mean);
    const float inv2 = inv_sd(var * inv * inv), mb2 = rbf((mean - mb) * inv);
    const f32x2 t0 = rbf2(rbf2(rbf2(hv - mb) * inv) - mb2) * inv2;
    const unsigned tb = pk2(t0[0], t0[1]), i0 = (tb & 0x7fffu) - TAB0, i1 = ((tb >> 16) & 0x7fffu) - TAB0;
    LAS const unsigned short* tab = (LAS const unsigned short*)(lds + L_TAB);
    f32x2 y = {bflo(tab[(i0 & 4095u) + ((tb >> 3) & 4096u)]), bflo(tab[(i1 & 4095u) + ((tb >> 19) & 4096u)])};
    if (!__all((i0 | i1) < 4096u)) {
      const float bias = bflo(lds[L_X + 320]);
#pragma nounroll
      for (int i = 0; i < 10; ++i) {
        const float t = __shfl(i & 1 ? bfhi(tb) : bflo(tb), i >> 1);
        float a = 0.f;
#pragma nounroll
        for (int k = 0; k < 12; ++k) a = mix2(t, up2(lds[L_MIX + lane + 64 * k]), up2(lds[L_MIX + 768 + lane + 64 * k]), lds[L_MIX + 1536 + lane + 64 * k], a);
        a = rbf(rbf(sum64(a)) + bias);
        if (lane == (i >> 1)) { if (i & 1) y[1] = a; else y[0] = a; }
      }
    }
    const f32x2 o = hv + y;
    __builtin_amdgcn_raw_buffer_store_b32(pk2(o[0], o[1]), mk(out, 4 * D * 2), a_out, 0, 16);
  }
  if (bid == 0 && tid == 64) __builtin_amdgcn_raw_buffer_store_b32(tagw, wsr, 0, 0, 16);
}

extern "C" void kernel_launch(void* const* d_in, const int* in_sizes, int n_in, void* d_out, int out_size, void* d_ws, size_t ws_size,
                              hipStream_t stream) {
  typedef const unsigned short* P;
  fused_decode<<<256, 512, 0, stream>>>((P)d_in[0], (P)d_in[1], (P)d_in[2], (P)d_in[3], (P)d_in[4], (P)d_in[6], (P)d_in[7], (P)d_in[8], (P)d_in[9],
                                        (unsigned short*)d_out, (unsigned char*)d_ws);
}
```
